```python
import torch

def _softmax(x, axis=-1):
    return torch.softmax(x, dim=axis)
def _gelu(x):
    return torch.nn.functional.gelu(x, approximate="tanh")
def _silu(x):
    return torch.nn.functional.silu(x)
def _sigmoid(x):
    return torch.sigmoid(x)
def _layernorm(x, axis=-1, eps=1e-5):
    m = x.mean(dim=axis, keepdim=True)
    v = x.var(dim=axis, keepdim=True, unbiased=False)
    return (x - m) / torch.sqrt(v + eps)
def _scatter_add(tgt, idx, src, axis=0):
    return tgt.clone().index_add_(axis, idx, src)
def _segment_sum(src, seg, n):
    out = torch.zeros((n,) + src.shape[1:], dtype=src.dtype)
    return out.index_add_(0, seg, src)
def _pad(x, pw):
    flat = tuple(v for pair in reversed(pw) for v in pair)
    return torch.nn.functional.pad(x, flat)

kernel_name = 'recurrent_scan__sliding_local_dilated__swiglu_ffn__prompt'

def setup_inputs():

    _w = (lambda a: (lambda t: t.to(torch.bfloat16) if t.is_floating_point() else t)(torch.as_tensor(a)))
    def _A_setup():
        B, S, D = 1, 2048, 1024
        return {
            "x": _w((lambda *s: torch.randn(*s, dtype=torch.bfloat16) * 0.05)(B, S, D)),
            "wqkv": _w((lambda *s: torch.randn(*s, dtype=torch.bfloat16) * 0.05)(D, 3 * D)),
            "wo": _w((lambda *s: torch.randn(*s, dtype=torch.bfloat16) * 0.05)(D, D)),
        }
    _A_in = _A_setup()

    def _B_setup():
        B, S, D, F = 1, 2048, 1024, 3584
        return {
            "x": _w((lambda *s: torch.randn(*s, dtype=torch.bfloat16) * 0.05)(B, S, D)),
            "w_gate": _w((lambda *s: torch.randn(*s, dtype=torch.bfloat16) * 0.05)(D, F)),
            "w_up": _w((lambda *s: torch.randn(*s, dtype=torch.bfloat16) * 0.05)(D, F)),
            "w_down": _w((lambda *s: torch.randn(*s, dtype=torch.bfloat16) * 0.05)(F, D)),
        }
    _B_in = _B_setup()

    return {
        "A_x": _A_in["x"],
        "A_wqkv": _A_in["wqkv"],
        "A_wo": _A_in["wo"],
        "B_x": _B_in["x"],
        "B_w_gate": _B_in["w_gate"],
        "B_w_up": _B_in["w_up"],
        "B_w_down": _B_in["w_down"],
    }

def reference(A_x, A_wqkv, A_wo, B_x, B_w_gate, B_w_up, B_w_down):
    def _A_ref(x, wqkv, wo):
        B, S, D = x.shape
        H = 32; Dh = D // H
        W, dil = 8, 2
        qkv = (lambda x, *d: x.permute(*d))((x @ wqkv).reshape(B, S, 3, H, Dh), 2, 0, 3, 1, 4)
        q, k, v = qkv[0], qkv[1], qkv[2]
        scale = Dh ** -0.5
        pos = torch.arange(S)
        off = torch.arange(W) * dil
        idx = pos[:, None] - off[None, :]
        mask = idx >= 0
        idx_c = torch.where(mask, idx, 0).reshape(-1)
        kg = k[:, :, idx_c, :].reshape(B, H, S, W, Dh)
        vg = v[:, :, idx_c, :].reshape(B, H, S, W, Dh)
        logits = torch.einsum("bhsd,bhswd->bhsw", q, kg) * scale
        logits = torch.where(mask[None, None, :, :], logits, -1e9)
        attn = _softmax(logits, axis=-1)
        o = torch.einsum("bhsw,bhswd->bhsd", attn, vg)
        return (lambda x, *d: x.permute(*d))(o, 0, 2, 1, 3).reshape(B, S, D) @ wo

    def _B_ref(x, w_gate, w_up, w_down):
        g = _silu(x @ w_gate)
        u = x @ w_up
        return (g * u) @ w_down

    x = A_x
    xn = _layernorm(x, axis=-1)
    s = _A_ref(xn, A_wqkv, A_wo)
    g = _silu(_B_ref(xn, B_w_gate, B_w_up, B_w_down))
    return x + s * g
```

```cpp
#include <hip/hip_runtime.h>
#include <hip/hip_bf16.h>
#include <stdint.h>

namespace naive {
typedef unsigned short u16;
__device__ __forceinline__ float b2f(u16 b) { return __uint_as_float(((unsigned)b) << 16); }
__device__ __forceinline__ u16 f2b(float f) { unsigned u = __float_as_uint(f); u += 0x7FFFu + ((u >> 16) & 1u); return (u16)(u >> 16); }
__device__ __forceinline__ float rb(float f) { return b2f(f2b(f)); }

constexpr int S = 2048, D = 1024, F = 3584, H = 32, DH = 32;

__global__ void k_ln(const u16* x, u16* xn) {
  int row = blockIdx.x; int t = threadIdx.x;
  __shared__ float red[256];
  float v[4]; float s = 0;
  for (int i = 0; i < 4; ++i) { v[i] = b2f(x[row * D + t * 4 + i]); s += v[i]; }
  red[t] = s; __syncthreads();
  for (int o = 128; o > 0; o >>= 1) { if (t < o) red[t] += red[t + o]; __syncthreads(); }
  float mean = red[0] / D; __syncthreads();
  float q = 0; for (int i = 0; i < 4; ++i) { float d = v[i] - mean; q += d * d; }
  red[t] = q; __syncthreads();
  for (int o = 128; o > 0; o >>= 1) { if (t < o) red[t] += red[t + o]; __syncthreads(); }
  float var = red[0] / D;
  float mb = rb(mean), vb = rb(var);
  float den = rb(sqrtf(rb(vb + 1e-5f)));
  for (int i = 0; i < 4; ++i) xn[row * D + t * 4 + i] = f2b(rb(v[i] - mb) / den);
}

__global__ void k_gemm(const u16* A, const u16* B, u16* C, int M, int N, int K) {
  __shared__ float As[32][33], Bs[32][33];
  int tx = threadIdx.x, ty = threadIdx.y; int n = blockIdx.x * 32 + tx, m = blockIdx.y * 32 + ty;
  float acc = 0;
  for (int k0 = 0; k0 < K; k0 += 32) {
    As[ty][tx] = b2f(A[(size_t)m * K + k0 + tx]);
    Bs[ty][tx] = b2f(B[(size_t)(k0 + ty) * N + n]);
    __syncthreads();
    for (int k = 0; k < 32; ++k) acc += As[ty][k] * Bs[k][tx];
    __syncthreads();
  }
  C[(size_t)m * N + n] = f2b(acc);
}

__global__ void k_attn(const u16* qkv, u16* o) {
  int idx = blockIdx.x * blockDim.x + threadIdx.x;
  int s = idx / H, h = idx % H;
  const u16* q = qkv + (size_t)s * 3 * D + h * DH;
  float lg[8]; float mx = -1e30f;
  for (int w = 0; w < 8; ++w) {
    int p = s - 2 * w;
    if (p >= 0) {
      const u16* k = qkv + (size_t)p * 3 * D + D + h * DH; float a = 0;
      for (int d = 0; d < DH; ++d) a += b2f(q[d]) * b2f(k[d]);
      lg[w] = rb(rb(a) * 0.17677669529663687f);
    } else lg[w] = rb(-1e9f);
    mx = fmaxf(mx, lg[w]);
  }
  float sum = 0; for (int w = 0; w < 8; ++w) { lg[w] = expf(lg[w] - mx); sum += lg[w]; }
  for (int w = 0; w < 8; ++w) lg[w] = rb(lg[w] / sum);
  for (int d = 0; d < DH; ++d) {
    float a = 0;
    for (int w = 0; w < 8; ++w) { int p = s - 2 * w; if (p < 0) p = 0; a += lg[w] * b2f(qkv[(size_t)p * 3 * D + 2 * D + h * DH + d]); }
    o[(size_t)s * D + h * DH + d] = f2b(a);
  }
}

__global__ void k_glu(const u16* g, const u16* u, u16* h, int n) {
  int i = blockIdx.x * blockDim.x + threadIdx.x; if (i >= n) return;
  float gv = b2f(g[i]); float sl = rb(gv / (1.0f + expf(-gv))); h[i] = f2b(sl * b2f(u[i]));
}
__global__ void k_final(const u16* x, const u16* s, const u16* f, u16* out, int n) {
  int i = blockIdx.x * blockDim.x + threadIdx.x; if (i >= n) return;
  float fv = b2f(f[i]); float sl = rb(fv / (1.0f + expf(-fv))); out[i] = f2b(b2f(x[i]) + rb(b2f(s[i]) * sl));
}

static void run(void* const* d_in, void* d_out, void* d_ws, hipStream_t stream) {
  const u16* x = (const u16*)d_in[0]; const u16* wqkv = (const u16*)d_in[1]; const u16* wo = (const u16*)d_in[2];
  const u16* wg = (const u16*)d_in[4]; const u16* wu = (const u16*)d_in[5]; const u16* wd = (const u16*)d_in[6];
  u16* ws = (u16*)d_ws;
  u16* xn = ws; u16* qkv = xn + S * D; u16* o = qkv + S * 3 * D; u16* s = o + S * D; u16* g = s + S * D; u16* u = g + S * F; u16* h = u + S * F; u16* f = h + S * F;
  k_ln<<<S, 256, 0, stream>>>(x, xn);
  k_gemm<<<dim3(3 * D / 32, S / 32), dim3(32, 32), 0, stream>>>(xn, wqkv, qkv, S, 3 * D, D);
  k_attn<<<S * H / 256, 256, 0, stream>>>(qkv, o);
  k_gemm<<<dim3(D / 32, S / 32), dim3(32, 32), 0, stream>>>(o, wo, s, S, D, D);
  k_gemm<<<dim3(F / 32, S / 32), dim3(32, 32), 0, stream>>>(xn, wg, g, S, F, D);
  k_gemm<<<dim3(F / 32, S / 32), dim3(32, 32), 0, stream>>>(xn, wu, u, S, F, D);
  k_glu<<<S * F / 256, 256, 0, stream>>>(g, u, h, S * F);
  k_gemm<<<dim3(D / 32, S / 32), dim3(32, 32), 0, stream>>>(h, wd, f, S, D, F);
  k_final<<<S * D / 256, 256, 0, stream>>>(x, s, f, (u16*)d_out, S * D);
}
}

extern "C" void kernel_launch(void* const* d_in, const int* in_sizes, int n_in, void* d_out, int out_size, void* d_ws, size_t ws_size, hipStream_t stream) {
  naive::run(d_in, d_out, d_ws, stream);
}
```

```cpp
#include <hip/hip_runtime.h>
#include <stdint.h>

#define LAS __attribute__((address_space(3)))
#define GAS __attribute__((address_space(1)))
typedef __bf16 bf16x8 __attribute__((ext_vector_type(8)));
typedef short s16x4 __attribute__((ext_vector_type(4)));
typedef short s16x8 __attribute__((ext_vector_type(8)));
typedef float f32x4 __attribute__((ext_vector_type(4)));
typedef float f32x2 __attribute__((ext_vector_type(2)));
typedef unsigned u32x4 __attribute__((ext_vector_type(4)));
typedef unsigned u32x2 __attribute__((ext_vector_type(2)));
typedef int i32x4 __attribute__((ext_vector_type(4)));
typedef unsigned short u16;

constexpr int S_ = 2048, D_ = 1024, F_ = 3584;
constexpr int KS2 = 144;
constexpr size_t WS_FLAGS = 0;
constexpr size_t WS_FLAGS_BYTES = 65536;
constexpr size_t WS_XN = 65536;
constexpr size_t WS_A2 = WS_XN + 4194304;
constexpr size_t WS_HALO = WS_A2 + (size_t)128 * KS2 * 1024;
constexpr size_t WS_PART = WS_HALO + 8 * 32 * 2048;
constexpr size_t WS_STAMP = WS_PART + (size_t)128 * 65536;

#define P_STEPS 80
#define ST_AUX 16

__device__ __forceinline__ i32x4 make_srd(const void* p, unsigned bytes) {
  const unsigned long long a = (unsigned long long)p;
  i32x4 r; r.x = (int)(unsigned)a; r.y = (int)((unsigned)(a >> 32) & 0xffffu); r.z = (int)bytes; r.w = 0x00020000; return r;
}
__device__ __forceinline__ void glds16(unsigned voff, i32x4 srd, unsigned soff, unsigned ldsaddr) {
  asm volatile("s_mov_b32 m0, %3\n\ts_nop 0\n\tbuffer_load_dwordx4 %0, %1, %2 offen lds" ::"v"(voff), "s"(srd), "s"(soff), "s"(ldsaddr) : "memory");
}
#define WAIT_V(n) asm volatile("s_waitcnt vmcnt(" #n ")" ::: "memory")
#define WAIT_L(n) asm volatile("s_waitcnt lgkmcnt(" #n ")" ::: "memory")
#define SCHED() __builtin_amdgcn_sched_barrier(0)
#define BAR() do { SCHED(); __builtin_amdgcn_s_barrier(); SCHED(); } while (0)

typedef __bf16 bf16x2 __attribute__((ext_vector_type(2)));
__device__ __forceinline__ unsigned cvt_pk(float lo, float hi) { const f32x2 v = {lo, hi}; return __builtin_bit_cast(unsigned, __builtin_convertvector(v, bf16x2)); }
__device__ __forceinline__ float bf_lo(unsigned u) { return __uint_as_float(u << 16); }
__device__ __forceinline__ float bf_hi(unsigned u) { return __uint_as_float(u & 0xffff0000u); }
__device__ __forceinline__ float rbf(float f) { return bf_lo(cvt_pk(f, f)); }
__device__ __forceinline__ bf16x8 pack8(const f32x4& a, const f32x4& b) {
  u32x4 w; w.x = cvt_pk(a[0], a[1]); w.y = cvt_pk(a[2], a[3]); w.z = cvt_pk(b[0], b[1]); w.w = cvt_pk(b[2], b[3]);
  return __builtin_bit_cast(bf16x8, w);
}
__device__ __forceinline__ bf16x8 lds_tr8(LAS unsigned char* p0, LAS unsigned char* p1) {
  s16x4 a = __builtin_amdgcn_ds_read_tr16_b64_v4i16((LAS s16x4*)p0);
  s16x4 b = __builtin_amdgcn_ds_read_tr16_b64_v4i16((LAS s16x4*)p1);
  s16x8 c = __builtin_shufflevector(a, b, 0, 1, 2, 3, 4, 5, 6, 7);
  return __builtin_bit_cast(bf16x8, c);
}
__device__ __forceinline__ float silu_f(float g) { return g * __builtin_amdgcn_rcpf(1.0f + __builtin_amdgcn_exp2f(g * -1.44269504089f)); }
#define MFMA(a, b, c) __builtin_amdgcn_mfma_f32_16x16x32_bf16(a, b, c, 0, 0, 0)

#define STAMP(k) do {} while (0)

__global__ __launch_bounds__(512) void k0_ln(const u16* __restrict__ x, unsigned char* __restrict__ ws) {
  __shared__ float red[2][8][16];
  const int tid = threadIdx.x, w = tid >> 6, l = tid & 63, m = l & 15, g = l >> 4;
  const int mf = blockIdx.x;
  if (tid < 32) ((u32x4*)(ws + WS_FLAGS))[mf * 32 + tid] = (u32x4){0u, 0u, 0u, 0u};
  const u16* row = x + (size_t)(mf * 16 + m) * D_ + w * 128 + g * 8;
  u32x4 v[4];
#pragma unroll
  for (int i = 0; i < 4; ++i) v[i] = *(const u32x4*)(row + i * 32);
  float f[32]; float s = 0.f;
#pragma unroll
  for (int i = 0; i < 4; ++i)
#pragma unroll
    for (int j = 0; j < 4; ++j) { f[i * 8 + 2 * j] = bf_lo(v[i][j]); f[i * 8 + 2 * j + 1] = bf_hi(v[i][j]); s += f[i * 8 + 2 * j] + f[i * 8 + 2 * j + 1]; }
  s += __shfl_xor(s, 16); s += __shfl_xor(s, 32);
  if (g == 0) red[0][w][m] = s;
  __syncthreads();
  float mean = 0.f;
#pragma unroll
  for (int i = 0; i < 8; ++i) mean += red[0][i][m];
  mean *= (1.0f / D_);
  float q = 0.f;
#pragma unroll
  for (int i = 0; i < 32; ++i) { const float d = f[i] - mean; q += d * d; }
  q += __shfl_xor(q, 16); q += __shfl_xor(q, 32);
  if (g == 0) red[1][w][m] = q;
  __syncthreads();
  float var = 0.f;
#pragma unroll
  for (int i = 0; i < 8; ++i) var += red[1][i][m];
  var *= (1.0f / D_);
  const float mb = rbf(mean);
  const float den = rbf(sqrtf(rbf(rbf(var) + 1e-5f)));
  unsigned char* dst = ws + WS_XN + ((size_t)mf * 32 + w * 4) * 1024 + l * 16;
#pragma unroll
  for (int i = 0; i < 4; ++i) {
    u32x4 o;
#pragma unroll
    for (int j = 0; j < 4; ++j) o[j] = cvt_pk(rbf(f[i * 8 + 2 * j] - mb) / den, rbf(f[i * 8 + 2 * j + 1] - mb) / den);
    *(u32x4*)(dst + i * 1024) = o;
  }
}

constexpr int K1_ASLOT = 16384, K1_BSLOT = 20480, K1_BOFF = 4 * K1_ASLOT, K1_LDS = K1_BOFF + 4 * K1_BSLOT;

struct K1Ctx {
  LAS unsigned char* lds;
  unsigned aoff;
  unsigned vb[6];
  unsigned vst0, vst1;
  i32x4 s0, s1, s2;
  unsigned so0, so1;
  unsigned ldst;
};

template <int TYPE> __device__ __forceinline__ void k1_stage_b(const K1Ctx& c, int s, int slot, unsigned cnt, int lim) {
  const unsigned d = K1_BOFF + slot * K1_BSLOT + c.ldst;
  unsigned t0, t1;
  if (TYPE == 0) {
    const unsigned kq = (unsigned)s * (32u * 6144u) + c.so0, kg = (unsigned)s * (32u * 7168u) + c.so0;
    asm volatile(
      "s_cmp_ge_u32 %10, %11\n\ts_cbranch_scc1 .Lsk%=\n\t"
      "s_mov_b32 m0, %9\n\ts_add_u32 %0, %7, 0x800\n\tbuffer_load_dwordx4 %2, %4, %7 offen lds\n\t"
      "s_add_u32 m0, %9, 0x400\n\ts_add_u32 %1, %7, 0x1000\n\tbuffer_load_dwordx4 %2, %4, %0 offen lds\n\t"
      "s_add_u32 m0, %9, 0x800\n\ts_nop 0\n\tbuffer_load_dwordx4 %2, %4, %1 offen lds\n\t"
      "s_add_u32 m0, %9, 0xc00\n\ts_nop 0\n\tbuffer_load_dwordx4 %3, %5, %8 offen lds\n\t"
      "s_add_u32 m0, %9, 0x1000\n\ts_nop 0\n\tbuffer_load_dwordx4 %3, %6, %8 offen lds\n"
      ".Lsk%=:"
      : "=&s"(t0), "=&s"(t1)
      : "v"(c.vst0), "v"(c.vst1), "s"(c.s0), "s"(c.s1), "s"(c.s2), "s"(kq), "s"(kg), "s"(d), "s"(cnt), "i"(lim) : "memory", "scc");
  } else {
    const unsigned k0 = (unsigned)s * (32u * 7168u) + c.so0, k1 = (unsigned)s * (32u * 7168u) + c.so1;
    asm volatile(
      "s_cmp_ge_u32 %10, %11\n\ts_cbranch_scc1 .Lsk%=\n\t"
      "s_mov_b32 m0, %9\n\ts_add_u32 %0, %7, 0x80\n\tbuffer_load_dwordx4 %2, %4, %7 offen lds\n\t"
      "s_add_u32 m0, %9, 0x400\n\ts_nop 0\n\tbuffer_load_dwordx4 %2, %4, %0 offen lds\n\t"
      "s_add_u32 m0, %9, 0x800\n\ts_nop 0\n\tbuffer_load_dwordx4 %2, %5, %7 offen lds\n\t"
      "s_add_u32 m0, %9, 0xc00\n\ts_nop 0\n\tbuffer_load_dwordx4 %2, %5, %0 offen lds\n\t"
      "s_add_u32 m0, %9, 0x1000\n\ts_nop 0\n\tbuffer_load_dwordx4 %3, %6, %8 offen lds\n"
      ".Lsk%=:"
      : "=&s"(t0), "=&s"(t1)
      : "v"(c.vst0), "v"(c.vst1), "s"(c.s0), "s"(c.s1), "s"(c.s2), "s"(k0), "s"(k1), "s"(d), "s"(cnt), "i"(lim) : "memory", "scc");
  }
}
__device__ __forceinline__ void k1_stage_a(const K1Ctx& c, int s, int slot, unsigned cnt, int lim) {
  const unsigned d = slot * K1_ASLOT + c.ldst;
  const unsigned k = (unsigned)s * 1024u + c.so0;
  unsigned t0;
  asm volatile(
    "s_cmp_ge_u32 %5, %6\n\ts_cbranch_scc1 .Lsk%=\n\t"
    "s_mov_b32 m0, %4\n\ts_add_u32 %0, %3, 0x8000\n\tbuffer_load_dwordx4 %1, %2, %3 offen lds\n\t"
    "s_add_u32 m0, %4, 0x400\n\ts_nop 0\n\tbuffer_load_dwordx4 %1, %2, %0 offen lds\n\t"
    "s_add_u32 m0, %4, 0x800\n\ts_add_u32 %0, %3, 0x10000\n\tbuffer_load_dwordx4 %1, %2, %0 offen lds\n\t"
    "s_add_u32 m0, %4, 0xc00\n\ts_add_u32 %0, %3, 0x18000\n\tbuffer_load_dwordx4 %1, %2, %0 offen lds\n"
    ".Lsk%=:"
    : "=&s"(t0) : "v"(c.vst0), "s"(c.s0), "s"(k), "s"(d), "s"(cnt), "i"(lim) : "memory", "scc");
}
#define WAIT_V2(STEADY, TAIL, cnt, lim) do { asm volatile("s_cmp_lt_u32 %0, %1\n\ts_cbranch_scc1 .Lw%=\n\ts_waitcnt vmcnt(" #TAIL ")\n.Lw%=:\n\ts_waitcnt vmcnt(" #STEADY ")" :: "s"(cnt), "i"(lim) : "memory", "scc"); } while (0)
#define BAR_IF(cnt, lim) do { SCHED(); asm volatile("s_cmp_ge_u32 %0, %1\n\ts_cbranch_scc1 .Lb%=\n\ts_barrier\n.Lb%=:" :: "s"(cnt), "i"(lim) : "memory", "scc"); SCHED(); } while (0)
template <int TYPE> __device__ __forceinline__ void k1_read(const K1Ctx& c, int slot, bf16x8 (&af)[4], bf16x8 (&bfr)[10]) {
  LAS unsigned char* lds = c.lds;
  const int bs = slot * K1_BSLOT;
  if (TYPE == 0) {
#pragma unroll
    for (int j = 0; j < 8; ++j) { LAS unsigned char* p = lds + c.vb[j & 1] + bs + (j >> 1) * 1024; bfr[j] = lds_tr8(p, p + 512); }
#pragma unroll
    for (int j = 0; j < 2; ++j) { LAS unsigned char* p = lds + c.vb[2 + j] + bs; bfr[8 + j] = lds_tr8(p, p + 512); }
  } else {
#pragma unroll
    for (int j = 0; j < 8; ++j) { LAS unsigned char* p = lds + c.vb[j & 3] + bs + (j >> 2) * 2048; bfr[j] = lds_tr8(p, p + 512); }
#pragma unroll
    for (int j = 0; j < 2; ++j) { LAS unsigned char* p = lds + c.vb[4 + j] + bs; bfr[8 + j] = lds_tr8(p, p + 256); }
  }
#pragma unroll
  for (int i = 0; i < 4; ++i) af[i] = *(LAS bf16x8*)(lds + c.aoff + slot * K1_ASLOT + i * 1024);
}
template <int TYPE> __device__ __forceinline__ void k1_mma(f32x4 (&acc)[4][10], const bf16x8 (&af)[4], const bf16x8 (&bfr)[10]) {
  __builtin_amdgcn_s_setprio(1);
#pragma unroll
  for (int j = 0; j < 10; ++j)
#pragma unroll
    for (int i = 0; i < 4; ++i) {
      if (TYPE == 0 && (j == 4 || j == 5)) acc[i][j] = MFMA(af[i], bfr[j], acc[i][j]);
      else acc[i][j] = MFMA(bfr[j], af[i], acc[i][j]);
    }
  __builtin_amdgcn_s_setprio(0);
}

template <int TYPE> __device__ __forceinline__ void k1_body(LAS unsigned char* lds, const u16* wqkv, const u16* wg, const u16* wu, unsigned char* ws, int pm, int pa  ) {
  const int tid = threadIdx.x;
  const int w = __builtin_amdgcn_readfirstlane(tid >> 6), l = tid & 63;
  const int grp = w >> 2, wr = w & 3;
  const int g = l >> 4, q = (l & 15) >> 2, p = l & 3;
  STAMP(0);
  K1Ctx c; c.lds = lds;
  c.aoff = l * 16 + wr * 4096;
  {
    const unsigned s2 = ((q >> 1) & 1) | ((g & 1) << 1);
    const unsigned lanep = K1_BOFF + g * 5120 + q * 128 + (p >> 1) * 16 + (p & 1) * 8;
    if (TYPE == 0) {
      c.vb[0] = lanep + (((2 * grp) ^ s2) << 5); c.vb[1] = lanep + (((2 * grp + 1) ^ s2) << 5);
      c.vb[2] = c.vb[0] + 4096; c.vb[3] = c.vb[1] + 4096; c.vb[4] = 0; c.vb[5] = 0;
    } else {
#pragma unroll
      for (int j = 0; j < 4; ++j) c.vb[j] = lanep + ((j ^ s2) << 5) + grp * 1024;
      const unsigned mx = K1_BOFF + (2 * (g >> 1) * 5 + 4) * 1024 + (g & 1) * 512 + q * 64 + (((2 * grp + (p >> 1)) ^ ((g & 1) << 1)) << 4) + (p & 1) * 8;
      c.vb[4] = mx; c.vb[5] = mx + 5120;
    }
  }
  const int c0 = 1024 + 160 * pa;
  const int odd = pa & 1;
  if (grp == 0) {
    const unsigned r8 = l >> 3, sw = (((r8 >> 1) & 1) | ((wr & 1) << 1)) << 1, ch = (l & 7) ^ sw;
    c.ldst = wr * 5120;
    if (TYPE == 0) {
      c.vst0 = (8 * wr + r8) * 6144 + ch * 16; c.vst1 = (8 * wr + r8) * 7168 + ch * 16;
      c.s0 = make_srd(wqkv, D_ * 3 * D_ * 2); c.s1 = make_srd(wg, D_ * F_ * 2); c.s2 = make_srd(wu, D_ * F_ * 2);
      c.so0 = pa * 128; c.so1 = 0;
    } else {
      c.vst0 = (8 * wr + r8) * 7168 + ch * 16;
      const unsigned r16 = l >> 2, chm = (l & 3) ^ (((r16 >> 3) & 1) << 1);
      c.vst1 = (16 * (wr >> 1) + r16) * 7168 + chm * 16;
      c.s0 = make_srd(wg, D_ * F_ * 2); c.s1 = make_srd(wu, D_ * F_ * 2); c.s2 = make_srd((wr & 1) ? wu : wg, D_ * F_ * 2);
      c.so0 = (c0 + (odd ? 32 : 0)) * 2; c.so1 = (c0 + (odd ? 0 : 128)) * 2;
    }
  } else {
    c.vst0 = l * 16; c.vst1 = 0;
    c.s0 = make_srd(ws + WS_XN, S_ * D_ * 2); c.s1 = c.s0; c.s2 = c.s0;
    c.so0 = (pm * 16 + 4 * wr) * 32768; c.so1 = 0;
    c.ldst = wr * 4096;
  }

  f32x4 acc[4][10];
#pragma unroll
  for (int i = 0; i < 4; ++i)
#pragma unroll
    for (int j = 0; j < 10; ++j) acc[i][j] = (f32x4){0.f, 0.f, 0.f, 0.f};
  bf16x8 af[4], bfr[10];

  if (grp == 0) {
    k1_stage_b<TYPE>(c, 0, 0, 0, 1); k1_stage_b<TYPE>(c, 1, 1, 0, 1); k1_stage_b<TYPE>(c, 2, 2, 0, 1);
    WAIT_V(10); BAR();
    STAMP(1);
#define K1_G0_STEP(J, TAILV) do { \
      k1_read<TYPE>(c, J, af, bfr); SCHED(); \
      k1_stage_b<TYPE>(c, s + (J) + 3, ((J) + 3) & 3, s, (J) == 0 ? 32 : 28); \
      WAIT_L(0); BAR(); \
      k1_mma<TYPE>(acc, af, bfr); \
      WAIT_V2(10, TAILV, s, 28); \
      BAR(); } while (0)
#pragma nounroll
    for (unsigned s = 0; s < 32; s += 4) {
      K1_G0_STEP(0, 10); K1_G0_STEP(1, 5); K1_G0_STEP(2, 0); K1_G0_STEP(3, 0);
    }
  } else {
    k1_stage_a(c, 0, 0, 0, 1); k1_stage_a(c, 1, 1, 0, 1); k1_stage_a(c, 2, 2, 0, 1);
    WAIT_V(8); BAR();
    BAR();
#define K1_G1_STEP(J, TAILV) do { \
      k1_read<TYPE>(c, J, af, bfr); SCHED(); \
      k1_stage_a(c, s + (J) + 3, ((J) + 3) & 3, s, (J) == 0 ? 32 : 28); \
      WAIT_L(0); WAIT_V2(8, TAILV, s, 28); \
      BAR(); \
      k1_mma<TYPE>(acc, af, bfr); \
      if ((J) < 3) BAR(); else BAR_IF(s, 28); } while (0)
#pragma nounroll
    for (unsigned s = 0; s < 32; s += 4) {
      K1_G1_STEP(0, 8); K1_G1_STEP(1, 4); K1_G1_STEP(2, 0); K1_G1_STEP(3, 0);
    }
  }
  STAMP(2);

  const __amdgpu_buffer_rsrc_t a2 = __builtin_amdgcn_make_buffer_rsrc(ws + WS_A2, 0, 128 * KS2 * 1024, 0x00020000);
  const unsigned mf0 = pm * 16 + 4 * wr;
  if (TYPE == 1) {
    const unsigned ksf = c0 / 32 + (odd ? 1 : 0) + 2 * grp, ksm = c0 / 32 + (odd ? 0 : 4);
#pragma unroll
    for (int i = 0; i < 4; ++i) {
      const unsigned base = (mf0 + i) * (KS2 * 1024);
#pragma unroll
      for (int t = 0; t < 2; ++t) {
        f32x4 h0, h1;
#pragma unroll
        for (int r = 0; r < 4; ++r) { h0[r] = silu_f(acc[i][2 * t][r]) * acc[i][2 * t + 4][r]; h1[r] = silu_f(acc[i][2 * t + 1][r]) * acc[i][2 * t + 5][r]; }
        __builtin_amdgcn_raw_buffer_store_b128(__builtin_bit_cast(u32x4, pack8(h0, h1)), a2, l * 16 + base + (ksf + t) * 1024, 0, ST_AUX);
      }
      f32x4 h0;
#pragma unroll
      for (int r = 0; r < 4; ++r) h0[r] = silu_f(acc[i][8][r]) * acc[i][9][r];
      u32x2 o; o.x = cvt_pk(h0[0], h0[1]); o.y = cvt_pk(h0[2], h0[3]);
      __builtin_amdgcn_raw_buffer_store_b64(o, a2, l * 16 + grp * 8 + base + ksm * 1024, 0, ST_AUX);
    }
  } else {
    const int head = 2 * pa + grp;
    bf16x8 kop[5]; u32x2 vp[5][2];
#pragma unroll
    for (int i = 0; i < 4; ++i) {
      kop[i + 1] = pack8(acc[i][2], acc[i][3]);
#pragma unroll
      for (int d = 0; d < 2; ++d) { vp[i + 1][d].x = cvt_pk(acc[i][4 + d][0], acc[i][4 + d][1]); vp[i + 1][d].y = cvt_pk(acc[i][4 + d][2], acc[i][4 + d][3]); }
    }
    const u32x4 klast = __builtin_bit_cast(u32x4, kop[4]);
    const u32x4 vlast = (u32x4){vp[4][0].x, vp[4][0].y, vp[4][1].x, vp[4][1].y};
    const __amdgpu_buffer_rsrc_t hr = __builtin_amdgcn_make_buffer_rsrc(ws + WS_HALO, 0, 8 * 32 * 2048, 0x00020000);
    GAS unsigned* flags = (GAS unsigned*)(ws + WS_FLAGS);
    if (wr == 3) {
      if (pm < 7) {
        const unsigned ho = (pm * 32 + head) * 2048;
        __builtin_amdgcn_raw_buffer_store_b128(klast, hr, l * 16 + ho, 0, 16);
        __builtin_amdgcn_raw_buffer_store_b128(vlast, hr, l * 16 + ho + 1024, 0, 16);
        WAIT_V(0);
        if (l == 0) __hip_atomic_store(flags + (pm * 32 + head) * 16, 1u, __ATOMIC_RELAXED, __HIP_MEMORY_SCOPE_AGENT);
      }
    } else {
      *(LAS u32x4*)(lds + w * 2048 + l * 16) = klast;
      *(LAS u32x4*)(lds + w * 2048 + 1024 + l * 16) = vlast;
    }
#pragma unroll
    for (int i = 0; i < 4; ++i) {
      f32x4 h0, h1;
#pragma unroll
      for (int r = 0; r < 4; ++r) { h0[r] = silu_f(acc[i][6][r]) * acc[i][8][r]; h1[r] = silu_f(acc[i][7][r]) * acc[i][9][r]; }
      __builtin_amdgcn_raw_buffer_store_b128(__builtin_bit_cast(u32x4, pack8(h0, h1)), a2, l * 16 + (mf0 + i) * (KS2 * 1024) + (2 * pa + grp) * 1024, 0, ST_AUX);
    }
    WAIT_L(0); BAR();
    const int m = l & 15, par = m & 1;
    const float scale = 0.17677669529663687f * 1.44269504089f;
    auto attend = [&](int i, bool have_prev) {
      const bf16x8 qop = pack8(acc[i][0], acc[i][1]);
      const f32x4 z = (f32x4){0.f, 0.f, 0.f, 0.f};
      const f32x4 s0 = MFMA(kop[i + 1], qop, z);
      const f32x4 s1 = MFMA(kop[i], qop, z);
      float e[8]; float mx = -3.0e38f;
#pragma unroll
      for (int r = 0; r < 4; ++r) {
        const int pk = 4 * g + r;
        const bool v1 = ((r & 1) == par) && (pk >= m + 2) && have_prev, v0 = ((r & 1) == par) && (pk <= m);
        e[r] = v1 ? s1[r] * scale : -3.0e38f; e[4 + r] = v0 ? s0[r] * scale : -3.0e38f;
        mx = fmaxf(mx, fmaxf(e[r], e[4 + r]));
      }
      mx = fmaxf(mx, __shfl_xor(mx, 16)); mx = fmaxf(mx, __shfl_xor(mx, 32));
      float sum = 0.f;
#pragma unroll
      for (int r = 0; r < 8; ++r) { e[r] = __builtin_amdgcn_exp2f(e[r] - mx); sum += e[r]; }
      sum += __shfl_xor(sum, 16); sum += __shfl_xor(sum, 32);
      const float inv = __builtin_amdgcn_rcpf(sum);
      const bf16x8 pop = pack8((f32x4){e[0] * inv, e[1] * inv, e[2] * inv, e[3] * inv}, (f32x4){e[4] * inv, e[5] * inv, e[6] * inv, e[7] * inv});
      f32x4 o[2];
#pragma unroll
      for (int d = 0; d < 2; ++d) {
        const u32x4 vv = (u32x4){vp[i][d].x, vp[i][d].y, vp[i + 1][d].x, vp[i + 1][d].y};
        o[d] = MFMA(__builtin_bit_cast(bf16x8, vv), pop, z);
      }
      __builtin_amdgcn_raw_buffer_store_b128(__builtin_bit_cast(u32x4, pack8(o[0], o[1])), a2, l * 16 + (mf0 + i) * (KS2 * 1024) + (112 + head) * 1024, 0, ST_AUX);
    };
    attend(1, true); attend(2, true); attend(3, true);
    bool have_prev = true;
    u32x4 kh, vh;
    if (wr == 0) {
      if (pm > 0) {
        GAS unsigned* fl = flags + ((pm - 1) * 32 + head) * 16;
        for (unsigned spins = 0; spins < (1u << 22); ++spins) {
          if (__builtin_amdgcn_readfirstlane(__hip_atomic_load(fl, __ATOMIC_RELAXED, __HIP_MEMORY_SCOPE_AGENT)) != 0u) break;
          __builtin_amdgcn_s_sleep(1);
        }
        asm volatile("" ::: "memory");
        const unsigned ho = ((pm - 1) * 32 + head) * 2048;
        kh = __builtin_amdgcn_raw_buffer_load_b128(hr, l * 16, ho, 16);
        vh = __builtin_amdgcn_raw_buffer_load_b128(hr, l * 16, ho + 1024, 16);
      } else { kh = (u32x4){0u, 0u, 0u, 0u}; vh = kh; have_prev = false; }
    } else {
      kh = *(LAS u32x4*)(lds + (w - 1) * 2048 + l * 16);
      vh = *(LAS u32x4*)(lds + (w - 1) * 2048 + 1024 + l * 16);
    }
    kop[0] = __builtin_bit_cast(bf16x8, kh);
    vp[0][0] = (u32x2){vh.x, vh.y}; vp[0][1] = (u32x2){vh.z, vh.w};
    attend(0, have_prev);
  }
  STAMP(3);
}

__global__ __launch_bounds__(512, 2) void k1_gemm(const u16* __restrict__ wqkv, const u16* __restrict__ wg, const u16* __restrict__ wu, unsigned char* __restrict__ ws) {
  extern __shared__ __attribute__((aligned(16))) unsigned char shm[];
  const int b = blockIdx.x, xcd = b & 7, idx = b >> 3;
  const int pm = idx & 7, pn = idx >> 3;
  const int pa = xcd * 2 + (pn >> 1);
  if (pn & 1) k1_body<1>((LAS unsigned char*)shm, wqkv, wg, wu, ws, pm, pa);
  else k1_body<0>((LAS unsigned char*)shm, wqkv, wg, wu, ws, pm, pa);
}

constexpr int K2_SLOT = 16384, K2_NSLOT = 8, K2_LDS = K2_SLOT * K2_NSLOT;
struct K2Ctx {
  LAS unsigned char* lds;
  unsigned aoff, vb[4];
  unsigned vsta, vstb;
  i32x4 sa, swd, swo;
  unsigned soa, sob, ldst;
};
__device__ __forceinline__ void k2_stage(const K2Ctx& c, i32x4 sb, unsigned ka, unsigned kb, int slot, unsigned cnt, unsigned lim) {
  const unsigned d = slot * K2_SLOT + c.ldst;
  unsigned t0;
  asm volatile(
    "s_cmp_ge_u32 %8, %9\n\ts_cbranch_scc1 .Lsk%=\n\t"
    "s_mov_b32 m0, %7\n\ts_add_u32 %0, %5, 0x24000\n\tbuffer_load_dwordx4 %1, %3, %5 offen lds\n\t"
    "s_add_u32 m0, %7, 0x400\n\ts_nop 0\n\tbuffer_load_dwordx4 %1, %3, %0 offen lds\n\t"
    "s_add_u32 m0, %7, 0x2000\n\ts_add_u32 %0, %6, 0x80\n\tbuffer_load_dwordx4 %2, %4, %6 offen lds\n\t"
    "s_add_u32 m0, %7, 0x2400\n\ts_nop 0\n\tbuffer_load_dwordx4 %2, %4, %0 offen lds\n"
    ".Lsk%=:"
    : "=&s"(t0) : "v"(c.vsta), "v"(c.vstb), "s"(c.sa), "s"(sb), "s"(ka), "s"(kb), "s"(d), "s"(cnt), "s"(lim) : "memory", "scc");
}
__device__ __forceinline__ void k2_read(const K2Ctx& c, int slot, bf16x8 (&af)[4], bf16x8 (&bfr)[4]) {
  LAS unsigned char* lds = c.lds + slot * K2_SLOT;
#pragma unroll
  for (int j = 0; j < 4; ++j) { LAS unsigned char* p = lds + c.vb[j]; bfr[j] = lds_tr8(p, p + 4096); }
#pragma unroll
  for (int i = 0; i < 4; ++i) af[i] = *(LAS bf16x8*)(lds + c.aoff + i * 1024);
}
__device__ __forceinline__ void k2_mma(f32x4 (&acc)[4][4], const bf16x8 (&af)[4], const bf16x8 (&bfr)[4]) {
  __builtin_amdgcn_s_setprio(1);
#pragma unroll
  for (int j = 0; j < 4; ++j)
#pragma unroll
    for (int i = 0; i < 4; ++i) acc[i][j] = MFMA(bfr[j], af[i], acc[i][j]);
  __builtin_amdgcn_s_setprio(0);
}

template <int GRP> __device__ __forceinline__ void k2_run(const K2Ctx& c, f32x4 (&acc)[4][4], int n, i32x4 sb, unsigned ksA, unsigned ksB, unsigned stop) {
  bf16x8 af[4], bfr[4];
  unsigned ka = ksA * 1024u + c.soa, kb = ksB * 65536u + c.sob;
#pragma nounroll
  for (unsigned u = 0; u < (unsigned)n; u += 4) {
#pragma unroll
    for (int j = 0; j < 4; ++j) {
      const int slot = (2 * j + GRP) & 7;
      const unsigned uu = u + j;
      k2_read(c, slot, af, bfr); SCHED();
      k2_stage(c, sb, ka, kb, (slot + 7) & 7, uu, stop);
      ka += 1024u; kb += 65536u;
      WAIT_L(0);
      asm volatile("s_cmp_lt_u32 %0, %1\n\ts_cbranch_scc1 .Lw%=\n\ts_waitcnt vmcnt(0)\n.Lw%=:\n\ts_waitcnt vmcnt(12)" :: "s"(uu), "s"(stop) : "memory", "scc");
      BAR();
      k2_mma(acc, af, bfr);
      BAR();
    }
  }
}

template <bool ISP> __device__ __forceinline__ void k2_body(LAS unsigned char* lds, const u16* x, const u16* wo, const u16* wd, u16* out, unsigned char* ws, int rt, int ct) {
  constexpr int NT = ISP ? P_STEPS : KS2 - P_STEPS;
  static_assert(NT % 8 == 0, "positions must be a multiple of 8");
  const int tid = threadIdx.x;
  const int w = __builtin_amdgcn_readfirstlane(tid >> 6), l = tid & 63;
  const int grp = w >> 2, w4 = w & 3, wm = w4 >> 1, wn = w4 & 1;
  const int g = l >> 4, q = (l & 15) >> 2, p = l & 3;
  STAMP(0);
  K2Ctx c; c.lds = lds;
  c.aoff = l * 16 + wm * 4096;
  {
    const unsigned row = 4 * (g & 1) + q, s2 = (row >> 1) & 3;
    const unsigned lanep = 8192 + (g >> 1) * 2048 + wn * 1024 + row * 128 + (p >> 1) * 16 + (p & 1) * 8;
#pragma unroll
    for (int j = 0; j < 4; ++j) c.vb[j] = lanep + ((j ^ s2) << 5);
    const unsigned r8 = l >> 3, ch = (l & 7) ^ (((r8 >> 1) & 3) << 1);
    c.vsta = l * 16; c.vstb = (8 * w4 + r8) * 2048 + ch * 16;
    c.sa = make_srd(ws + WS_A2, 128 * KS2 * 1024); c.swd = make_srd(wd, F_ * D_ * 2); c.swo = make_srd(wo, D_ * D_ * 2);
    c.soa = (rt * 8 + 2 * w4) * (KS2 * 1024); c.sob = ct * 256; c.ldst = w4 * 2048;
  }
  f32x4 accf[4][4], accs[4][4];
#pragma unroll
  for (int i = 0; i < 4; ++i)
#pragma unroll
    for (int j = 0; j < 4; ++j) { accf[i][j] = (f32x4){0.f, 0.f, 0.f, 0.f}; accs[i][j] = accf[i][j]; }

  constexpr int HALFN = NT / 2, H0 = P_STEPS - 32;
  constexpr unsigned BIG = 0x7fffffffu;
  if (grp == 0) {
    const unsigned o0 = ISP ? 0 : H0 + HALFN;
#pragma unroll
    for (int u = 0; u < 3; ++u) k2_stage(c, c.swd, (o0 + u) * 1024u + c.soa, (o0 + u) * 65536u + c.sob, 2 * u + 1, 0, 1);
    BAR();
    STAMP(1);
    if (ISP) { k2_run<0>(c, accs, 32, c.swd, o0 + 3, o0 + 3, BIG); k2_run<0>(c, accf, HALFN - 32, c.swd, o0 + 35, o0 + 35, HALFN - 35); }
    else k2_run<0>(c, accf, HALFN, c.swd, o0 + 3, o0 + 3, HALFN - 3);
    BAR();
  } else {
    if (ISP) {
#pragma unroll
      for (int u = 0; u < 4; ++u) k2_stage(c, c.swo, (112 + u) * 1024u + c.soa, u * 65536u + c.sob, 2 * u, 0, 1);
    } else {
#pragma unroll
      for (int u = 0; u < 4; ++u) k2_stage(c, c.swd, (H0 + u) * 1024u + c.soa, (H0 + u) * 65536u + c.sob, 2 * u, 0, 1);
    }
    WAIT_V(12); BAR();
    BAR();
    if (ISP) { k2_run<1>(c, accf, 28, c.swo, 112 + 4, 4, BIG); k2_run<1>(c, accf, HALFN - 28, c.swd, HALFN, HALFN, HALFN - 32); }
    else k2_run<1>(c, accf, HALFN, c.swd, H0 + 4, H0 + 4, HALFN - 4);
  }
  STAMP(2);
  const int tile = rt * 8 + ct;
  GAS unsigned* cnt = (GAS unsigned*)(ws + WS_FLAGS + 32768) + tile * 16;
  const __amdgpu_buffer_rsrc_t pr = __builtin_amdgcn_make_buffer_rsrc(ws + WS_PART, 0, 128 * 65536, 0x00020000);
  LAS unsigned char* xb = lds + w4 * 24576 + l * 16;
  if (grp == 0) {
#pragma unroll
    for (int i = 0; i < 2; ++i)
#pragma unroll
      for (int j = 0; j < 4; ++j) {
        *(LAS f32x4*)(xb + (i * 4 + j) * 1024) = accf[2 + i][j];
        if (ISP) *(LAS f32x4*)(xb + 8192 + (i * 4 + j) * 1024) = accs[2 + i][j];
      }
  } else {
#pragma unroll
    for (int i = 0; i < 2; ++i)
#pragma unroll
      for (int j = 0; j < 4; ++j) *(LAS f32x4*)(xb + 16384 + (i * 4 + j) * 1024) = accf[i][j];
  }
  WAIT_L(0); BAR();
  f32x4 f[2][4], sv[2][4];
  if (grp == 0) {
#pragma unroll
    for (int i = 0; i < 2; ++i)
#pragma unroll
      for (int j = 0; j < 4; ++j) { f[i][j] = accf[i][j] + *(LAS f32x4*)(xb + 16384 + (i * 4 + j) * 1024); sv[i][j] = accs[i][j]; }
  } else {
#pragma unroll
    for (int i = 0; i < 2; ++i)
#pragma unroll
      for (int j = 0; j < 4; ++j) {
        f[i][j] = accf[2 + i][j] + *(LAS f32x4*)(xb + (i * 4 + j) * 1024);
        if (ISP) sv[i][j] = *(LAS f32x4*)(xb + 8192 + (i * 4 + j) * 1024);
      }
  }
  const unsigned po = tile * 65536 + w * 8192;
  if (!ISP) {
#pragma unroll
    for (int i = 0; i < 2; ++i)
#pragma unroll
      for (int j = 0; j < 4; ++j) __builtin_amdgcn_raw_buffer_store_b128(__builtin_bit_cast(u32x4, f[i][j]), pr, l * 16 + po + (i * 4 + j) * 1024, 0, 16);
    WAIT_V(0);
    if (l == 0) __hip_atomic_fetch_add(cnt, 1u, __ATOMIC_RELAXED, __HIP_MEMORY_SCOPE_AGENT);
  } else {
    const int m = l & 15;
    const int row0 = rt * 128 + wm * 64 + (2 * grp) * 16 + m, col0 = ct * 128 + wn * 64 + 4 * g;
    u32x2 xr[2][4];
#pragma unroll
    for (int i = 0; i < 2; ++i)
#pragma unroll
      for (int j = 0; j < 4; ++j) xr[i][j] = *(const u32x2*)(x + (size_t)(row0 + i * 16) * D_ + col0 + j * 16);
    if (w == 0) {
      for (unsigned spins = 0; spins < (1u << 22); ++spins) {
        if ((unsigned)__builtin_amdgcn_readfirstlane(__hip_atomic_load(cnt, __ATOMIC_RELAXED, __HIP_MEMORY_SCOPE_AGENT)) >= 8u) break;
        __builtin_amdgcn_s_sleep(1);
      }
    }
    asm volatile("" ::: "memory"); BAR(); asm volatile("" ::: "memory");
#pragma unroll
    for (int i = 0; i < 2; ++i)
#pragma unroll
      for (int j = 0; j < 4; ++j) f[i][j] += __builtin_bit_cast(f32x4, __builtin_amdgcn_raw_buffer_load_b128(pr, l * 16, po + (i * 4 + j) * 1024, 16));
#pragma unroll
    for (int i = 0; i < 2; ++i)
#pragma unroll
      for (int j = 0; j < 4; ++j) {
        float o[4];
        const float xv[4] = {bf_lo(xr[i][j].x), bf_hi(xr[i][j].x), bf_lo(xr[i][j].y), bf_hi(xr[i][j].y)};
#pragma unroll
        for (int r = 0; r < 4; ++r) o[r] = xv[r] + rbf(rbf(sv[i][j][r]) * rbf(silu_f(rbf(f[i][j][r]))));
        u32x2 ov; ov.x = cvt_pk(o[0], o[1]); ov.y = cvt_pk(o[2], o[3]);
        *(u32x2*)(out + (size_t)(row0 + i * 16) * D_ + col0 + j * 16) = ov;
      }
  }
  STAMP(3);
}

__global__ __launch_bounds__(512, 2) void k2_gemm(const u16* __restrict__ x, const u16* __restrict__ wo, const u16* __restrict__ wd, u16* __restrict__ out, unsigned char* __restrict__ ws) {
  extern __shared__ __attribute__((aligned(16))) unsigned char shm[];
  const int b = blockIdx.x, xcd = b & 7, idx = b >> 3;
  const int pr = xcd & 3;
  const int rt = (pr & 1) * 8 + (idx & 7), ct = (pr >> 1) * 4 + (idx >> 3);
  if (xcd < 4) k2_body<true>((LAS unsigned char*)shm, x, wo, wd, out, ws, rt, ct);
  else k2_body<false>((LAS unsigned char*)shm, x, wo, wd, out, ws, rt, ct);
}

extern "C" void kernel_launch(void* const* d_in, const int* in_sizes, int n_in, void* d_out, int out_size, void* d_ws, size_t ws_size, hipStream_t stream) {
  const u16* x = (const u16*)d_in[0]; const u16* wqkv = (const u16*)d_in[1]; const u16* wo = (const u16*)d_in[2];
  const u16* wg = (const u16*)d_in[4]; const u16* wu = (const u16*)d_in[5]; const u16* wd = (const u16*)d_in[6];
  unsigned char* ws = (unsigned char*)d_ws;
  (void)hipFuncSetAttribute((const void*)k1_gemm, hipFuncAttributeMaxDynamicSharedMemorySize, K1_LDS);
  (void)hipFuncSetAttribute((const void*)k2_gemm, hipFuncAttributeMaxDynamicSharedMemorySize, K2_LDS);
  k0_ln<<<128, 512, 0, stream>>>(x, ws);
  k1_gemm<<<256, 512, K1_LDS, stream>>>(wqkv, wg, wu, ws);
  k2_gemm<<<256, 512, K2_LDS, stream>>>(x, wo, wd, (u16*)d_out, ws);
}
```

```cpp
#include <hip/hip_runtime.h>
#include <stdint.h>

#define LAS __attribute__((address_space(3)))
#define GAS __attribute__((address_space(1)))
typedef __bf16 bf16x8 __attribute__((ext_vector_type(8)));
typedef short s16x4 __attribute__((ext_vector_type(4)));
typedef short s16x8 __attribute__((ext_vector_type(8)));
typedef float f32x4 __attribute__((ext_vector_type(4)));
typedef float f32x2 __attribute__((ext_vector_type(2)));
typedef unsigned u32x4 __attribute__((ext_vector_type(4)));
typedef unsigned u32x2 __attribute__((ext_vector_type(2)));
typedef int i32x4 __attribute__((ext_vector_type(4)));
typedef unsigned short u16;

constexpr int S_ = 2048, D_ = 1024, F_ = 3584;
constexpr int KS2 = 144;
constexpr size_t WS_FLAGS = 0;
constexpr size_t WS_FLAGS_BYTES = 65536;
constexpr size_t WS_XN = 65536;
constexpr size_t WS_A2 = WS_XN + 4194304;
constexpr size_t WS_HALO = WS_A2 + (size_t)128 * KS2 * 1024;
constexpr size_t WS_PART = WS_HALO + 8 * 32 * 2048;
constexpr size_t WS_STAMP = WS_PART + (size_t)128 * 65536;

#define P_STEPS 80
#define ST_AUX 16

__device__ __forceinline__ i32x4 make_srd(const void* p, unsigned bytes) {
  const unsigned long long a = (unsigned long long)p;
  i32x4 r; r.x = (int)(unsigned)a; r.y = (int)((unsigned)(a >> 32) & 0xffffu); r.z = (int)bytes; r.w = 0x00020000; return r;
}
__device__ __forceinline__ void glds16(unsigned voff, i32x4 srd, unsigned soff, unsigned ldsaddr) {
  asm volatile("s_mov_b32 m0, %3\n\ts_nop 0\n\tbuffer_load_dwordx4 %0, %1, %2 offen lds" ::"v"(voff), "s"(srd), "s"(soff), "s"(ldsaddr) : "memory");
}
#define WAIT_V(n) asm volatile("s_waitcnt vmcnt(" #n ")" ::: "memory")
#define WAIT_L(n) asm volatile("s_waitcnt lgkmcnt(" #n ")" ::: "memory")
#define SCHED() __builtin_amdgcn_sched_barrier(0)
#define BAR() do { SCHED(); __builtin_amdgcn_s_barrier(); SCHED(); } while (0)

typedef __bf16 bf16x2 __attribute__((ext_vector_type(2)));
__device__ __forceinline__ unsigned cvt_pk(float lo, float hi) { const f32x2 v = {lo, hi}; return __builtin_bit_cast(unsigned, __builtin_convertvector(v, bf16x2)); }
__device__ __forceinline__ float bf_lo(unsigned u) { return __uint_as_float(u << 16); }
__device__ __forceinline__ float bf_hi(unsigned u) { return __uint_as_float(u & 0xffff0000u); }
__device__ __forceinline__ float rbf(float f) { return bf_lo(cvt_pk(f, f)); }
__device__ __forceinline__ bf16x8 pack8(const f32x4& a, const f32x4& b) {
  u32x4 w; w.x = cvt_pk(a[0], a[1]); w.y = cvt_pk(a[2], a[3]); w.z = cvt_pk(b[0], b[1]); w.w = cvt_pk(b[2], b[3]);
  return __builtin_bit_cast(bf16x8, w);
}
__device__ __forceinline__ bf16x8 lds_tr8(LAS unsigned char* p0, LAS unsigned char* p1) {
  s16x4 a = __builtin_amdgcn_ds_read_tr16_b64_v4i16((LAS s16x4*)p0);
  s16x4 b = __builtin_amdgcn_ds_read_tr16_b64_v4i16((LAS s16x4*)p1);
  s16x8 c = __builtin_shufflevector(a, b, 0, 1, 2, 3, 4, 5, 6, 7);
  return __builtin_bit_cast(bf16x8, c);
}
__device__ __forceinline__ float silu_f(float g) { return g * __builtin_amdgcn_rcpf(1.0f + __builtin_amdgcn_exp2f(g * -1.44269504089f)); }
#define MFMA(a, b, c) __builtin_amdgcn_mfma_f32_16x16x32_bf16(a, b, c, 0, 0, 0)

#define STAMP(k) do {} while (0)

__device__ __forceinline__ float xsum16_32(float v) {
  const auto a = __builtin_amdgcn_permlane16_swap(__float_as_uint(v), __float_as_uint(v), false, false);
  v = __uint_as_float(a[0]) + __uint_as_float(a[1]);
  const auto b = __builtin_amdgcn_permlane32_swap(__float_as_uint(v), __float_as_uint(v), false, false);
  return __uint_as_float(b[0]) + __uint_as_float(b[1]);
}
__device__ __forceinline__ float xmax16_32(float v) {
  const auto a = __builtin_amdgcn_permlane16_swap(__float_as_uint(v), __float_as_uint(v), false, false);
  v = fmaxf(__uint_as_float(a[0]), __uint_as_float(a[1]));
  const auto b = __builtin_amdgcn_permlane32_swap(__float_as_uint(v), __float_as_uint(v), false, false);
  return fmaxf(__uint_as_float(b[0]), __uint_as_float(b[1]));
}
#define PF_STRIDE 128
#define K0_PF_ROWS 0
__global__ __launch_bounds__(512) void k0_ln(const u16* __restrict__ x, const u16* __restrict__ wqkv, const u16* __restrict__ wg, const u16* __restrict__ wu, unsigned char* __restrict__ ws) {
  __shared__ f32x2 red[8][16];
  const int tid = threadIdx.x, w = tid >> 6, l = tid & 63, m = l & 15, g = l >> 4;
  const int mf = blockIdx.x;
  STAMP(0);
  if (mf >= 128) {
    const __amdgpu_buffer_rsrc_t rq = __builtin_amdgcn_make_buffer_rsrc((void*)wqkv, 0, K0_PF_ROWS * 6144, 0x00020000);
    const __amdgpu_buffer_rsrc_t rg = __builtin_amdgcn_make_buffer_rsrc((void*)wg, 0, K0_PF_ROWS * 7168, 0x00020000);
    const __amdgpu_buffer_rsrc_t ru = __builtin_amdgcn_make_buffer_rsrc((void*)wu, 0, K0_PF_ROWS * 7168, 0x00020000);
    constexpr int PER = K0_PF_ROWS ? (K0_PF_ROWS * 7168 / PF_STRIDE + 65535) / 65536 : 1;
    const unsigned vo = ((mf - 128) * 512 + tid) * PF_STRIDE;
    unsigned acc = 0, v[3 * PER];
#pragma unroll
    for (int i = 0; i < PER; ++i) {
      v[3 * i] = __builtin_amdgcn_raw_buffer_load_b32(rg, vo + i * 65536 * PF_STRIDE, 0, 0);
      v[3 * i + 1] = __builtin_amdgcn_raw_buffer_load_b32(ru, vo + i * 65536 * PF_STRIDE, 0, 0);
      v[3 * i + 2] = __builtin_amdgcn_raw_buffer_load_b32(rq, vo + i * 65536 * PF_STRIDE, 0, 0);
    }
#pragma unroll
    for (int i = 0; i < 3 * PER; ++i) acc ^= v[i];
    asm volatile("" :: "v"(acc));
    STAMP(1); STAMP(2); STAMP(3);
    return;
  }
  if (tid < 32) ((u32x4*)(ws + WS_FLAGS))[mf * 32 + tid] = (u32x4){0u, 0u, 0u, 0u};
  const u16* row = x + (size_t)(mf * 16 + m) * D_;
  u32x4 v[4];
#pragma unroll
  for (int i = 0; i < 4; ++i) v[i] = *(const u32x4*)(row + w * 128 + g * 8 + i * 32);
  const float piv = __uint_as_float((unsigned)row[0] << 16);
  float f[32]; float s = 0.f, q = 0.f;
#pragma unroll
  for (int i = 0; i < 4; ++i)
#pragma unroll
    for (int j = 0; j < 4; ++j) {
      const float a = bf_lo(v[i][j]), b = bf_hi(v[i][j]); f[i * 8 + 2 * j] = a; f[i * 8 + 2 * j + 1] = b;
      const float da = a - piv, db = b - piv; s += da + db; q += da * da + db * db;
    }
  s = xsum16_32(s); q = xsum16_32(q);
  STAMP(1);
  if (g == 0) red[w][m] = (f32x2){s, q};
  __syncthreads();
  float st = 0.f, qt = 0.f;
#pragma unroll
  for (int i = 0; i < 8; ++i) { const f32x2 r = red[i][m]; st += r.x; qt += r.y; }
  const float md = st * (1.0f / D_), mean = piv + md, var = fmaxf(qt * (1.0f / D_) - md * md, 0.f);
  const float mb = rbf(mean);
  const float inv = 1.0f / rbf(sqrtf(rbf(rbf(var) + 1e-5f)));
  const __amdgpu_buffer_rsrc_t xr = __builtin_amdgcn_make_buffer_rsrc(ws + WS_XN, 0, S_ * D_ * 2, 0x00020000);
#pragma unroll
  for (int i = 0; i < 4; ++i) {
    u32x4 o;
#pragma unroll
    for (int j = 0; j < 4; ++j) o[j] = cvt_pk(rbf(f[i * 8 + 2 * j] - mb) * inv, rbf(f[i * 8 + 2 * j + 1] - mb) * inv);
    __builtin_amdgcn_raw_buffer_store_b128(o, xr, (mf * 32 + w * 4 + i) * 1024 + l * 16, 0, ST_AUX);
  }
  STAMP(2);
  STAMP(3);
}

constexpr int K1_ASLOT = 16384, K1_BSLOT = 20480, K1_BOFF = 4 * K1_ASLOT, K1_LDS = K1_BOFF + 4 * K1_BSLOT;

struct K1Ctx {
  LAS unsigned char* lds;
  unsigned aoff;
  unsigned vb[6];
  unsigned vst0, vst1;
  i32x4 s0, s1, s2;
  unsigned so0, so1;
  unsigned ldst;
};

template <int TYPE> __device__ __forceinline__ void k1_stage_b(const K1Ctx& c, int s, int slot, unsigned cnt, int lim) {
  const unsigned d = K1_BOFF + slot * K1_BSLOT + c.ldst;
  unsigned t0, t1;
  if (TYPE == 0) {
    const unsigned kq = (unsigned)s * (32u * 6144u) + c.so0, kg = (unsigned)s * (32u * 7168u) + c.so0;
    asm volatile(
      "s_cmp_ge_u32 %10, %11\n\ts_cbranch_scc1 .Lsk%=\n\t"
      "s_mov_b32 m0, %9\n\ts_add_u32 %0, %7, 0x800\n\tbuffer_load_dwordx4 %2, %4, %7 offen lds\n\t"
      "s_add_u32 m0, %9, 0x400\n\ts_add_u32 %1, %7, 0x1000\n\tbuffer_load_dwordx4 %2, %4, %0 offen lds\n\t"
      "s_add_u32 m0, %9, 0x800\n\ts_nop 0\n\tbuffer_load_dwordx4 %2, %4, %1 offen lds\n\t"
      "s_add_u32 m0, %9, 0xc00\n\ts_nop 0\n\tbuffer_load_dwordx4 %3, %5, %8 offen lds\n\t"
      "s_add_u32 m0, %9, 0x1000\n\ts_nop 0\n\tbuffer_load_dwordx4 %3, %6, %8 offen lds\n"
      ".Lsk%=:"
      : "=&s"(t0), "=&s"(t1)
      : "v"(c.vst0), "v"(c.vst1), "s"(c.s0), "s"(c.s1), "s"(c.s2), "s"(kq), "s"(kg), "s"(d), "s"(cnt), "i"(lim) : "memory", "scc");
  } else {
    const unsigned k0 = (unsigned)s * (32u * 7168u) + c.so0, k1 = (unsigned)s * (32u * 7168u) + c.so1;
    asm volatile(
      "s_cmp_ge_u32 %10, %11\n\ts_cbranch_scc1 .Lsk%=\n\t"
      "s_mov_b32 m0, %9\n\ts_add_u32 %0, %7, 0x80\n\tbuffer_load_dwordx4 %2, %4, %7 offen lds\n\t"
      "s_add_u32 m0, %9, 0x400\n\ts_nop 0\n\tbuffer_load_dwordx4 %2, %4, %0 offen lds\n\t"
      "s_add_u32 m0, %9, 0x800\n\ts_nop 0\n\tbuffer_load_dwordx4 %2, %5, %7 offen lds\n\t"
      "s_add_u32 m0, %9, 0xc00\n\ts_nop 0\n\tbuffer_load_dwordx4 %2, %5, %0 offen lds\n\t"
      "s_add_u32 m0, %9, 0x1000\n\ts_nop 0\n\tbuffer_load_dwordx4 %3, %6, %8 offen lds\n"
      ".Lsk%=:"
      : "=&s"(t0), "=&s"(t1)
      : "v"(c.vst0), "v"(c.vst1), "s"(c.s0), "s"(c.s1), "s"(c.s2), "s"(k0), "s"(k1), "s"(d), "s"(cnt), "i"(lim) : "memory", "scc");
  }
}
__device__ __forceinline__ void k1_stage_a(const K1Ctx& c, int s, int slot, unsigned cnt, int lim) {
  const unsigned d = slot * K1_ASLOT + c.ldst;
  const unsigned k = (unsigned)s * 1024u + c.so0;
  unsigned t0;
  asm volatile(
    "s_cmp_ge_u32 %5, %6\n\ts_cbranch_scc1 .Lsk%=\n\t"
    "s_mov_b32 m0, %4\n\ts_add_u32 %0, %3, 0x8000\n\tbuffer_load_dwordx4 %1, %2, %3 offen lds\n\t"
    "s_add_u32 m0, %4, 0x400\n\ts_nop 0\n\tbuffer_load_dwordx4 %1, %2, %0 offen lds\n\t"
    "s_add_u32 m0, %4, 0x800\n\ts_add_u32 %0, %3, 0x10000\n\tbuffer_load_dwordx4 %1, %2, %0 offen lds\n\t"
    "s_add_u32 m0, %4, 0xc00\n\ts_add_u32 %0, %3, 0x18000\n\tbuffer_load_dwordx4 %1, %2, %0 offen lds\n"
    ".Lsk%=:"
    : "=&s"(t0) : "v"(c.vst0), "s"(c.s0), "s"(k), "s"(d), "s"(cnt), "i"(lim) : "memory", "scc");
}
#define WAIT_V2(STEADY, TAIL, cnt, lim) do { asm volatile("s_cmp_lt_u32 %0, %1\n\ts_cbranch_scc1 .Lw%=\n\ts_waitcnt vmcnt(" #TAIL ")\n.Lw%=:\n\ts_waitcnt vmcnt(" #STEADY ")" :: "s"(cnt), "i"(lim) : "memory", "scc"); } while (0)
#define BAR_IF(cnt, lim) do { SCHED(); asm volatile("s_cmp_ge_u32 %0, %1\n\ts_cbranch_scc1 .Lb%=\n\ts_barrier\n.Lb%=:" :: "s"(cnt), "i"(lim) : "memory", "scc"); SCHED(); } while (0)
template <int TYPE> __device__ __forceinline__ void k1_read(const K1Ctx& c, int slot, bf16x8 (&af)[4], bf16x8 (&bfr)[10]) {
  LAS unsigned char* lds = c.lds;
  const int bs = slot * K1_BSLOT;
  if (TYPE == 0) {
#pragma unroll
    for (int j = 0; j < 8; ++j) { LAS unsigned char* p = lds + c.vb[j & 1] + bs + (j >> 1) * 1024; bfr[j] = lds_tr8(p, p + 512); }
#pragma unroll
    for (int j = 0; j < 2; ++j) { LAS unsigned char* p = lds + c.vb[2 + j] + bs; bfr[8 + j] = lds_tr8(p, p + 512); }
  } else {
#pragma unroll
    for (int j = 0; j < 8; ++j) { LAS unsigned char* p = lds + c.vb[j & 3] + bs + (j >> 2) * 2048; bfr[j] = lds_tr8(p, p + 512); }
#pragma unroll
    for (int j = 0; j < 2; ++j) { LAS unsigned char* p = lds + c.vb[4 + j] + bs; bfr[8 + j] = lds_tr8(p, p + 256); }
  }
#pragma unroll
  for (int i = 0; i < 4; ++i) af[i] = *(LAS bf16x8*)(lds + c.aoff + slot * K1_ASLOT + i * 1024);
}
#define K1_EARLY 0
template <int TYPE, int N0, int N1> __device__ __forceinline__ void k1_mma(f32x4 (&acc)[4][10], const bf16x8 (&af)[4], const bf16x8 (&bfr)[10]) {
#pragma unroll
  for (int n = N0; n < N1; ++n) {
    const int j = n >> 2, i = n & 3;
    if (TYPE == 0 && (j == 4 || j == 5)) acc[i][j] = MFMA(af[i], bfr[j], acc[i][j]);
    else acc[i][j] = MFMA(bfr[j], af[i], acc[i][j]);
  }
}
__device__ __forceinline__ void k1_pf(unsigned voff, i32x4 srd, unsigned soff, unsigned on, unsigned cnt, int lim) {
  asm volatile("s_cmp_eq_u32 %3, 0\n\ts_cbranch_scc1 .Lpf%=\n\ts_cmp_ge_u32 %4, %5\n\ts_cbranch_scc1 .Lpf%=\n\t"
               "s_mov_b32 m0, %6\n\ts_nop 0\n\tbuffer_load_dword %0, %1, %2 offen lds\n.Lpf%=:"
               ::"v"(voff), "s"(srd), "s"(soff), "s"(on), "s"(cnt), "i"(lim), "i"(K1_LDS) : "memory", "scc");
}
#define WAIT_A(PFN, PFT, PLN, PLT, pf, cnt, lim) do { asm volatile( \
    "s_cmp_lt_u32 %1, %2\n\ts_cbranch_scc1 .Ls%=\n\t" \
    "s_cmp_lg_u32 %0, 0\n\ts_cbranch_scc1 .Lt%=\n\ts_waitcnt vmcnt(" #PLT ")\n.Lt%=:\n\ts_waitcnt vmcnt(" #PFT ")\n" \
    ".Ls%=:\n\ts_cmp_lg_u32 %0, 0\n\ts_cbranch_scc1 .Lu%=\n\ts_waitcnt vmcnt(" #PLN ")\n.Lu%=:\n\ts_waitcnt vmcnt(" #PFN ")" \
    :: "s"(pf), "s"(cnt), "i"(lim) : "memory", "scc"); } while (0)

template <int TYPE> __device__ __forceinline__ void k1_body(LAS unsigned char* lds, const u16* wqkv, const u16* wg, const u16* wu, const u16* wo, const u16* wd, unsigned char* ws, int pm, int pa  ) {
  const int tid = threadIdx.x;
  const int w = __builtin_amdgcn_readfirstlane(tid >> 6), l = tid & 63;
  const int grp = w >> 2, wr = w & 3;
  const int g = l >> 4, q = (l & 15) >> 2, p = l & 3;
  STAMP(0);
  K1Ctx c; c.lds = lds;
  c.aoff = l * 16 + wr * 4096;
  {
    const unsigned s2 = ((q >> 1) & 1) | ((g & 1) << 1);
    const unsigned lanep = K1_BOFF + g * 5120 + q * 128 + (p >> 1) * 16 + (p & 1) * 8;
    if (TYPE == 0) {
      c.vb[0] = lanep + (((2 * grp) ^ s2) << 5); c.vb[1] = lanep + (((2 * grp + 1) ^ s2) << 5);
      c.vb[2] = c.vb[0] + 4096; c.vb[3] = c.vb[1] + 4096; c.vb[4] = 0; c.vb[5] = 0;
    } else {
#pragma unroll
      for (int j = 0; j < 4; ++j) c.vb[j] = lanep + ((j ^ s2) << 5) + grp * 1024;
      const unsigned mx = K1_BOFF + (2 * (g >> 1) * 5 + 4) * 1024 + (g & 1) * 512 + q * 64 + (((2 * grp + (p >> 1)) ^ ((g & 1) << 1)) << 4) + (p & 1) * 8;
      c.vb[4] = mx; c.vb[5] = mx + 5120;
    }
  }
  const int c0 = 1024 + 160 * pa;
  const int odd = pa & 1;
  if (grp == 0) {
    const unsigned r8 = l >> 3, sw = (((r8 >> 1) & 1) | ((wr & 1) << 1)) << 1, ch = (l & 7) ^ sw;
    c.ldst = wr * 5120;
    if (TYPE == 0) {
      c.vst0 = (8 * wr + r8) * 6144 + ch * 16; c.vst1 = (8 * wr + r8) * 7168 + ch * 16;
      c.s0 = make_srd(wqkv, D_ * 3 * D_ * 2); c.s1 = make_srd(wg, D_ * F_ * 2); c.s2 = make_srd(wu, D_ * F_ * 2);
      c.so0 = pa * 128; c.so1 = 0;
    } else {
      c.vst0 = (8 * wr + r8) * 7168 + ch * 16;
      const unsigned r16 = l >> 2, chm = (l & 3) ^ (((r16 >> 3) & 1) << 1);
      c.vst1 = (16 * (wr >> 1) + r16) * 7168 + chm * 16;
      c.s0 = make_srd(wg, D_ * F_ * 2); c.s1 = make_srd(wu, D_ * F_ * 2); c.s2 = make_srd((wr & 1) ? wu : wg, D_ * F_ * 2);
      c.so0 = (c0 + (odd ? 32 : 0)) * 2; c.so1 = (c0 + (odd ? 0 : 128)) * 2;
    }
  } else {
    c.vst0 = l * 16; c.vst1 = 0;
    c.s0 = make_srd(ws + WS_XN, S_ * D_ * 2); c.s1 = c.s0; c.s2 = c.s0;
    c.so0 = (pm * 16 + 4 * wr) * 32768; c.so1 = 0;
    c.ldst = wr * 4096;
  }

  f32x4 acc[4][10];
#pragma unroll
  for (int i = 0; i < 4; ++i)
#pragma unroll
    for (int j = 0; j < 10; ++j) acc[i][j] = (f32x4){0.f, 0.f, 0.f, 0.f};
  bf16x8 af[4], bfr[10];

  if (grp == 0) {
    k1_stage_b<TYPE>(c, 0, 0, 0, 1); k1_stage_b<TYPE>(c, 1, 1, 0, 1); k1_stage_b<TYPE>(c, 2, 2, 0, 1);
    WAIT_V(10); BAR();
    STAMP(1);
#define K1_G0_STEP(J, TAILV) do { \
      k1_read<TYPE>(c, J, af, bfr); SCHED(); \
      k1_stage_b<TYPE>(c, s + (J) + 3, ((J) + 3) & 3, s, (J) == 0 ? 32 : 28); \
      WAIT_L(0); BAR(); \
      __builtin_amdgcn_s_setprio(1); \
      k1_mma<TYPE, 0, 40 - K1_EARLY>(acc, af, bfr); \
      SCHED(); WAIT_V2(10, TAILV, s, 28); \
      BAR(); \
      if (K1_EARLY) __builtin_amdgcn_s_setprio(3); \
      k1_mma<TYPE, 40 - K1_EARLY, 40>(acc, af, bfr); \
      __builtin_amdgcn_s_setprio(0); SCHED(); } while (0)
#pragma nounroll
    for (unsigned s = 0; s < 32; s += 4) {
      K1_G0_STEP(0, 10); K1_G0_STEP(1, 5); K1_G0_STEP(2, 0); K1_G0_STEP(3, 0);
    }
  } else {
#define PF_ON 1
#define K1_PF_ROW0 (K0_PF_ROWS > 96 ? K0_PF_ROWS : 96)
    const unsigned pf = __builtin_amdgcn_readfirstlane((w == 4 && PF_ON) ? 1 : 0), pfv = (blockIdx.x * 64 + l) * PF_STRIDE;
    const i32x4 sg = make_srd(wg, D_ * F_ * 2), su = make_srd(wu, D_ * F_ * 2), sq = make_srd(wqkv, D_ * 3 * D_ * 2), sd = make_srd(wd, F_ * D_ * 2), so = make_srd(wo, D_ * D_ * 2);
    constexpr unsigned OOR = 0x7f000000u, MB = 16384u * PF_STRIDE;
    constexpr unsigned NG = ((1024 - K1_PF_ROW0) * 7168u + MB - 1) / MB, NQ = ((1024 - K1_PF_ROW0) * 6144u + MB - 1) / MB, ND = (F_ * D_ * 2 + MB - 1) / MB, NO = (D_ * D_ * 2 + MB - 1) / MB;
    static_assert(NG + (ND + 1) / 2 <= 8 && NG + ND / 2 <= 7 && NQ + NO <= 7, "prefetch schedule does not fit");
    k1_pf(pfv, sq, OOR, pf, 0, 1); k1_stage_a(c, 0, 0, 0, 1);
    k1_pf(pfv, sq, OOR, pf, 0, 1); k1_stage_a(c, 1, 1, 0, 1);
    k1_stage_a(c, 2, 2, 0, 1);
    WAIT_A(9, 9, 8, 8, pf, 0u, 1); BAR();
    BAR();
#define K1_G1_STEP(J, PFN, PFT, PLT) do { \
      k1_read<TYPE>(c, J, af, bfr); SCHED(); \
      if ((J) == 0) k1_pf(pfv, it < NG ? sg : sd, it < NG ? K1_PF_ROW0 * 7168u + it * MB : (it - NG) * MB, pf, s, 32); \
      if ((J) == 1) k1_pf(pfv, it < NG ? su : sd, it < NG ? K1_PF_ROW0 * 7168u + it * MB : (it - NG + (ND + 1) / 2) * MB, pf, s, 28); \
      if ((J) == 2) k1_pf(pfv, it < NQ ? sq : so, it < NQ ? K1_PF_ROW0 * 6144u + it * MB : (it - NQ) * MB, pf, s, 28); \
      k1_stage_a(c, s + (J) + 3, ((J) + 3) & 3, s, (J) == 0 ? 32 : 28); \
      WAIT_L(0); WAIT_A(PFN, PFT, 8, PLT, pf, s, 28); \
      BAR(); \
      __builtin_amdgcn_s_setprio(1); \
      k1_mma<TYPE, 0, 40 - K1_EARLY>(acc, af, bfr); \
      if ((J) < 3) BAR(); else BAR_IF(s, 28); \
      if (K1_EARLY) __builtin_amdgcn_s_setprio(3); \
      k1_mma<TYPE, 40 - K1_EARLY, 40>(acc, af, bfr); \
      __builtin_amdgcn_s_setprio(0); SCHED(); } while (0)
#pragma nounroll
    for (unsigned s = 0; s < 32; s += 4) {
      const unsigned it = s >> 2;
      K1_G1_STEP(0, 9, 9, 8); K1_G1_STEP(1, 10, 5, 4); K1_G1_STEP(2, 10, 0, 0); K1_G1_STEP(3, 9, 0, 0);
    }
  }
  STAMP(2);

  const __amdgpu_buffer_rsrc_t a2 = __builtin_amdgcn_make_buffer_rsrc(ws + WS_A2, 0, 128 * KS2 * 1024, 0x00020000);
  const unsigned mf0 = pm * 16 + 4 * wr;
  if (TYPE == 1) {
    const unsigned ksf = c0 / 32 + (odd ? 1 : 0) + 2 * grp, ksm = c0 / 32 + (odd ? 0 : 4);
#pragma unroll
    for (int i = 0; i < 4; ++i) {
      const unsigned base = (mf0 + i) * (KS2 * 1024);
#pragma unroll
      for (int t = 0; t < 2; ++t) {
        f32x4 h0, h1;
#pragma unroll
        for (int r = 0; r < 4; ++r) { h0[r] = silu_f(acc[i][2 * t][r]) * acc[i][2 * t + 4][r]; h1[r] = silu_f(acc[i][2 * t + 1][r]) * acc[i][2 * t + 5][r]; }
        __builtin_amdgcn_raw_buffer_store_b128(__builtin_bit_cast(u32x4, pack8(h0, h1)), a2, l * 16 + base + (ksf + t) * 1024, 0, ST_AUX);
      }
      f32x4 h0;
#pragma unroll
      for (int r = 0; r < 4; ++r) h0[r] = silu_f(acc[i][8][r]) * acc[i][9][r];
      u32x2 o; o.x = cvt_pk(h0[0], h0[1]); o.y = cvt_pk(h0[2], h0[3]);
      __builtin_amdgcn_raw_buffer_store_b64(o, a2, l * 16 + grp * 8 + base + ksm * 1024, 0, ST_AUX);
    }
  } else {
    const int head = 2 * pa + grp;
    bf16x8 kop[5]; u32x2 vp[5][2];
#pragma unroll
    for (int i = 0; i < 4; ++i) {
      kop[i + 1] = pack8(acc[i][2], acc[i][3]);
#pragma unroll
      for (int d = 0; d < 2; ++d) { vp[i + 1][d].x = cvt_pk(acc[i][4 + d][0], acc[i][4 + d][1]); vp[i + 1][d].y = cvt_pk(acc[i][4 + d][2], acc[i][4 + d][3]); }
    }
    const u32x4 klast = __builtin_bit_cast(u32x4, kop[4]);
    const u32x4 vlast = (u32x4){vp[4][0].x, vp[4][0].y, vp[4][1].x, vp[4][1].y};
    const __amdgpu_buffer_rsrc_t hr = __builtin_amdgcn_make_buffer_rsrc(ws + WS_HALO, 0, 8 * 32 * 2048, 0x00020000);
    GAS unsigned* flags = (GAS unsigned*)(ws + WS_FLAGS);
    const bool send = (wr == 3) && (pm < 7);
    if (send) {
      const unsigned ho = (pm * 32 + head) * 2048;
      __builtin_amdgcn_raw_buffer_store_b128(klast, hr, l * 16 + ho, 0, 16);
      __builtin_amdgcn_raw_buffer_store_b128(vlast, hr, l * 16 + ho + 1024, 0, 16);
    }
    if (wr != 3) {
      *(LAS u32x4*)(lds + w * 2048 + l * 16) = klast;
      *(LAS u32x4*)(lds + w * 2048 + 1024 + l * 16) = vlast;
    }
    bf16x8 qop[4];
#pragma unroll
    for (int i = 0; i < 4; ++i) qop[i] = pack8(acc[i][0], acc[i][1]);
#pragma unroll
    for (int i = 0; i < 4; ++i) {
      f32x4 h0, h1;
#pragma unroll
      for (int r = 0; r < 4; ++r) { h0[r] = silu_f(acc[i][6][r]) * acc[i][8][r]; h1[r] = silu_f(acc[i][7][r]) * acc[i][9][r]; }
      __builtin_amdgcn_raw_buffer_store_b128(__builtin_bit_cast(u32x4, pack8(h0, h1)), a2, l * 16 + (mf0 + i) * (KS2 * 1024) + (2 * pa + grp) * 1024, 0, ST_AUX);
    }
    if (send) {
      WAIT_V(4);
      if (l == 0) __hip_atomic_store(flags + (pm * 32 + head) * 16, 1u, __ATOMIC_RELAXED, __HIP_MEMORY_SCOPE_AGENT);
    }
    WAIT_L(0); BAR();
    const int m = l & 15; const bool par = m & 1;
    const bool own0 = 4 * g + (m & 1) <= m, own1 = 4 * g + (m & 1) + 2 <= m;
    const unsigned hmask = par ? 0xffff0000u : 0x0000ffffu;
    const float scale = 0.17677669529663687f * 1.44269504089f;
    auto attend = [&](int i, bool have_prev) {
      const f32x4 z = (f32x4){0.f, 0.f, 0.f, 0.f};
      const f32x4 s0 = MFMA(kop[i + 1], qop[i], z);
      const f32x4 s1 = MFMA(kop[i], qop[i], z);
      float l0 = own0 ? (par ? s0[1] : s0[0]) : (par ? s1[1] : s1[0]);
      float l1 = own1 ? (par ? s0[3] : s0[2]) : (par ? s1[3] : s1[2]);
      l0 *= scale; l1 *= scale;
      if (!have_prev) { l0 = own0 ? l0 : -3.0e38f; l1 = own1 ? l1 : -3.0e38f; }
      const float mx = xmax16_32(fmaxf(l0, l1));
      const float e0 = __builtin_amdgcn_exp2f(l0 - mx), e1 = __builtin_amdgcn_exp2f(l1 - mx);
      const float inv = __builtin_amdgcn_rcpf(xsum16_32(e0 + e1));
      const unsigned w0 = cvt_pk(e0 * inv, e0 * inv) & hmask, w1 = cvt_pk(e1 * inv, e1 * inv) & hmask;
      const u32x4 pw = (u32x4){own0 ? 0u : w0, own1 ? 0u : w1, own0 ? w0 : 0u, own1 ? w1 : 0u};
      const bf16x8 pop = __builtin_bit_cast(bf16x8, pw);
      f32x4 o[2];
#pragma unroll
      for (int d = 0; d < 2; ++d) {
        const u32x4 vv = (u32x4){vp[i][d].x, vp[i][d].y, vp[i + 1][d].x, vp[i + 1][d].y};
        o[d] = MFMA(__builtin_bit_cast(bf16x8, vv), pop, z);
      }
      __builtin_amdgcn_raw_buffer_store_b128(__builtin_bit_cast(u32x4, pack8(o[0], o[1])), a2, l * 16 + (mf0 + i) * (KS2 * 1024) + (112 + head) * 1024, 0, ST_AUX);
    };
    attend(1, true); attend(2, true); attend(3, true);
    bool have_prev = true;
    u32x4 kh, vh;
    if (wr == 0) {
      if (pm > 0) {
        GAS unsigned* fl = flags + ((pm - 1) * 32 + head) * 16;
        for (unsigned spins = 0; spins < (1u << 18); ++spins) {
          if (__builtin_amdgcn_readfirstlane(__hip_atomic_load(fl, __ATOMIC_RELAXED, __HIP_MEMORY_SCOPE_AGENT)) != 0u) break;
          __builtin_amdgcn_s_sleep(1);
        }
        asm volatile("" ::: "memory");
        const unsigned ho = ((pm - 1) * 32 + head) * 2048;
        kh = __builtin_amdgcn_raw_buffer_load_b128(hr, l * 16 + ho, 0, 16);
        vh = __builtin_amdgcn_raw_buffer_load_b128(hr, l * 16 + ho + 1024, 0, 16);
      } else { kh = (u32x4){0u, 0u, 0u, 0u}; vh = kh; have_prev = false; }
    } else {
      kh = *(LAS u32x4*)(lds + (w - 1) * 2048 + l * 16);
      vh = *(LAS u32x4*)(lds + (w - 1) * 2048 + 1024 + l * 16);
    }
    kop[0] = __builtin_bit_cast(bf16x8, kh);
    vp[0][0] = (u32x2){vh.x, vh.y}; vp[0][1] = (u32x2){vh.z, vh.w};
    attend(0, have_prev);
  }
  STAMP(3);
}

__global__ __launch_bounds__(512, 2) void k1_gemm(const u16* __restrict__ wqkv, const u16* __restrict__ wg, const u16* __restrict__ wu, const u16* __restrict__ wo, const u16* __restrict__ wd, unsigned char* __restrict__ ws) {
  extern __shared__ __attribute__((aligned(16))) unsigned char shm[];
  const int b = blockIdx.x, xcd = b & 7, idx = b >> 3;
  const int pm = idx & 7, pn = idx >> 3;
#define PA_SHIFT 0
  const int pa = (xcd * 2 + (pn >> 1) + PA_SHIFT) & 15;
  if (pn & 1) k1_body<1>((LAS unsigned char*)shm, wqkv, wg, wu, wo, wd, ws, pm, pa);
  else k1_body<0>((LAS unsigned char*)shm, wqkv, wg, wu, wo, wd, ws, pm, pa);
}

constexpr int K2_SLOT = 16384, K2_LDS = K2_SLOT * 8;
constexpr int K2_TP = P_STEPS, K2_TQ = KS2 - P_STEPS;
static_assert(K2_TP % 8 == 0 && K2_TQ % 8 == 0 && K2_TP >= 48, "step counts must be multiples of 8");

struct K2Frag { bf16x8 a[4], b[4]; };
struct K2Rd { LAS unsigned char* lds; unsigned aoff[2], vb[2][4]; };
template <int SLOT> __device__ __forceinline__ void k2_read(const K2Rd& c, K2Frag& f) {
  constexpr int H = SLOT >> 2, O = (SLOT & 3) * K2_SLOT;
#pragma unroll
  for (int j = 0; j < 4; ++j) { LAS unsigned char* p = c.lds + c.vb[H][j] + O; f.b[j] = lds_tr8(p, p + 4096); }
#pragma unroll
  for (int i = 0; i < 4; ++i) f.a[i] = *(LAS bf16x8*)(c.lds + c.aoff[H] + O + i * 1024);
}
template <int SLOTN> __device__ __forceinline__ void k2_step(const K2Rd& c, f32x4 (&acc)[4][4], const K2Frag& cur, K2Frag& nxt) {
  k2_read<SLOTN>(c, nxt);
#pragma unroll
  for (int j = 0; j < 4; ++j)
#pragma unroll
    for (int i = 0; i < 4; ++i) acc[i][j] = MFMA(cur.b[j], cur.a[i], acc[i][j]);
#pragma unroll
  for (int k = 0; k < 6; ++k) { __builtin_amdgcn_sched_group_barrier(0x8, 1, 0); __builtin_amdgcn_sched_group_barrier(0x100, 2, 0); }
  __builtin_amdgcn_sched_group_barrier(0x8, 10, 0);
  SCHED(); __builtin_amdgcn_s_waitcnt(0xC07F); asm volatile("" ::: "memory"); BAR();
}
__device__ __forceinline__ void k2_consume(const K2Rd& c, f32x4 (&acc)[4][4], K2Frag& f0, K2Frag& f1, int iters) {
#pragma nounroll
  for (int it = 0; it < iters; ++it) {
    k2_step<1>(c, acc, f0, f1); k2_step<2>(c, acc, f1, f0); k2_step<3>(c, acc, f0, f1); k2_step<4>(c, acc, f1, f0);
    k2_step<5>(c, acc, f0, f1); k2_step<6>(c, acc, f1, f0); k2_step<7>(c, acc, f0, f1); k2_step<0>(c, acc, f1, f0);
  }
}

struct K2Ld { unsigned vsta, vstb; i32x4 sa; unsigned ldst; };
__device__ __forceinline__ void k2_stage(const K2Ld& c, i32x4 sb, unsigned ka, unsigned kb, int slot, unsigned cnt, unsigned lim) {
  const unsigned d = slot * K2_SLOT + c.ldst;
  unsigned t0;
  asm volatile(
    "s_cmp_ge_u32 %8, %9\n\ts_cbranch_scc1 .Lsk%=\n\t"
    "s_mov_b32 m0, %7\n\ts_add_u32 %0, %5, 0x24000\n\tbuffer_load_dwordx4 %1, %3, %5 offen lds\n\t"
    "s_add_u32 m0, %7, 0x400\n\ts_nop 0\n\tbuffer_load_dwordx4 %1, %3, %0 offen lds\n\t"
    "s_add_u32 m0, %7, 0x2000\n\ts_add_u32 %0, %6, 0x80\n\tbuffer_load_dwordx4 %2, %4, %6 offen lds\n\t"
    "s_add_u32 m0, %7, 0x2400\n\ts_nop 0\n\tbuffer_load_dwordx4 %2, %4, %0 offen lds\n"
    ".Lsk%=:"
    : "=&s"(t0) : "v"(c.vsta), "v"(c.vstb), "s"(c.sa), "s"(sb), "s"(ka), "s"(kb), "s"(d), "s"(cnt), "s"(lim) : "memory", "scc");
}
__device__ __forceinline__ void k2_load(const K2Ld& c, i32x4 sb, unsigned ka, unsigned kb, int iters, unsigned stop) {
#pragma nounroll
  for (int it = 0; it < iters; ++it) {
#pragma unroll
    for (int j = 0; j < 8; ++j) {
      k2_stage(c, sb, ka, kb, j, (unsigned)(it * 8 + j), stop);
      ka += 1024u; kb += 65536u;
      WAIT_V(24); BAR();
    }
  }
}
template <int AUX> __device__ __forceinline__ void glds16x(unsigned voff, i32x4 srd, unsigned soff, unsigned ldsaddr) {
  if (AUX == 16) asm volatile("s_mov_b32 m0, %3\n\ts_nop 0\n\tbuffer_load_dwordx4 %0, %1, %2 offen sc1 lds" ::"v"(voff), "s"(srd), "s"(soff), "s"(ldsaddr) : "memory");
  else asm volatile("s_mov_b32 m0, %3\n\ts_nop 0\n\tbuffer_load_dwordx4 %0, %1, %2 offen lds" ::"v"(voff), "s"(srd), "s"(soff), "s"(ldsaddr) : "memory");
}

template <bool ISP> __device__ __forceinline__ void k2_body(LAS unsigned char* lds, const u16* x, const u16* wo, const u16* wd, u16* out, unsigned char* ws, int rt, int ct) {
  constexpr int NT = ISP ? K2_TP : K2_TQ;
  const int tid = threadIdx.x;
  const int w = __builtin_amdgcn_readfirstlane(tid >> 6), l = tid & 63;
  const int w4 = w & 3, wm = w4 >> 1, wn = w4 & 1;
  const int g = l >> 4, q = (l & 15) >> 2, p = l & 3;
  const int tile = rt * 8 + ct;
  GAS unsigned* cnt = (GAS unsigned*)(ws + WS_FLAGS + 32768) + tile * 16;
  STAMP(0);
  if (w >= 4) {
    K2Ld c;
    const unsigned r8 = l >> 3, ch = (l & 7) ^ (((r8 >> 1) & 3) << 1);
    c.vsta = l * 16; c.vstb = (8 * w4 + r8) * 2048 + ch * 16;
    c.sa = make_srd(ws + WS_A2, 128 * KS2 * 1024);
    const i32x4 swd = make_srd(wd, F_ * D_ * 2), swo = make_srd(wo, D_ * D_ * 2);
    const unsigned soa = (rt * 8 + 2 * w4) * (KS2 * 1024), sob = ct * 256;
    c.ldst = w4 * 2048;
    constexpr unsigned H0 = K2_TP - 32;
#pragma unroll
    for (int t = 0; t < 8; ++t) {
      if (ISP) k2_stage(c, swo, (112 + t) * 1024u + soa, t * 65536u + sob, t, 0, 1);
      else k2_stage(c, swd, (H0 + t) * 1024u + soa, (H0 + t) * 65536u + sob, t, 0, 1);
    }
    WAIT_V(24); BAR();
    BAR();
    if (ISP) { k2_load(c, swo, (112 + 8) * 1024u + soa, 8 * 65536u + sob, 3, 0x7fffffffu); k2_load(c, swd, soa, sob, (NT - 32) / 8, 0x7fffffffu); }
    else k2_load(c, swd, (H0 + 8) * 1024u + soa, (H0 + 8) * 65536u + sob, (NT - 8) / 8, 0x7fffffffu);
    const i32x4 sx = make_srd(x, S_ * D_ * 2), sp = make_srd(ws + WS_PART, 128 * 65536);
    if (ISP) {
      const unsigned xv = ((rt * 128 + wm * 64 + (l & 15)) * D_ + ct * 128 + wn * 64 + (((g & 1) << 1) | (g >> 1)) * 8) * 2;
#pragma unroll
      for (int k = 0; k < 2; ++k) {
#pragma unroll
        for (int e = 0; e < 4; ++e) { const int blk = k * 4 + e; glds16x<0>(xv, sx, (blk >> 1) * 16 * D_ * 2 + (blk & 1) * 64, k * 16384 + w4 * 4096 + e * 1024); }
        WAIT_V(24); BAR();
      }
      WAIT_V(20); BAR(); WAIT_V(16); BAR();  WAIT_V(12); BAR();
      WAIT_V(0);
      if (w4 == 0) {
        for (unsigned spins = 0; spins < (1u << 18); ++spins) {
          if ((unsigned)__builtin_amdgcn_readfirstlane(__hip_atomic_load(cnt, __ATOMIC_RELAXED, __HIP_MEMORY_SCOPE_AGENT)) >= 4u) break;
          __builtin_amdgcn_s_sleep(1);
        }
      }
      asm volatile("" ::: "memory"); BAR(); asm volatile("" ::: "memory");
#pragma unroll
      for (int e = 0; e < 16; ++e) glds16x<16>(l * 16, sp, tile * 65536 + w4 * 16384 + e * 1024, 32768 + w4 * 16384 + e * 1024);
      BAR();
      WAIT_V(0); BAR();
    } else {
      WAIT_V(20); BAR(); WAIT_V(16); BAR(); WAIT_V(12); BAR(); WAIT_V(8); BAR(); WAIT_V(4); BAR(); WAIT_V(0); BAR(); BAR(); BAR();
    }
    return;
  }
  K2Rd c; c.lds = lds;
  {
    const unsigned row = 4 * (g & 1) + q, s2 = (row >> 1) & 3;
    const unsigned lanep = 8192 + (g >> 1) * 2048 + wn * 1024 + row * 128 + (p >> 1) * 16 + (p & 1) * 8;
#pragma unroll
    for (int h = 0; h < 2; ++h) {
      c.aoff[h] = l * 16 + wm * 4096 + h * 65536;
#pragma unroll
      for (int j = 0; j < 4; ++j) c.vb[h][j] = lanep + ((j ^ s2) << 5) + h * 65536;
    }
  }
  f32x4 accf[4][4], accs[4][4];
#pragma unroll
  for (int i = 0; i < 4; ++i)
#pragma unroll
    for (int j = 0; j < 4; ++j) { accf[i][j] = (f32x4){0.f, 0.f, 0.f, 0.f}; accs[i][j] = accf[i][j]; }
  K2Frag f0, f1;
  BAR();
  STAMP(1);
  k2_read<0>(c, f0);
  WAIT_L(0); BAR();
  if (ISP) { k2_consume(c, accs, f0, f1, 4); k2_consume(c, accf, f0, f1, (NT - 32) / 8); }
  else k2_consume(c, accf, f0, f1, NT / 8);
  STAMP(2);
  if (!ISP) {
    const __amdgpu_buffer_rsrc_t pr = __builtin_amdgcn_make_buffer_rsrc(ws + WS_PART, 0, 128 * 65536, 0x00020000);
    const unsigned po = tile * 65536 + w4 * 16384 + l * 16;
#pragma unroll
    for (int i = 0; i < 4; ++i)
#pragma unroll
      for (int j = 0; j < 4; ++j) __builtin_amdgcn_raw_buffer_store_b128(__builtin_bit_cast(u32x4, accf[i][j]), pr, po + (i * 4 + j) * 1024, 0, 16);
    WAIT_V(0);
    if (l == 0) __hip_atomic_fetch_add(cnt, 1u, __ATOMIC_RELAXED, __HIP_MEMORY_SCOPE_AGENT);
  } else {
    const int m = l & 15;
    u16* orow = out + (size_t)(rt * 128 + wm * 64 + m) * D_ + ct * 128 + wn * 64 + (((g & 1) << 1) | (g >> 1)) * 8;
#pragma unroll
    for (int i = 0; i < 4; ++i)
#pragma unroll
      for (int jp = 0; jp < 2; ++jp) {
        unsigned y[2][2];
#pragma unroll
        for (int jj = 0; jj < 2; ++jj) {
          const int j = 2 * jp + jj;
          const f32x4 f = accf[i][j] + *(LAS f32x4*)(lds + 32768 + w4 * 16384 + (i * 4 + j) * 1024 + l * 16);
          float v[4];
#pragma unroll
          for (int r = 0; r < 4; ++r) v[r] = accs[i][j][r] * silu_f(f[r]);
          y[jj][0] = cvt_pk(v[0], v[1]); y[jj][1] = cvt_pk(v[2], v[3]);
        }
        const auto s0 = __builtin_amdgcn_permlane16_swap(y[0][0], y[1][0], false, false);
        const auto s1 = __builtin_amdgcn_permlane16_swap(y[0][1], y[1][1], false, false);
        const u32x4 xr = *(LAS u32x4*)(lds + ((i * 2 + jp) >> 2) * 16384 + w4 * 4096 + ((i * 2 + jp) & 3) * 1024 + l * 16);
        const unsigned yy[4] = {s0[0], s1[0], s0[1], s1[1]};
        u32x4 o;
#pragma unroll
        for (int r = 0; r < 4; ++r) o[r] = cvt_pk(bf_lo(xr[r]) + bf_lo(yy[r]), bf_hi(xr[r]) + bf_hi(yy[r]));
        *(u32x4*)(orow + (size_t)i * 16 * D_ + jp * 32) = o;
      }
  }
  STAMP(3);
}

__global__ __launch_bounds__(512, 2) void k2_gemm(const u16* __restrict__ x, const u16* __restrict__ wo, const u16* __restrict__ wd, u16* __restrict__ out, unsigned char* __restrict__ ws) {
  extern __shared__ __attribute__((aligned(16))) unsigned char shm[];
  const int b = blockIdx.x, xcd = b & 7, idx = b >> 3;
  const int pr = xcd & 3;
  const int rt = (pr & 1) * 8 + (idx & 7), ct = (pr >> 1) * 4 + (idx >> 3);
  if (xcd < 4) k2_body<true>((LAS unsigned char*)shm, x, wo, wd, out, ws, rt, ct);
  else k2_body<false>((LAS unsigned char*)shm, x, wo, wd, out, ws, rt, ct);
}

extern "C" void kernel_launch(void* const* d_in, const int* in_sizes, int n_in, void* d_out, int out_size, void* d_ws, size_t ws_size, hipStream_t stream) {
  const u16* x = (const u16*)d_in[0]; const u16* wqkv = (const u16*)d_in[1]; const u16* wo = (const u16*)d_in[2];
  const u16* wg = (const u16*)d_in[4]; const u16* wu = (const u16*)d_in[5]; const u16* wd = (const u16*)d_in[6];
  unsigned char* ws = (unsigned char*)d_ws;
  (void)hipFuncSetAttribute((const void*)k1_gemm, hipFuncAttributeMaxDynamicSharedMemorySize, K1_LDS + 256);
  (void)hipFuncSetAttribute((const void*)k2_gemm, hipFuncAttributeMaxDynamicSharedMemorySize, K2_LDS);
  k0_ln<<<K0_PF_ROWS ? 256 : 128, 512, 0, stream>>>(x, wqkv, wg, wu, ws);
  k1_gemm<<<256, 512, K1_LDS + 256, stream>>>(wqkv, wg, wu, wo, wd, ws);
  k2_gemm<<<256, 512, K2_LDS, stream>>>(x, wo, wd, (u16*)d_out, ws);
}
```

```cpp
#include <hip/hip_runtime.h>
#include <stdint.h>

#define LAS __attribute__((address_space(3)))
#define GAS __attribute__((address_space(1)))
typedef __bf16 bf16x8 __attribute__((ext_vector_type(8)));
typedef short s16x4 __attribute__((ext_vector_type(4)));
typedef short s16x8 __attribute__((ext_vector_type(8)));
typedef float f32x4 __attribute__((ext_vector_type(4)));
typedef float f32x2 __attribute__((ext_vector_type(2)));
typedef unsigned u32x4 __attribute__((ext_vector_type(4)));
typedef unsigned u32x2 __attribute__((ext_vector_type(2)));
typedef int i32x4 __attribute__((ext_vector_type(4)));
typedef unsigned short u16;

constexpr int S_ = 2048, D_ = 1024, F_ = 3584;
constexpr int KS2 = 144;
constexpr size_t WS_GEN = 0;
constexpr size_t WS_LNF = 256;
constexpr size_t WS_T1F = 2048;
constexpr size_t WS_HLF = 4096;
constexpr size_t WS_PTF = 32768;
constexpr size_t WS_XN = 65536;
constexpr size_t WS_A2 = WS_XN + 4194304;
constexpr size_t WS_HALO = WS_A2 + (size_t)128 * KS2 * 1024;
constexpr size_t WS_PART = WS_HALO + 8 * 32 * 2048;
constexpr size_t WS_STAMP = WS_PART + (size_t)128 * 65536;

#define P_STEPS 80
#define ST_AUX 16

__device__ __forceinline__ i32x4 make_srd(const void* p, unsigned bytes) {
  const unsigned long long a = (unsigned long long)p;
  i32x4 r; r.x = (int)(unsigned)a; r.y = (int)((unsigned)(a >> 32) & 0xffffu); r.z = (int)bytes; r.w = 0x00020000; return r;
}
__device__ __forceinline__ void glds16(unsigned voff, i32x4 srd, unsigned soff, unsigned ldsaddr) {
  asm volatile("s_mov_b32 m0, %3\n\ts_nop 0\n\tbuffer_load_dwordx4 %0, %1, %2 offen lds" ::"v"(voff), "s"(srd), "s"(soff), "s"(ldsaddr) : "memory");
}
#define WAIT_V(n) asm volatile("s_waitcnt vmcnt(" #n ")" ::: "memory")
#define WAIT_L(n) asm volatile("s_waitcnt lgkmcnt(" #n ")" ::: "memory")
#define SCHED() __builtin_amdgcn_sched_barrier(0)
#define BAR() do { SCHED(); __builtin_amdgcn_s_barrier(); SCHED(); } while (0)

typedef __bf16 bf16x2 __attribute__((ext_vector_type(2)));
__device__ __forceinline__ unsigned cvt_pk(float lo, float hi) { const f32x2 v = {lo, hi}; return __builtin_bit_cast(unsigned, __builtin_convertvector(v, bf16x2)); }
__device__ __forceinline__ float bf_lo(unsigned u) { return __uint_as_float(u << 16); }
__device__ __forceinline__ float bf_hi(unsigned u) { return __uint_as_float(u & 0xffff0000u); }
__device__ __forceinline__ float rbf(float f) { return bf_lo(cvt_pk(f, f)); }
__device__ __forceinline__ bf16x8 pack8(const f32x4& a, const f32x4& b) {
  u32x4 w; w.x = cvt_pk(a[0], a[1]); w.y = cvt_pk(a[2], a[3]); w.z = cvt_pk(b[0], b[1]); w.w = cvt_pk(b[2], b[3]);
  return __builtin_bit_cast(bf16x8, w);
}
__device__ __forceinline__ bf16x8 lds_tr8(LAS unsigned char* p0, LAS unsigned char* p1) {
  s16x4 a = __builtin_amdgcn_ds_read_tr16_b64_v4i16((LAS s16x4*)p0);
  s16x4 b = __builtin_amdgcn_ds_read_tr16_b64_v4i16((LAS s16x4*)p1);
  s16x8 c = __builtin_shufflevector(a, b, 0, 1, 2, 3, 4, 5, 6, 7);
  return __builtin_bit_cast(bf16x8, c);
}
__device__ __forceinline__ float silu_f(float g) { return g * __builtin_amdgcn_rcpf(1.0f + __builtin_amdgcn_exp2f(g * -1.44269504089f)); }
#define MFMA(a, b, c) __builtin_amdgcn_mfma_f32_16x16x32_bf16(a, b, c, 0, 0, 0)

typedef unsigned long long u64;
typedef GAS u64 gu64;
__device__ __forceinline__ void flag_set(unsigned char* ws, size_t off, u64 tag) { __hip_atomic_store((gu64*)(ws + off), tag, __ATOMIC_RELAXED, __HIP_MEMORY_SCOPE_AGENT); }
__device__ __forceinline__ void flag_wait(unsigned char* ws, size_t off, int n, u64 tag, int l) {
  gu64* p = (gu64*)(ws + off) + (l < n ? l : 0);
  for (unsigned spins = 0; spins < (1u << 18); ++spins) {
    const u64 v = __hip_atomic_load(p, __ATOMIC_RELAXED, __HIP_MEMORY_SCOPE_AGENT);
    if (__all(v == tag)) break;
    __builtin_amdgcn_s_sleep(1);
  }
  asm volatile("" ::: "memory");
}

#define STAMP(k) do {} while (0)

__device__ __forceinline__ float xsum16_32(float v) {
  const auto a = __builtin_amdgcn_permlane16_swap(__float_as_uint(v), __float_as_uint(v), false, false);
  v = __uint_as_float(a[0]) + __uint_as_float(a[1]);
  const auto b = __builtin_amdgcn_permlane32_swap(__float_as_uint(v), __float_as_uint(v), false, false);
  return __uint_as_float(b[0]) + __uint_as_float(b[1]);
}
__device__ __forceinline__ float xmax16_32(float v) {
  const auto a = __builtin_amdgcn_permlane16_swap(__float_as_uint(v), __float_as_uint(v), false, false);
  v = fmaxf(__uint_as_float(a[0]), __uint_as_float(a[1]));
  const auto b = __builtin_amdgcn_permlane32_swap(__float_as_uint(v), __float_as_uint(v), false, false);
  return fmaxf(__uint_as_float(b[0]), __uint_as_float(b[1]));
}
#define PF_STRIDE 128
constexpr int K1_ASLOT = 16384, K1_BSLOT = 20480, K1_BOFF = 4 * K1_ASLOT, K1_LDS = K1_BOFF + 4 * K1_BSLOT;
constexpr int LDS_PFDUMP = K1_LDS, LDS_LNRED = K1_LDS + 256, LDS_TOTAL = K1_LDS + 256 + 1024, LDS_HALO = 131072;

struct K1Ctx {
  LAS unsigned char* lds;
  unsigned aoff;
  unsigned vb[6];
  unsigned vst0, vst1;
  i32x4 s0, s1, s2;
  unsigned so0, so1;
  unsigned ldst;
};

template <int TYPE> __device__ __forceinline__ void k1_stage_b(const K1Ctx& c, int s, int slot, unsigned cnt, int lim) {
  const unsigned d = K1_BOFF + slot * K1_BSLOT + c.ldst;
  unsigned t0, t1;
  if (TYPE == 0) {
    const unsigned kq = (unsigned)s * (32u * 6144u) + c.so0, kg = (unsigned)s * (32u * 7168u) + c.so0;
    asm volatile(
      "s_cmp_ge_u32 %10, %11\n\ts_cbranch_scc1 .Lsk%=\n\t"
      "s_mov_b32 m0, %9\n\ts_add_u32 %0, %7, 0x800\n\tbuffer_load_dwordx4 %2, %4, %7 offen lds\n\t"
      "s_add_u32 m0, %9, 0x400\n\ts_add_u32 %1, %7, 0x1000\n\tbuffer_load_dwordx4 %2, %4, %0 offen lds\n\t"
      "s_add_u32 m0, %9, 0x800\n\ts_nop 0\n\tbuffer_load_dwordx4 %2, %4, %1 offen lds\n\t"
      "s_add_u32 m0, %9, 0xc00\n\ts_nop 0\n\tbuffer_load_dwordx4 %3, %5, %8 offen lds\n\t"
      "s_add_u32 m0, %9, 0x1000\n\ts_nop 0\n\tbuffer_load_dwordx4 %3, %6, %8 offen lds\n"
      ".Lsk%=:"
      : "=&s"(t0), "=&s"(t1)
      : "v"(c.vst0), "v"(c.vst1), "s"(c.s0), "s"(c.s1), "s"(c.s2), "s"(kq), "s"(kg), "s"(d), "s"(cnt), "i"(lim) : "memory", "scc");
  } else {
    const unsigned k0 = (unsigned)s * (32u * 7168u) + c.so0, k1 = (unsigned)s * (32u * 7168u) + c.so1;
    asm volatile(
      "s_cmp_ge_u32 %10, %11\n\ts_cbranch_scc1 .Lsk%=\n\t"
      "s_mov_b32 m0, %9\n\ts_add_u32 %0, %7, 0x80\n\tbuffer_load_dwordx4 %2, %4, %7 offen lds\n\t"
      "s_add_u32 m0, %9, 0x400\n\ts_nop 0\n\tbuffer_load_dwordx4 %2, %4, %0 offen lds\n\t"
      "s_add_u32 m0, %9, 0x800\n\ts_nop 0\n\tbuffer_load_dwordx4 %2, %5, %7 offen lds\n\t"
      "s_add_u32 m0, %9, 0xc00\n\ts_nop 0\n\tbuffer_load_dwordx4 %2, %5, %0 offen lds\n\t"
      "s_add_u32 m0, %9, 0x1000\n\ts_nop 0\n\tbuffer_load_dwordx4 %3, %6, %8 offen lds\n"
      ".Lsk%=:"
      : "=&s"(t0), "=&s"(t1)
      : "v"(c.vst0), "v"(c.vst1), "s"(c.s0), "s"(c.s1), "s"(c.s2), "s"(k0), "s"(k1), "s"(d), "s"(cnt), "i"(lim) : "memory", "scc");
  }
}
__device__ __forceinline__ void k1_stage_a(const K1Ctx& c, int s, int slot, unsigned cnt, int lim) {
  const unsigned d = slot * K1_ASLOT + c.ldst;
  const unsigned k = (unsigned)s * 1024u + c.so0;
  unsigned t0;
  asm volatile(
    "s_cmp_ge_u32 %5, %6\n\ts_cbranch_scc1 .Lsk%=\n\t"
    "s_mov_b32 m0, %4\n\ts_add_u32 %0, %3, 0x8000\n\tbuffer_load_dwordx4 %1, %2, %3 offen sc1 lds\n\t"
    "s_add_u32 m0, %4, 0x400\n\ts_nop 0\n\tbuffer_load_dwordx4 %1, %2, %0 offen sc1 lds\n\t"
    "s_add_u32 m0, %4, 0x800\n\ts_add_u32 %0, %3, 0x10000\n\tbuffer_load_dwordx4 %1, %2, %0 offen sc1 lds\n\t"
    "s_add_u32 m0, %4, 0xc00\n\ts_add_u32 %0, %3, 0x18000\n\tbuffer_load_dwordx4 %1, %2, %0 offen sc1 lds\n"
    ".Lsk%=:"
    : "=&s"(t0) : "v"(c.vst0), "s"(c.s0), "s"(k), "s"(d), "s"(cnt), "i"(lim) : "memory", "scc");
}
#define WAIT_V2(STEADY, TAIL, cnt, lim) do { asm volatile("s_cmp_lt_u32 %0, %1\n\ts_cbranch_scc1 .Lw%=\n\ts_waitcnt vmcnt(" #TAIL ")\n.Lw%=:\n\ts_waitcnt vmcnt(" #STEADY ")" :: "s"(cnt), "i"(lim) : "memory", "scc"); } while (0)
#define BAR_IF(cnt, lim) do { SCHED(); asm volatile("s_cmp_ge_u32 %0, %1\n\ts_cbranch_scc1 .Lb%=\n\ts_barrier\n.Lb%=:" :: "s"(cnt), "i"(lim) : "memory", "scc"); SCHED(); } while (0)
template <int TYPE> __device__ __forceinline__ void k1_read(const K1Ctx& c, int slot, bf16x8 (&af)[4], bf16x8 (&bfr)[10]) {
  LAS unsigned char* lds = c.lds;
  const int bs = slot * K1_BSLOT;
  if (TYPE == 0) {
#pragma unroll
    for (int j = 0; j < 8; ++j) { LAS unsigned char* p = lds + c.vb[j & 1] + bs + (j >> 1) * 1024; bfr[j] = lds_tr8(p, p + 512); }
#pragma unroll
    for (int j = 0; j < 2; ++j) { LAS unsigned char* p = lds + c.vb[2 + j] + bs; bfr[8 + j] = lds_tr8(p, p + 512); }
  } else {
#pragma unroll
    for (int j = 0; j < 8; ++j) { LAS unsigned char* p = lds + c.vb[j & 3] + bs + (j >> 2) * 2048; bfr[j] = lds_tr8(p, p + 512); }
#pragma unroll
    for (int j = 0; j < 2; ++j) { LAS unsigned char* p = lds + c.vb[4 + j] + bs; bfr[8 + j] = lds_tr8(p, p + 256); }
  }
#pragma unroll
  for (int i = 0; i < 4; ++i) af[i] = *(LAS bf16x8*)(lds + c.aoff + slot * K1_ASLOT + i * 1024);
}
#define K1_EARLY 0
template <int TYPE, int N0, int N1> __device__ __forceinline__ void k1_mma(f32x4 (&acc)[4][10], const bf16x8 (&af)[4], const bf16x8 (&bfr)[10]) {
#pragma unroll
  for (int n = N0; n < N1; ++n) {
    const int j = n >> 2, i = n & 3;
    if (TYPE == 0 && (j == 4 || j == 5)) acc[i][j] = MFMA(af[i], bfr[j], acc[i][j]);
    else acc[i][j] = MFMA(bfr[j], af[i], acc[i][j]);
  }
}
__device__ __forceinline__ void k1_pf(unsigned voff, i32x4 srd, unsigned soff, unsigned on, unsigned cnt, int lim) {
  asm volatile("s_cmp_lg_u32 %3, 4\n\ts_cbranch_scc1 .Lpf%=\n\ts_cmp_ge_u32 %4, %5\n\ts_cbranch_scc1 .Lpf%=\n\t"
               "s_mov_b32 m0, %6\n\ts_nop 0\n\tbuffer_load_dword %0, %1, %2 offen lds\n.Lpf%=:"
               ::"v"(voff), "s"(srd), "s"(soff), "s"(on), "s"(cnt), "i"(lim), "i"(LDS_PFDUMP) : "memory", "scc");
}
#define WAIT_A(PFN, PFT, PLN, PLT, pf, cnt, lim) do { asm volatile( \
    "s_cmp_lt_u32 %1, %2\n\ts_cbranch_scc1 .Ls%=\n\t" \
    "s_cmp_eq_u32 %0, 4\n\ts_cbranch_scc1 .Lt%=\n\ts_waitcnt vmcnt(" #PLT ")\n.Lt%=:\n\ts_waitcnt vmcnt(" #PFT ")\n" \
    ".Ls%=:\n\ts_cmp_eq_u32 %0, 4\n\ts_cbranch_scc1 .Lu%=\n\ts_waitcnt vmcnt(" #PLN ")\n.Lu%=:\n\ts_waitcnt vmcnt(" #PFN ")" \
    :: "s"(pf), "s"(cnt), "i"(lim) : "memory", "scc"); } while (0)

constexpr int K2_SLOT = 16384;
constexpr int K2_TP = P_STEPS, K2_TQ = KS2 - P_STEPS;
static_assert(K2_TP % 8 == 0 && K2_TQ % 8 == 0 && K2_TP >= 48, "step counts must be multiples of 8");
constexpr unsigned K2_H0 = K2_TP - 32;
__device__ __forceinline__ unsigned k2_vstb(int w4, int l) { const unsigned r8 = l >> 3, ch = (l & 7) ^ (((r8 >> 1) & 3) << 1); return (8 * w4 + r8) * 2048 + ch * 16; }
__device__ __forceinline__ void k2_prologue_b(int w4, int l, const u16* wo, const u16* wd, bool isp, int ct) {
  const i32x4 sb = isp ? make_srd(wo, D_ * D_ * 2) : make_srd(wd, F_ * D_ * 2);
  const unsigned vb = k2_vstb(w4, l), k0 = (isp ? 0u : K2_H0) * 65536u + ct * 256;
#pragma unroll
  for (int t = 0; t < 8; ++t) { glds16(vb, sb, k0 + t * 65536u, t * K2_SLOT + 8192 + w4 * 2048); glds16(vb, sb, k0 + t * 65536u + 128u, t * K2_SLOT + 8192 + w4 * 2048 + 1024); }
}

template <int TYPE> __device__ __forceinline__ u64 k1_body(LAS unsigned char* lds, const u16* x, const u16* wqkv, const u16* wg, const u16* wu, const u16* wo, const u16* wd, unsigned char* ws, int pm, int pa  , int lnmf, bool isp, int ct) {
  const int tid = threadIdx.x;
  const int w = __builtin_amdgcn_readfirstlane(tid >> 6), l = tid & 63;
  const int grp = w >> 2, wr = w & 3;
  const int g = l >> 4, q = (l & 15) >> 2, p = l & 3;
  STAMP(0);
  K1Ctx c; c.lds = lds;
  c.aoff = l * 16 + wr * 4096;
  {
    const unsigned s2 = ((q >> 1) & 1) | ((g & 1) << 1);
    const unsigned lanep = K1_BOFF + g * 5120 + q * 128 + (p >> 1) * 16 + (p & 1) * 8;
    if (TYPE == 0) {
      c.vb[0] = lanep + (((2 * grp) ^ s2) << 5); c.vb[1] = lanep + (((2 * grp + 1) ^ s2) << 5);
      c.vb[2] = c.vb[0] + 4096; c.vb[3] = c.vb[1] + 4096; c.vb[4] = 0; c.vb[5] = 0;
    } else {
#pragma unroll
      for (int j = 0; j < 4; ++j) c.vb[j] = lanep + ((j ^ s2) << 5) + grp * 1024;
      const unsigned mx = K1_BOFF + (2 * (g >> 1) * 5 + 4) * 1024 + (g & 1) * 512 + q * 64 + (((2 * grp + (p >> 1)) ^ ((g & 1) << 1)) << 4) + (p & 1) * 8;
      c.vb[4] = mx; c.vb[5] = mx + 5120;
    }
  }
  const int c0 = 1024 + 160 * pa;
  const int odd = pa & 1;
  if (grp == 0) {
    const unsigned r8 = l >> 3, sw = (((r8 >> 1) & 1) | ((wr & 1) << 1)) << 1, ch = (l & 7) ^ sw;
    c.ldst = wr * 5120;
    if (TYPE == 0) {
      c.vst0 = (8 * wr + r8) * 6144 + ch * 16; c.vst1 = (8 * wr + r8) * 7168 + ch * 16;
      c.s0 = make_srd(wqkv, D_ * 3 * D_ * 2); c.s1 = make_srd(wg, D_ * F_ * 2); c.s2 = make_srd(wu, D_ * F_ * 2);
      c.so0 = pa * 128; c.so1 = 0;
    } else {
      c.vst0 = (8 * wr + r8) * 7168 + ch * 16;
      const unsigned r16 = l >> 2, chm = (l & 3) ^ (((r16 >> 3) & 1) << 1);
      c.vst1 = (16 * (wr >> 1) + r16) * 7168 + chm * 16;
      c.s0 = make_srd(wg, D_ * F_ * 2); c.s1 = make_srd(wu, D_ * F_ * 2); c.s2 = make_srd((wr & 1) ? wu : wg, D_ * F_ * 2);
      c.so0 = (c0 + (odd ? 32 : 0)) * 2; c.so1 = (c0 + (odd ? 0 : 128)) * 2;
    }
  } else {
    c.vst0 = l * 16; c.vst1 = 0;
    c.s0 = make_srd(ws + WS_XN, S_ * D_ * 2); c.s1 = c.s0; c.s2 = c.s0;
    c.so0 = (pm * 16 + 4 * wr) * 32768; c.so1 = 0;
    c.ldst = wr * 4096;
  }

  unsigned glo, ghi;
  if (grp == 0) {
    u32x2 gen;
    asm volatile("global_load_dwordx2 %0, %1, off sc1" : "=&v"(gen) : "v"(ws + WS_GEN) : "memory");
    k1_stage_b<TYPE>(c, 0, 0, 0, 1); k1_stage_b<TYPE>(c, 1, 1, 0, 1); k1_stage_b<TYPE>(c, 2, 2, 0, 1);
    asm volatile("s_waitcnt vmcnt(15)" : "+v"(gen) :: "memory");
    glo = __builtin_amdgcn_readfirstlane(gen.x); ghi = __builtin_amdgcn_readfirstlane(gen.y);
  } else {
    u32x2 gen;
    asm volatile("global_load_dwordx2 %0, %1, off sc1\n\ts_waitcnt vmcnt(0)" : "=&v"(gen) : "v"(ws + WS_GEN) : "memory");
    glo = __builtin_amdgcn_readfirstlane(gen.x); ghi = __builtin_amdgcn_readfirstlane(gen.y);
  }
  const u64 tag = (((u64)ghi << 32) | glo) + 1;

  if (lnmf >= 0) {
    const int m = l & 15;
    const u16* row = x + (size_t)(lnmf * 16 + m) * D_;
    u32x4 v[4];
#pragma unroll
    for (int i = 0; i < 4; ++i) v[i] = *(const u32x4*)(row + w * 128 + g * 8 + i * 32);
    const float piv = __uint_as_float((unsigned)row[0] << 16);
    float f[32]; float sm = 0.f, sq = 0.f;
#pragma unroll
    for (int i = 0; i < 4; ++i)
#pragma unroll
      for (int j = 0; j < 4; ++j) {
        const float a = bf_lo(v[i][j]), b = bf_hi(v[i][j]); f[i * 8 + 2 * j] = a; f[i * 8 + 2 * j + 1] = b;
        const float da = a - piv, db = b - piv; sm += da + db; sq += da * da + db * db;
      }
    sm = xsum16_32(sm); sq = xsum16_32(sq);
    LAS f32x2* red = (LAS f32x2*)(lds + LDS_LNRED);
    if (g == 0) red[w * 16 + m] = (f32x2){sm, sq};
    WAIT_L(0); BAR();
    float st = 0.f, qt = 0.f;
#pragma unroll
    for (int i = 0; i < 8; ++i) { const f32x2 r = red[i * 16 + m]; st += r.x; qt += r.y; }
    const float md = st * (1.0f / D_), mean = piv + md, var = fmaxf(qt * (1.0f / D_) - md * md, 0.f);
    const float mb = rbf(mean);
    const float inv = 1.0f / rbf(sqrtf(rbf(rbf(var) + 1e-5f)));
    const __amdgpu_buffer_rsrc_t xr = __builtin_amdgcn_make_buffer_rsrc(ws + WS_XN, 0, S_ * D_ * 2, 0x00020000);
#pragma unroll
    for (int i = 0; i < 4; ++i) {
      u32x4 o;
#pragma unroll
      for (int j = 0; j < 4; ++j) o[j] = cvt_pk(rbf(f[i * 8 + 2 * j] - mb) * inv, rbf(f[i * 8 + 2 * j + 1] - mb) * inv);
      __builtin_amdgcn_raw_buffer_store_b128(o, xr, (lnmf * 32 + w * 4 + i) * 1024 + l * 16, 0, 16);
    }
    WAIT_V(0); BAR();
    if (tid == 0) flag_set(ws, WS_LNF + lnmf * 8, tag);
  }

  f32x4 acc[4][10];
#pragma unroll
  for (int i = 0; i < 4; ++i)
#pragma unroll
    for (int j = 0; j < 10; ++j) acc[i][j] = (f32x4){0.f, 0.f, 0.f, 0.f};
  bf16x8 af[4], bfr[10];

  if (grp == 0) {
    WAIT_V(10); BAR();
    STAMP(1);
#define K1_G0_STEP(J, TAILV) do { \
      k1_read<TYPE>(c, J, af, bfr); SCHED(); \
      k1_stage_b<TYPE>(c, s + (J) + 3, ((J) + 3) & 3, s, (J) == 0 ? 32 : 28); \
      WAIT_L(0); BAR(); \
      __builtin_amdgcn_s_setprio(1); \
      k1_mma<TYPE, 0, 40 - K1_EARLY>(acc, af, bfr); \
      SCHED(); WAIT_V2(10, TAILV, s, 28); \
      BAR(); \
      if (K1_EARLY) __builtin_amdgcn_s_setprio(3); \
      k1_mma<TYPE, 40 - K1_EARLY, 40>(acc, af, bfr); \
      __builtin_amdgcn_s_setprio(0); SCHED(); } while (0)
#pragma nounroll
    for (unsigned s = 0; s < 32; s += 4) {
      K1_G0_STEP(0, 10); K1_G0_STEP(1, 5); K1_G0_STEP(2, 0); K1_G0_STEP(3, 0);
    }
  } else {
#define PF_ON 1
#define K1_PF_ROW0 96
    const unsigned pf = PF_ON ? w : 99  , pfv = (blockIdx.x * 64 + l) * PF_STRIDE;
    const i32x4 sg = make_srd(wg, D_ * F_ * 2), su = make_srd(wu, D_ * F_ * 2), sq = make_srd(wqkv, D_ * 3 * D_ * 2), sd = make_srd(wd, F_ * D_ * 2), so = make_srd(wo, D_ * D_ * 2);
    constexpr unsigned OOR = 0x7f000000u, MB = 16384u * PF_STRIDE;
    constexpr unsigned NG = ((1024 - K1_PF_ROW0) * 7168u + MB - 1) / MB, NQ = ((1024 - K1_PF_ROW0) * 6144u + MB - 1) / MB, ND = (F_ * D_ * 2 + MB - 1) / MB, NO = (D_ * D_ * 2 + MB - 1) / MB;
    static_assert(NG + (ND + 1) / 2 <= 8 && NG + ND / 2 <= 7 && NQ + NO <= 7, "prefetch schedule does not fit");
    flag_wait(ws, WS_LNF + (pm * 16 + 4 * wr) * 8, 4, tag, l);
    k1_pf(pfv, sq, OOR, pf, 0, 1); k1_stage_a(c, 0, 0, 0, 1);
    k1_pf(pfv, sq, OOR, pf, 0, 1); k1_stage_a(c, 1, 1, 0, 1);
    k1_stage_a(c, 2, 2, 0, 1);
    WAIT_A(9, 9, 8, 8, pf, 0u, 1); BAR();
    BAR();
#define K1_G1_STEP(J, PFN, PFT, PLT) do { \
      k1_read<TYPE>(c, J, af, bfr); SCHED(); \
      if ((J) == 0) k1_pf(pfv, it < NG ? sg : sd, it < NG ? K1_PF_ROW0 * 7168u + it * MB : (it - NG) * MB, pf, s, 32); \
      if ((J) == 1) k1_pf(pfv, it < NG ? su : sd, it < NG ? K1_PF_ROW0 * 7168u + it * MB : (it - NG + (ND + 1) / 2) * MB, pf, s, 28); \
      if ((J) == 2) k1_pf(pfv, it < NQ ? sq : so, it < NQ ? K1_PF_ROW0 * 6144u + it * MB : (it - NQ) * MB, pf, s, 28); \
      k1_stage_a(c, s + (J) + 3, ((J) + 3) & 3, s, (J) == 0 ? 32 : 28); \
      WAIT_L(0); WAIT_A(PFN, PFT, 8, PLT, pf, s, 28); \
      BAR(); \
      __builtin_amdgcn_s_setprio(1); \
      k1_mma<TYPE, 0, 40 - K1_EARLY>(acc, af, bfr); \
      if ((J) < 3) BAR(); else BAR_IF(s, 28); \
      if (K1_EARLY) __builtin_amdgcn_s_setprio(3); \
      k1_mma<TYPE, 40 - K1_EARLY, 40>(acc, af, bfr); \
      __builtin_amdgcn_s_setprio(0); SCHED(); } while (0)
#pragma nounroll
    for (unsigned s = 0; s < 32; s += 4) {
      const unsigned it = s >> 2;
      K1_G1_STEP(0, 9, 9, 8); K1_G1_STEP(1, 10, 5, 4); K1_G1_STEP(2, 10, 0, 0); K1_G1_STEP(3, 9, 0, 0);
    }
    k2_prologue_b(wr, l, wo, wd, isp, ct);
  }
  STAMP(2);

  const __amdgpu_buffer_rsrc_t a2 = __builtin_amdgcn_make_buffer_rsrc(ws + WS_A2, 0, 128 * KS2 * 1024, 0x00020000);
  const unsigned mf0 = pm * 16 + 4 * wr;
  if (TYPE == 1) {
    const unsigned ksf = c0 / 32 + (odd ? 1 : 0) + 2 * grp, ksm = c0 / 32 + (odd ? 0 : 4);
#pragma unroll
    for (int i = 0; i < 4; ++i) {
      const unsigned base = (mf0 + i) * (KS2 * 1024);
#pragma unroll
      for (int t = 0; t < 2; ++t) {
        f32x4 h0, h1;
#pragma unroll
        for (int r = 0; r < 4; ++r) { h0[r] = silu_f(acc[i][2 * t][r]) * acc[i][2 * t + 4][r]; h1[r] = silu_f(acc[i][2 * t + 1][r]) * acc[i][2 * t + 5][r]; }
        __builtin_amdgcn_raw_buffer_store_b128(__builtin_bit_cast(u32x4, pack8(h0, h1)), a2, l * 16 + base + (ksf + t) * 1024, 0, ST_AUX);
      }
      f32x4 h0;
#pragma unroll
      for (int r = 0; r < 4; ++r) h0[r] = silu_f(acc[i][8][r]) * acc[i][9][r];
      u32x2 o; o.x = cvt_pk(h0[0], h0[1]); o.y = cvt_pk(h0[2], h0[3]);
      __builtin_amdgcn_raw_buffer_store_b64(o, a2, l * 16 + grp * 8 + base + ksm * 1024, 0, ST_AUX);
    }
  } else {
    const int head = 2 * pa + grp;
    bf16x8 kop[5]; u32x2 vp[5][2];
#pragma unroll
    for (int i = 0; i < 4; ++i) {
      kop[i + 1] = pack8(acc[i][2], acc[i][3]);
#pragma unroll
      for (int d = 0; d < 2; ++d) { vp[i + 1][d].x = cvt_pk(acc[i][4 + d][0], acc[i][4 + d][1]); vp[i + 1][d].y = cvt_pk(acc[i][4 + d][2], acc[i][4 + d][3]); }
    }
    const u32x4 klast = __builtin_bit_cast(u32x4, kop[4]);
    const u32x4 vlast = (u32x4){vp[4][0].x, vp[4][0].y, vp[4][1].x, vp[4][1].y};
    const __amdgpu_buffer_rsrc_t hr = __builtin_amdgcn_make_buffer_rsrc(ws + WS_HALO, 0, 8 * 32 * 2048, 0x00020000);
    const bool send = (wr == 3) && (pm < 7);
    if (send) {
      const unsigned ho = (pm * 32 + head) * 2048;
      __builtin_amdgcn_raw_buffer_store_b128(klast, hr, l * 16 + ho, 0, 16);
      __builtin_amdgcn_raw_buffer_store_b128(vlast, hr, l * 16 + ho + 1024, 0, 16);
    }
    if (wr != 3) {
      *(LAS u32x4*)(lds + LDS_HALO + w * 2048 + l * 16) = klast;
      *(LAS u32x4*)(lds + LDS_HALO + w * 2048 + 1024 + l * 16) = vlast;
    }
    bf16x8 qop[4];
#pragma unroll
    for (int i = 0; i < 4; ++i) qop[i] = pack8(acc[i][0], acc[i][1]);
#pragma unroll
    for (int i = 0; i < 4; ++i) {
      f32x4 h0, h1;
#pragma unroll
      for (int r = 0; r < 4; ++r) { h0[r] = silu_f(acc[i][6][r]) * acc[i][8][r]; h1[r] = silu_f(acc[i][7][r]) * acc[i][9][r]; }
      __builtin_amdgcn_raw_buffer_store_b128(__builtin_bit_cast(u32x4, pack8(h0, h1)), a2, l * 16 + (mf0 + i) * (KS2 * 1024) + (2 * pa + grp) * 1024, 0, ST_AUX);
    }
    if (send) {
      WAIT_V(4);
      if (l == 0) flag_set(ws, WS_HLF + (pm * 32 + head) * 64, tag);
    }
    WAIT_L(0); BAR();
    const int m = l & 15; const bool par = m & 1;
    const bool own0 = 4 * g + (m & 1) <= m, own1 = 4 * g + (m & 1) + 2 <= m;
    const unsigned hmask = par ? 0xffff0000u : 0x0000ffffu;
    const float scale = 0.17677669529663687f * 1.44269504089f;
    auto attend = [&](int i, bool have_prev) {
      const f32x4 z = (f32x4){0.f, 0.f, 0.f, 0.f};
      const f32x4 s0 = MFMA(kop[i + 1], qop[i], z);
      const f32x4 s1 = MFMA(kop[i], qop[i], z);
      float l0 = own0 ? (par ? s0[1] : s0[0]) : (par ? s1[1] : s1[0]);
      float l1 = own1 ? (par ? s0[3] : s0[2]) : (par ? s1[3] : s1[2]);
      l0 *= scale; l1 *= scale;
      if (!have_prev) { l0 = own0 ? l0 : -3.0e38f; l1 = own1 ? l1 : -3.0e38f; }
      const float mx = xmax16_32(fmaxf(l0, l1));
      const float e0 = __builtin_amdgcn_exp2f(l0 - mx), e1 = __builtin_amdgcn_exp2f(l1 - mx);
      const float inv = __builtin_amdgcn_rcpf(xsum16_32(e0 + e1));
      const unsigned w0 = cvt_pk(e0 * inv, e0 * inv) & hmask, w1 = cvt_pk(e1 * inv, e1 * inv) & hmask;
      const u32x4 pw = (u32x4){own0 ? 0u : w0, own1 ? 0u : w1, own0 ? w0 : 0u, own1 ? w1 : 0u};
      const bf16x8 pop = __builtin_bit_cast(bf16x8, pw);
      f32x4 o[2];
#pragma unroll
      for (int d = 0; d < 2; ++d) {
        const u32x4 vv = (u32x4){vp[i][d].x, vp[i][d].y, vp[i + 1][d].x, vp[i + 1][d].y};
        o[d] = MFMA(__builtin_bit_cast(bf16x8, vv), pop, z);
      }
      __builtin_amdgcn_raw_buffer_store_b128(__builtin_bit_cast(u32x4, pack8(o[0], o[1])), a2, l * 16 + (mf0 + i) * (KS2 * 1024) + (112 + head) * 1024, 0, ST_AUX);
    };
    attend(1, true); attend(2, true); attend(3, true);
    bool have_prev = true;
    u32x4 kh, vh;
    if (wr == 0) {
      if (pm > 0) {
        flag_wait(ws, WS_HLF + ((pm - 1) * 32 + head) * 64, 1, tag, l);
        const unsigned ho = ((pm - 1) * 32 + head) * 2048;
        kh = __builtin_amdgcn_raw_buffer_load_b128(hr, l * 16 + ho, 0, 16);
        vh = __builtin_amdgcn_raw_buffer_load_b128(hr, l * 16 + ho + 1024, 0, 16);
      } else { kh = (u32x4){0u, 0u, 0u, 0u}; vh = kh; have_prev = false; }
    } else {
      kh = *(LAS u32x4*)(lds + LDS_HALO + (w - 1) * 2048 + l * 16);
      vh = *(LAS u32x4*)(lds + LDS_HALO + (w - 1) * 2048 + 1024 + l * 16);
    }
    kop[0] = __builtin_bit_cast(bf16x8, kh);
    vp[0][0] = (u32x2){vh.x, vh.y}; vp[0][1] = (u32x2){vh.z, vh.w};
    attend(0, have_prev);
  }
  WAIT_V(0); BAR();
  if (tid == 0) flag_set(ws, WS_T1F + (pm * 32 + TYPE * 16 + pa) * 8, tag);
  STAMP(3);
  return tag;
}

struct K2Frag { bf16x8 a[4], b[4]; };
struct K2Rd { LAS unsigned char* lds; unsigned aoff[2], vb[2][4]; };
template <int SLOT> __device__ __forceinline__ void k2_read(const K2Rd& c, K2Frag& f) {
  constexpr int H = SLOT >> 2, O = (SLOT & 3) * K2_SLOT;
#pragma unroll
  for (int j = 0; j < 4; ++j) { LAS unsigned char* p = c.lds + c.vb[H][j] + O; f.b[j] = lds_tr8(p, p + 4096); }
#pragma unroll
  for (int i = 0; i < 4; ++i) f.a[i] = *(LAS bf16x8*)(c.lds + c.aoff[H] + O + i * 1024);
}
template <int SLOTN> __device__ __forceinline__ void k2_step(const K2Rd& c, f32x4 (&acc)[4][4], const K2Frag& cur, K2Frag& nxt) {
  k2_read<SLOTN>(c, nxt);
#pragma unroll
  for (int j = 0; j < 4; ++j)
#pragma unroll
    for (int i = 0; i < 4; ++i) acc[i][j] = MFMA(cur.b[j], cur.a[i], acc[i][j]);
#pragma unroll
  for (int k = 0; k < 6; ++k) { __builtin_amdgcn_sched_group_barrier(0x8, 1, 0); __builtin_amdgcn_sched_group_barrier(0x100, 2, 0); }
  __builtin_amdgcn_sched_group_barrier(0x8, 10, 0);
  SCHED(); __builtin_amdgcn_s_waitcnt(0xC07F); asm volatile("" ::: "memory"); BAR();
}
__device__ __forceinline__ void k2_consume(const K2Rd& c, f32x4 (&acc)[4][4], K2Frag& f0, K2Frag& f1, int iters) {
#pragma nounroll
  for (int it = 0; it < iters; ++it) {
    k2_step<1>(c, acc, f0, f1); k2_step<2>(c, acc, f1, f0); k2_step<3>(c, acc, f0, f1); k2_step<4>(c, acc, f1, f0);
    k2_step<5>(c, acc, f0, f1); k2_step<6>(c, acc, f1, f0); k2_step<7>(c, acc, f0, f1); k2_step<0>(c, acc, f1, f0);
  }
}

struct K2Ld { unsigned vsta, vstb; i32x4 sa; unsigned ldst; };
__device__ __forceinline__ void k2_stage(const K2Ld& c, i32x4 sb, unsigned ka, unsigned kb, int slot) {
  const unsigned d = slot * K2_SLOT + c.ldst;
  unsigned t0;
  asm volatile(
    "s_mov_b32 m0, %7\n\ts_add_u32 %0, %5, 0x24000\n\tbuffer_load_dwordx4 %1, %3, %5 offen sc1 lds\n\t"
    "s_add_u32 m0, %7, 0x400\n\ts_nop 0\n\tbuffer_load_dwordx4 %1, %3, %0 offen sc1 lds\n\t"
    "s_add_u32 m0, %7, 0x2000\n\ts_add_u32 %0, %6, 0x80\n\tbuffer_load_dwordx4 %2, %4, %6 offen lds\n\t"
    "s_add_u32 m0, %7, 0x2400\n\ts_nop 0\n\tbuffer_load_dwordx4 %2, %4, %0 offen lds"
    : "=&s"(t0) : "v"(c.vsta), "v"(c.vstb), "s"(c.sa), "s"(sb), "s"(ka), "s"(kb), "s"(d) : "memory", "scc");
}
__device__ __forceinline__ void k2_load(const K2Ld& c, i32x4 sb, unsigned ka, unsigned kb, int iters) {
#pragma nounroll
  for (int it = 0; it < iters; ++it) {
#pragma unroll
    for (int j = 0; j < 8; ++j) {
      k2_stage(c, sb, ka, kb, j);
      ka += 1024u; kb += 65536u;
      WAIT_V(24); BAR();
    }
  }
}
template <int AUX> __device__ __forceinline__ void glds16x(unsigned voff, i32x4 srd, unsigned soff, unsigned ldsaddr) {
  if (AUX == 16) asm volatile("s_mov_b32 m0, %3\n\ts_nop 0\n\tbuffer_load_dwordx4 %0, %1, %2 offen sc1 lds" ::"v"(voff), "s"(srd), "s"(soff), "s"(ldsaddr) : "memory");
  else asm volatile("s_mov_b32 m0, %3\n\ts_nop 0\n\tbuffer_load_dwordx4 %0, %1, %2 offen lds" ::"v"(voff), "s"(srd), "s"(soff), "s"(ldsaddr) : "memory");
}

template <bool ISP> __device__ __forceinline__ void k2_body(LAS unsigned char* lds, const u16* x, const u16* wo, const u16* wd, u16* out, unsigned char* ws, int rt, int ct, u64 tag) {
  constexpr int NT = ISP ? K2_TP : K2_TQ;
  int tid = threadIdx.x; asm volatile("" : "+v"(tid));
  const int w = __builtin_amdgcn_readfirstlane(tid >> 6), l = tid & 63;
  const int w4 = w & 3, wm = w4 >> 1, wn = w4 & 1;
  const int g = l >> 4, q = (l & 15) >> 2, p = l & 3;
  const int tile = rt * 8 + ct;
  if (w >= 4) {
    K2Ld c;
    c.vsta = l * 16; c.vstb = k2_vstb(w4, l);
    c.sa = make_srd(ws + WS_A2, 128 * KS2 * 1024);
    const i32x4 swd = make_srd(wd, F_ * D_ * 2), swo = make_srd(wo, D_ * D_ * 2);
    const unsigned soa = (rt * 8 + 2 * w4) * (KS2 * 1024), sob = ct * 256;
    c.ldst = w4 * 2048;
    flag_wait(ws, WS_T1F + (rt >> 1) * 256, 32, tag, l);
    const unsigned ka0 = (ISP ? 112u : K2_H0) * 1024u + soa;
#pragma unroll
    for (int t = 0; t < 8; ++t) { glds16x<16>(c.vsta, c.sa, ka0 + t * 1024u, t * K2_SLOT + c.ldst); glds16x<16>(c.vsta, c.sa, ka0 + t * 1024u + 0x24000u, t * K2_SLOT + c.ldst + 1024); }
    WAIT_V(12); BAR();
    BAR();
    {
      const i32x4 sb = ISP ? swo : swd;
      unsigned ka = ka0 + 8 * 1024u, kb = ((ISP ? 0u : K2_H0) + 8) * 65536u + sob;
#define K2_FIRST(J, N) do { k2_stage(c, sb, ka + (J) * 1024u, kb + (J) * 65536u, J); WAIT_V(N); BAR(); } while (0)
      K2_FIRST(0, 14); K2_FIRST(1, 16); K2_FIRST(2, 18); K2_FIRST(3, 20); K2_FIRST(4, 22); K2_FIRST(5, 24); K2_FIRST(6, 24); K2_FIRST(7, 24);
    }
    if (ISP) { k2_load(c, swo, (112 + 16) * 1024u + soa, 16 * 65536u + sob, 2); k2_load(c, swd, soa, sob, (NT - 32) / 8); }
    else k2_load(c, swd, (K2_H0 + 16) * 1024u + soa, (K2_H0 + 16) * 65536u + sob, (NT - 16) / 8);
    const i32x4 sx = make_srd(x, S_ * D_ * 2), sp = make_srd(ws + WS_PART, 128 * 65536);
    if (ISP) {
      const unsigned xv = ((rt * 128 + wm * 64 + (l & 15)) * D_ + ct * 128 + wn * 64 + (((g & 1) << 1) | (g >> 1)) * 8) * 2;
#pragma unroll
      for (int k = 0; k < 2; ++k) {
#pragma unroll
        for (int e = 0; e < 4; ++e) { const int blk = k * 4 + e; glds16x<0>(xv, sx, (blk >> 1) * 16 * D_ * 2 + (blk & 1) * 64, k * 16384 + w4 * 4096 + e * 1024); }
        WAIT_V(24); BAR();
      }
      WAIT_V(20); BAR(); WAIT_V(16); BAR();  WAIT_V(12); BAR();
      WAIT_V(0);
      if (w4 == 0) flag_wait(ws, WS_PTF + tile * 64, 4, tag, l);
      asm volatile("" ::: "memory"); BAR(); asm volatile("" ::: "memory");
#pragma unroll
      for (int e = 0; e < 16; ++e) glds16x<16>(l * 16, sp, tile * 65536 + w4 * 16384 + e * 1024, 32768 + w4 * 16384 + e * 1024);
      BAR();
      WAIT_V(0); BAR();
    } else {
      WAIT_V(20); BAR(); WAIT_V(16); BAR(); WAIT_V(12); BAR(); WAIT_V(8); BAR(); WAIT_V(4); BAR(); WAIT_V(0); BAR(); BAR(); BAR();
    }
    if (blockIdx.x == 0 && w == 5) {
#pragma unroll
      for (int k = 0; k < 4; ++k) flag_wait(ws, WS_T1F + k * 512, 64, tag, l);
      if (l == 0) flag_set(ws, WS_GEN, tag);
    }
    return;
  }
  K2Rd c; c.lds = lds;
  {
    const unsigned row = 4 * (g & 1) + q, s2 = (row >> 1) & 3;
    const unsigned lanep = 8192 + (g >> 1) * 2048 + wn * 1024 + row * 128 + (p >> 1) * 16 + (p & 1) * 8;
#pragma unroll
    for (int h = 0; h < 2; ++h) {
      c.aoff[h] = l * 16 + wm * 4096 + h * 65536;
#pragma unroll
      for (int j = 0; j < 4; ++j) c.vb[h][j] = lanep + ((j ^ s2) << 5) + h * 65536;
    }
  }
  f32x4 accf[4][4], accs[4][4];
#pragma unroll
  for (int i = 0; i < 4; ++i)
#pragma unroll
    for (int j = 0; j < 4; ++j) { accf[i][j] = (f32x4){0.f, 0.f, 0.f, 0.f}; accs[i][j] = accf[i][j]; }
  K2Frag f0, f1;
  BAR();
  STAMP(4);
  k2_read<0>(c, f0);
  WAIT_L(0); BAR();
  if (ISP) { k2_consume(c, accs, f0, f1, 4); k2_consume(c, accf, f0, f1, (NT - 32) / 8); }
  else k2_consume(c, accf, f0, f1, NT / 8);
  STAMP(5);
  if (!ISP) {
    const __amdgpu_buffer_rsrc_t pr = __builtin_amdgcn_make_buffer_rsrc(ws + WS_PART, 0, 128 * 65536, 0x00020000);
    const unsigned po = tile * 65536 + w4 * 16384 + l * 16;
#pragma unroll
    for (int i = 0; i < 4; ++i)
#pragma unroll
      for (int j = 0; j < 4; ++j) __builtin_amdgcn_raw_buffer_store_b128(__builtin_bit_cast(u32x4, accf[i][j]), pr, po + (i * 4 + j) * 1024, 0, 16);
    WAIT_V(0);
    if (l == 0) flag_set(ws, WS_PTF + tile * 64 + w4 * 8, tag);
  } else {
    const int m = l & 15;
    u16* orow = out + (size_t)(rt * 128 + wm * 64 + m) * D_ + ct * 128 + wn * 64 + (((g & 1) << 1) | (g >> 1)) * 8;
#pragma unroll
    for (int i = 0; i < 4; ++i)
#pragma unroll
      for (int jp = 0; jp < 2; ++jp) {
        unsigned y[2][2];
#pragma unroll
        for (int jj = 0; jj < 2; ++jj) {
          const int j = 2 * jp + jj;
          const f32x4 f = accf[i][j] + *(LAS f32x4*)(lds + 32768 + w4 * 16384 + (i * 4 + j) * 1024 + l * 16);
          float v[4];
#pragma unroll
          for (int r = 0; r < 4; ++r) v[r] = accs[i][j][r] * silu_f(f[r]);
          y[jj][0] = cvt_pk(v[0], v[1]); y[jj][1] = cvt_pk(v[2], v[3]);
        }
        const auto s0 = __builtin_amdgcn_permlane16_swap(y[0][0], y[1][0], false, false);
        const auto s1 = __builtin_amdgcn_permlane16_swap(y[0][1], y[1][1], false, false);
        const u32x4 xr = *(LAS u32x4*)(lds + ((i * 2 + jp) >> 2) * 16384 + w4 * 4096 + ((i * 2 + jp) & 3) * 1024 + l * 16);
        const unsigned yy[4] = {s0[0], s1[0], s0[1], s1[1]};
        u32x4 o;
#pragma unroll
        for (int r = 0; r < 4; ++r) o[r] = cvt_pk(bf_lo(xr[r]) + bf_lo(yy[r]), bf_hi(xr[r]) + bf_hi(yy[r]));
        *(u32x4*)(orow + (size_t)i * 16 * D_ + jp * 32) = o;
      }
  }
  STAMP(6);
}

__global__ __launch_bounds__(512, 2) void fused_block(const u16* __restrict__ x, const u16* __restrict__ wqkv, const u16* __restrict__ wo, const u16* __restrict__ wg,
                                                      const u16* __restrict__ wu, const u16* __restrict__ wd, u16* __restrict__ out, unsigned char* __restrict__ ws) {
  extern __shared__ __attribute__((aligned(16))) unsigned char shm[];
  LAS unsigned char* lds = (LAS unsigned char*)shm;
  const int b = blockIdx.x, xcd = b & 7, idx = b >> 3;
  const int pm = idx & 7, pn = idx >> 3, pa = xcd * 2 + (pn >> 1);
  const int lnmf = idx < 16 ? idx * 8 + xcd : -1;
  const int pr = xcd & 3, rt = (pr & 1) * 8 + (idx & 7), ct = (pr >> 1) * 4 + (idx >> 3);
  const bool isp = xcd < 4;
  u64 tag;
  if (pn & 1) tag = k1_body<1>(lds, x, wqkv, wg, wu, wo, wd, ws, pm, pa, lnmf, isp, ct);
  else tag = k1_body<0>(lds, x, wqkv, wg, wu, wo, wd, ws, pm, pa, lnmf, isp, ct);
  if (isp) k2_body<true>(lds, x, wo, wd, out, ws, rt, ct, tag);
  else k2_body<false>(lds, x, wo, wd, out, ws, rt, ct, tag);
}

extern "C" void kernel_launch(void* const* d_in, const int* in_sizes, int n_in, void* d_out, int out_size, void* d_ws, size_t ws_size, hipStream_t stream) {
  const u16* x = (const u16*)d_in[0]; const u16* wqkv = (const u16*)d_in[1]; const u16* wo = (const u16*)d_in[2];
  const u16* wg = (const u16*)d_in[4]; const u16* wu = (const u16*)d_in[5]; const u16* wd = (const u16*)d_in[6];
  (void)hipFuncSetAttribute((const void*)fused_block, hipFuncAttributeMaxDynamicSharedMemorySize, LDS_TOTAL);
  fused_block<<<256, 512, LDS_TOTAL, stream>>>(x, wqkv, wo, wg, wu, wd, (u16*)d_out, (unsigned char*)d_ws);
}
```

```cpp
#include <hip/hip_runtime.h>
#include <stdint.h>

#define LAS __attribute__((address_space(3)))
#define GAS __attribute__((address_space(1)))
typedef __bf16 bf16x8 __attribute__((ext_vector_type(8)));
typedef short s16x4 __attribute__((ext_vector_type(4)));
typedef short s16x8 __attribute__((ext_vector_type(8)));
typedef float f32x4 __attribute__((ext_vector_type(4)));
typedef float f32x2 __attribute__((ext_vector_type(2)));
typedef unsigned u32x4 __attribute__((ext_vector_type(4)));
typedef unsigned u32x2 __attribute__((ext_vector_type(2)));
typedef int i32x4 __attribute__((ext_vector_type(4)));
typedef unsigned short u16;

constexpr int S_ = 2048, D_ = 1024, F_ = 3584;
constexpr int KS2 = 144;
constexpr size_t WS_GEN = 0;
constexpr size_t REP = 4096;
constexpr size_t WS_LNF = 4096;
constexpr size_t WS_T1A = WS_LNF + 1024;
constexpr size_t WS_T1F = WS_LNF + 2048;
constexpr size_t WS_HLF = 4096 + 8 * REP;
constexpr size_t WS_PTF = WS_HLF + 16384;
static_assert(WS_PTF + 8192 <= 65536, "flag area");
constexpr size_t WS_XN = 65536;
constexpr size_t WS_A2 = WS_XN + 4194304;
constexpr size_t WS_HALO = WS_A2 + (size_t)128 * KS2 * 1024;
constexpr size_t WS_PART = WS_HALO + 8 * 32 * 2048;
constexpr size_t WS_STAMP = WS_PART + (size_t)128 * 65536;

#define P_STEPS 80
#define ST_AUX 16

__device__ __forceinline__ i32x4 make_srd(const void* p, unsigned bytes) {
  const unsigned long long a = (unsigned long long)p;
  i32x4 r; r.x = (int)(unsigned)a; r.y = (int)((unsigned)(a >> 32) & 0xffffu); r.z = (int)bytes; r.w = 0x00020000; return r;
}
__device__ __forceinline__ void glds16(unsigned voff, i32x4 srd, unsigned soff, unsigned ldsaddr) {
  asm volatile("s_mov_b32 m0, %3\n\ts_nop 0\n\tbuffer_load_dwordx4 %0, %1, %2 offen lds" ::"v"(voff), "s"(srd), "s"(soff), "s"(ldsaddr) : "memory");
}
#define WAIT_V(n) asm volatile("s_waitcnt vmcnt(" #n ")" ::: "memory")
#define WAIT_L(n) asm volatile("s_waitcnt lgkmcnt(" #n ")" ::: "memory")
#define SCHED() __builtin_amdgcn_sched_barrier(0)
#define BAR() do { SCHED(); __builtin_amdgcn_s_barrier(); SCHED(); } while (0)

typedef __bf16 bf16x2 __attribute__((ext_vector_type(2)));
__device__ __forceinline__ unsigned cvt_pk(float lo, float hi) { const f32x2 v = {lo, hi}; return __builtin_bit_cast(unsigned, __builtin_convertvector(v, bf16x2)); }
__device__ __forceinline__ float bf_lo(unsigned u) { return __uint_as_float(u << 16); }
__device__ __forceinline__ float bf_hi(unsigned u) { return __uint_as_float(u & 0xffff0000u); }
__device__ __forceinline__ float rbf(float f) { return bf_lo(cvt_pk(f, f)); }
__device__ __forceinline__ bf16x8 pack8(const f32x4& a, const f32x4& b) {
  u32x4 w; w.x = cvt_pk(a[0], a[1]); w.y = cvt_pk(a[2], a[3]); w.z = cvt_pk(b[0], b[1]); w.w = cvt_pk(b[2], b[3]);
  return __builtin_bit_cast(bf16x8, w);
}
__device__ __forceinline__ bf16x8 lds_tr8(LAS unsigned char* p0, LAS unsigned char* p1) {
  s16x4 a = __builtin_amdgcn_ds_read_tr16_b64_v4i16((LAS s16x4*)p0);
  s16x4 b = __builtin_amdgcn_ds_read_tr16_b64_v4i16((LAS s16x4*)p1);
  s16x8 c = __builtin_shufflevector(a, b, 0, 1, 2, 3, 4, 5, 6, 7);
  return __builtin_bit_cast(bf16x8, c);
}
__device__ __forceinline__ float silu_f(float g) { return g * __builtin_amdgcn_rcpf(1.0f + __builtin_amdgcn_exp2f(g * -1.44269504089f)); }
#define MFMA(a, b, c) __builtin_amdgcn_mfma_f32_16x16x32_bf16(a, b, c, 0, 0, 0)

typedef unsigned long long u64;
typedef GAS u64 gu64;
__device__ __forceinline__ void flag_set(unsigned char* ws, size_t off, u64 tag) { __hip_atomic_store((gu64*)(ws + off), tag, __ATOMIC_RELAXED, __HIP_MEMORY_SCOPE_AGENT); }
__device__ __forceinline__ void flag_set8(unsigned char* ws, size_t off, u64 tag, int l) { if (l < 8) flag_set(ws, off + l * REP, tag); }
__device__ __forceinline__ void flag_wait(unsigned char* ws, size_t off, int n, u64 tag, int l) {
  gu64* p = (gu64*)(ws + off) + (l < n ? l : 0);
  for (unsigned spins = 0; spins < (1u << 18); ++spins) {
    const u64 v = __hip_atomic_load(p, __ATOMIC_RELAXED, __HIP_MEMORY_SCOPE_AGENT);
    if (__all(v == tag)) break;
    __builtin_amdgcn_s_sleep(2);
  }
  asm volatile("" ::: "memory");
}

#define STAMP(k) do {} while (0)
#define STAMPT(k, T) do {} while (0)

__device__ __forceinline__ float xsum16_32(float v) {
  const auto a = __builtin_amdgcn_permlane16_swap(__float_as_uint(v), __float_as_uint(v), false, false);
  v = __uint_as_float(a[0]) + __uint_as_float(a[1]);
  const auto b = __builtin_amdgcn_permlane32_swap(__float_as_uint(v), __float_as_uint(v), false, false);
  return __uint_as_float(b[0]) + __uint_as_float(b[1]);
}
__device__ __forceinline__ float xmax16_32(float v) {
  const auto a = __builtin_amdgcn_permlane16_swap(__float_as_uint(v), __float_as_uint(v), false, false);
  v = fmaxf(__uint_as_float(a[0]), __uint_as_float(a[1]));
  const auto b = __builtin_amdgcn_permlane32_swap(__float_as_uint(v), __float_as_uint(v), false, false);
  return fmaxf(__uint_as_float(b[0]), __uint_as_float(b[1]));
}
#define PF_STRIDE 128
constexpr int K1_ASLOT = 16384, K1_BSLOT = 20480, K1_BOFF = 4 * K1_ASLOT, K1_LDS = K1_BOFF + 4 * K1_BSLOT;
constexpr int LDS_PFDUMP = K1_LDS, LDS_LNRED = K1_LDS + 256, LDS_TOTAL = K1_LDS + 256 + 1024, LDS_HALO = 131072;

struct K1Ctx {
  LAS unsigned char* lds;
  unsigned aoff;
  unsigned vb[6];
  unsigned vst0, vst1;
  i32x4 s0, s1, s2;
  unsigned so0, so1;
  unsigned ldst;
};

template <int TYPE> __device__ __forceinline__ void k1_stage_b(const K1Ctx& c, int s, int slot, unsigned cnt, int lim) {
  const unsigned d = K1_BOFF + slot * K1_BSLOT + c.ldst;
  unsigned t0, t1;
  if (TYPE == 0) {
    const unsigned kq = (unsigned)s * (32u * 6144u) + c.so0, kg = (unsigned)s * (32u * 7168u) + c.so0;
    asm volatile(
      "s_cmp_ge_u32 %10, %11\n\ts_cbranch_scc1 .Lsk%=\n\t"
      "s_mov_b32 m0, %9\n\ts_add_u32 %0, %7, 0x800\n\tbuffer_load_dwordx4 %2, %4, %7 offen lds\n\t"
      "s_add_u32 m0, %9, 0x400\n\ts_add_u32 %1, %7, 0x1000\n\tbuffer_load_dwordx4 %2, %4, %0 offen lds\n\t"
      "s_add_u32 m0, %9, 0x800\n\ts_nop 0\n\tbuffer_load_dwordx4 %2, %4, %1 offen lds\n\t"
      "s_add_u32 m0, %9, 0xc00\n\ts_nop 0\n\tbuffer_load_dwordx4 %3, %5, %8 offen lds\n\t"
      "s_add_u32 m0, %9, 0x1000\n\ts_nop 0\n\tbuffer_load_dwordx4 %3, %6, %8 offen lds\n"
      ".Lsk%=:"
      : "=&s"(t0), "=&s"(t1)
      : "v"(c.vst0), "v"(c.vst1), "s"(c.s0), "s"(c.s1), "s"(c.s2), "s"(kq), "s"(kg), "s"(d), "s"(cnt), "i"(lim) : "memory", "scc");
  } else {
    const unsigned k0 = (unsigned)s * (32u * 7168u) + c.so0, k1 = (unsigned)s * (32u * 7168u) + c.so1;
    asm volatile(
      "s_cmp_ge_u32 %10, %11\n\ts_cbranch_scc1 .Lsk%=\n\t"
      "s_mov_b32 m0, %9\n\ts_add_u32 %0, %7, 0x80\n\tbuffer_load_dwordx4 %2, %4, %7 offen lds\n\t"
      "s_add_u32 m0, %9, 0x400\n\ts_nop 0\n\tbuffer_load_dwordx4 %2, %4, %0 offen lds\n\t"
      "s_add_u32 m0, %9, 0x800\n\ts_nop 0\n\tbuffer_load_dwordx4 %2, %5, %7 offen lds\n\t"
      "s_add_u32 m0, %9, 0xc00\n\ts_nop 0\n\tbuffer_load_dwordx4 %2, %5, %0 offen lds\n\t"
      "s_add_u32 m0, %9, 0x1000\n\ts_nop 0\n\tbuffer_load_dwordx4 %3, %6, %8 offen lds\n"
      ".Lsk%=:"
      : "=&s"(t0), "=&s"(t1)
      : "v"(c.vst0), "v"(c.vst1), "s"(c.s0), "s"(c.s1), "s"(c.s2), "s"(k0), "s"(k1), "s"(d), "s"(cnt), "i"(lim) : "memory", "scc");
  }
}
__device__ __forceinline__ void k1_stage_a(const K1Ctx& c, int s, int slot, unsigned cnt, int lim) {
  const unsigned d = slot * K1_ASLOT + c.ldst;
  const unsigned k = (unsigned)s * 1024u + c.so0;
  unsigned t0;
  asm volatile(
    "s_cmp_ge_u32 %5, %6\n\ts_cbranch_scc1 .Lsk%=\n\t"
    "s_mov_b32 m0, %4\n\ts_add_u32 %0, %3, 0x8000\n\tbuffer_load_dwordx4 %1, %2, %3 offen sc1 lds\n\t"
    "s_add_u32 m0, %4, 0x400\n\ts_nop 0\n\tbuffer_load_dwordx4 %1, %2, %0 offen sc1 lds\n\t"
    "s_add_u32 m0, %4, 0x800\n\ts_add_u32 %0, %3, 0x10000\n\tbuffer_load_dwordx4 %1, %2, %0 offen sc1 lds\n\t"
    "s_add_u32 m0, %4, 0xc00\n\ts_add_u32 %0, %3, 0x18000\n\tbuffer_load_dwordx4 %1, %2, %0 offen sc1 lds\n"
    ".Lsk%=:"
    : "=&s"(t0) : "v"(c.vst0), "s"(c.s0), "s"(k), "s"(d), "s"(cnt), "i"(lim) : "memory", "scc");
}
#define WAIT_V2(STEADY, TAIL, cnt, lim) do { asm volatile("s_cmp_lt_u32 %0, %1\n\ts_cbranch_scc1 .Lw%=\n\ts_waitcnt vmcnt(" #TAIL ")\n.Lw%=:\n\ts_waitcnt vmcnt(" #STEADY ")" :: "s"(cnt), "i"(lim) : "memory", "scc"); } while (0)
#define BAR_IF(cnt, lim) do { SCHED(); asm volatile("s_cmp_ge_u32 %0, %1\n\ts_cbranch_scc1 .Lb%=\n\ts_barrier\n.Lb%=:" :: "s"(cnt), "i"(lim) : "memory", "scc"); SCHED(); } while (0)
template <int TYPE> __device__ __forceinline__ void k1_read(const K1Ctx& c, int slot, bf16x8 (&af)[4], bf16x8 (&bfr)[10]) {
  LAS unsigned char* lds = c.lds;
  const int bs = slot * K1_BSLOT;
  if (TYPE == 0) {
#pragma unroll
    for (int j = 0; j < 8; ++j) { LAS unsigned char* p = lds + c.vb[j & 1] + bs + (j >> 1) * 1024; bfr[j] = lds_tr8(p, p + 512); }
#pragma unroll
    for (int j = 0; j < 2; ++j) { LAS unsigned char* p = lds + c.vb[2 + j] + bs; bfr[8 + j] = lds_tr8(p, p + 512); }
  } else {
#pragma unroll
    for (int j = 0; j < 8; ++j) { LAS unsigned char* p = lds + c.vb[j & 3] + bs + (j >> 2) * 2048; bfr[j] = lds_tr8(p, p + 512); }
#pragma unroll
    for (int j = 0; j < 2; ++j) { LAS unsigned char* p = lds + c.vb[4 + j] + bs; bfr[8 + j] = lds_tr8(p, p + 256); }
  }
#pragma unroll
  for (int i = 0; i < 4; ++i) af[i] = *(LAS bf16x8*)(lds + c.aoff + slot * K1_ASLOT + i * 1024);
}
#define K1V 0
#define K1_EARLY 0
template <int TYPE, int N0, int N1> __device__ __forceinline__ void k1_mma(f32x4 (&acc)[4][10], const bf16x8 (&af)[4], const bf16x8 (&bfr)[10]) {
#pragma unroll
  for (int n = N0; n < N1; ++n) {
    const int j = n >> 2, i = n & 3;
    if (TYPE == 0 && (j == 4 || j == 5)) acc[i][j] = MFMA(af[i], bfr[j], acc[i][j]);
    else acc[i][j] = MFMA(bfr[j], af[i], acc[i][j]);
  }
}
__device__ __forceinline__ void k1_pf(unsigned voff, i32x4 srd, unsigned soff, unsigned on, unsigned cnt, int lim) {
  asm volatile("s_cmp_lg_u32 %3, 4\n\ts_cbranch_scc1 .Lpf%=\n\ts_cmp_ge_u32 %4, %5\n\ts_cbranch_scc1 .Lpf%=\n\t"
               "s_mov_b32 m0, %6\n\ts_nop 0\n\tbuffer_load_dword %0, %1, %2 offen lds\n.Lpf%=:"
               ::"v"(voff), "s"(srd), "s"(soff), "s"(on), "s"(cnt), "i"(lim), "i"(LDS_PFDUMP) : "memory", "scc");
}
#define WAIT_A(PFN, PFT, PLN, PLT, pf, cnt, lim) do { asm volatile( \
    "s_cmp_lt_u32 %1, %2\n\ts_cbranch_scc1 .Ls%=\n\t" \
    "s_cmp_eq_u32 %0, 4\n\ts_cbranch_scc1 .Lt%=\n\ts_waitcnt vmcnt(" #PLT ")\n.Lt%=:\n\ts_waitcnt vmcnt(" #PFT ")\n" \
    ".Ls%=:\n\ts_cmp_eq_u32 %0, 4\n\ts_cbranch_scc1 .Lu%=\n\ts_waitcnt vmcnt(" #PLN ")\n.Lu%=:\n\ts_waitcnt vmcnt(" #PFN ")" \
    :: "s"(pf), "s"(cnt), "i"(lim) : "memory", "scc"); } while (0)

constexpr int K2_SLOT = 16384;
constexpr int K2_TP = P_STEPS, K2_TQ = KS2 - P_STEPS;
static_assert(K2_TP % 8 == 0 && K2_TQ % 8 == 0 && K2_TP >= 48, "step counts must be multiples of 8");
constexpr unsigned K2_H0 = K2_TP - 32;
__device__ __forceinline__ unsigned k2_vstb(int w4, int l) { const unsigned r8 = l >> 3, ch = (l & 7) ^ (((r8 >> 1) & 3) << 1); return (8 * w4 + r8) * 2048 + ch * 16; }
__device__ __forceinline__ void k2_prologue_b(int w4, int l, const u16* wd, bool isp, int ct) {
  const i32x4 sb = make_srd(wd, F_ * D_ * 2);
  const unsigned vb = k2_vstb(w4, l), k0 = (isp ? 0u : K2_H0) * 65536u + ct * 256;
#pragma unroll
  for (int t = 0; t < 8; ++t) { glds16(vb, sb, k0 + t * 65536u, t * K2_SLOT + 8192 + w4 * 2048); glds16(vb, sb, k0 + t * 65536u + 128u, t * K2_SLOT + 8192 + w4 * 2048 + 1024); }
}

template <int TYPE> __device__ __forceinline__ u64 k1_body(LAS unsigned char* lds, const u16* x, const u16* wqkv, const u16* wg, const u16* wu, const u16* wo, const u16* wd, unsigned char* ws, int pm, int pa  , int lnmf, bool isp, int ct) {
  const int tid = threadIdx.x;
  const int w = __builtin_amdgcn_readfirstlane(tid >> 6), l = tid & 63;
  const int grp = w >> 2, wr = w & 3;
  const int g = l >> 4, q = (l & 15) >> 2, p = l & 3;
  STAMP(0);
  K1Ctx c; c.lds = lds;
  c.aoff = l * 16 + wr * 4096;
  {
    const unsigned s2 = ((q >> 1) & 1) | ((g & 1) << 1);
    const unsigned lanep = K1_BOFF + g * 5120 + q * 128 + (p >> 1) * 16 + (p & 1) * 8;
    if (TYPE == 0) {
      c.vb[0] = lanep + (((2 * grp) ^ s2) << 5); c.vb[1] = lanep + (((2 * grp + 1) ^ s2) << 5);
      c.vb[2] = c.vb[0] + 4096; c.vb[3] = c.vb[1] + 4096; c.vb[4] = 0; c.vb[5] = 0;
    } else {
#pragma unroll
      for (int j = 0; j < 4; ++j) c.vb[j] = lanep + ((j ^ s2) << 5) + grp * 1024;
      const unsigned mx = K1_BOFF + (2 * (g >> 1) * 5 + 4) * 1024 + (g & 1) * 512 + q * 64 + (((2 * grp + (p >> 1)) ^ ((g & 1) << 1)) << 4) + (p & 1) * 8;
      c.vb[4] = mx; c.vb[5] = mx + 5120;
    }
  }
  const int c0 = 1024 + 160 * pa;
  const int odd = pa & 1;
  if (grp == 0) {
    const unsigned r8 = l >> 3, sw = (((r8 >> 1) & 1) | ((wr & 1) << 1)) << 1, ch = (l & 7) ^ sw;
    c.ldst = wr * 5120;
    if (TYPE == 0) {
      c.vst0 = (8 * wr + r8) * 6144 + ch * 16; c.vst1 = (8 * wr + r8) * 7168 + ch * 16;
      c.s0 = make_srd(wqkv, D_ * 3 * D_ * 2); c.s1 = make_srd(wg, D_ * F_ * 2); c.s2 = make_srd(wu, D_ * F_ * 2);
      c.so0 = pa * 128; c.so1 = 0;
    } else {
      c.vst0 = (8 * wr + r8) * 7168 + ch * 16;
      const unsigned r16 = l >> 2, chm = (l & 3) ^ (((r16 >> 3) & 1) << 1);
      c.vst1 = (16 * (wr >> 1) + r16) * 7168 + chm * 16;
      c.s0 = make_srd(wg, D_ * F_ * 2); c.s1 = make_srd(wu, D_ * F_ * 2); c.s2 = make_srd((wr & 1) ? wu : wg, D_ * F_ * 2);
      c.so0 = (c0 + (odd ? 32 : 0)) * 2; c.so1 = (c0 + (odd ? 0 : 128)) * 2;
    }
  } else {
    c.vst0 = l * 16; c.vst1 = 0;
    c.s0 = make_srd(ws + WS_XN, S_ * D_ * 2); c.s1 = c.s0; c.s2 = c.s0;
    c.so0 = (pm * 16 + 4 * wr) * 32768; c.so1 = 0;
    c.ldst = wr * 4096;
  }

  u32x4 xv[4]; float piv = 0.f;
  if (lnmf >= 0) {
    const u16* row = x + (size_t)(lnmf * 16 + (l & 15)) * D_;
#pragma unroll
    for (int i = 0; i < 4; ++i) xv[i] = *(const u32x4*)(row + w * 128 + g * 8 + i * 32);
    piv = __uint_as_float((unsigned)row[0] << 16);
  }
  unsigned glo, ghi;
  if (grp == 0 && lnmf < 0) {
    u32x2 gen;
    asm volatile("global_load_dwordx2 %0, %1, off sc1" : "=&v"(gen) : "v"(ws + WS_GEN) : "memory");
    k1_stage_b<TYPE>(c, 0, 0, 0, 1); k1_stage_b<TYPE>(c, 1, 1, 0, 1); k1_stage_b<TYPE>(c, 2, 2, 0, 1);
    asm volatile("s_waitcnt vmcnt(15)" : "+v"(gen) :: "memory");
    glo = __builtin_amdgcn_readfirstlane(gen.x); ghi = __builtin_amdgcn_readfirstlane(gen.y);
  } else {
    u32x2 gen;
    asm volatile("global_load_dwordx2 %0, %1, off sc1\n\ts_waitcnt vmcnt(0)" : "=&v"(gen) : "v"(ws + WS_GEN) : "memory");
    glo = __builtin_amdgcn_readfirstlane(gen.x); ghi = __builtin_amdgcn_readfirstlane(gen.y);
  }
  const u64 tag = (((u64)ghi << 32) | glo) + 1;

  if (lnmf >= 0) {
    const int m = l & 15;
    const u32x4 (&v)[4] = xv;
    float f[32]; float sm = 0.f, sq = 0.f;
#pragma unroll
    for (int i = 0; i < 4; ++i)
#pragma unroll
      for (int j = 0; j < 4; ++j) {
        const float a = bf_lo(v[i][j]), b = bf_hi(v[i][j]); f[i * 8 + 2 * j] = a; f[i * 8 + 2 * j + 1] = b;
        const float da = a - piv, db = b - piv; sm += da + db; sq += da * da + db * db;
      }
    sm = xsum16_32(sm); sq = xsum16_32(sq);
    STAMPT(8, 0);
    LAS f32x2* red = (LAS f32x2*)(lds + LDS_LNRED);
    if (g == 0) red[w * 16 + m] = (f32x2){sm, sq};
    WAIT_L(0); BAR();
    float st = 0.f, qt = 0.f;
#pragma unroll
    for (int i = 0; i < 8; ++i) { const f32x2 r = red[i * 16 + m]; st += r.x; qt += r.y; }
    const float md = st * (1.0f / D_), mean = piv + md, var = fmaxf(qt * (1.0f / D_) - md * md, 0.f);
    const float mb = rbf(mean);
    const float inv = 1.0f / rbf(sqrtf(rbf(rbf(var) + 1e-5f)));
    const __amdgpu_buffer_rsrc_t xr = __builtin_amdgcn_make_buffer_rsrc(ws + WS_XN, 0, S_ * D_ * 2, 0x00020000);
#pragma unroll
    for (int i = 0; i < 4; ++i) {
      u32x4 o;
#pragma unroll
      for (int j = 0; j < 4; ++j) o[j] = cvt_pk(rbf(f[i * 8 + 2 * j] - mb) * inv, rbf(f[i * 8 + 2 * j + 1] - mb) * inv);
      __builtin_amdgcn_raw_buffer_store_b128(o, xr, (lnmf * 32 + w * 4 + i) * 1024 + l * 16, 0, 16);
    }
    STAMPT(9, 0);
    WAIT_V(0); BAR();
    if (w == 0) flag_set8(ws, WS_LNF + lnmf * 8, tag, l);
    STAMPT(10, 0);
    if (grp == 0) { k1_stage_b<TYPE>(c, 0, 0, 0, 1); k1_stage_b<TYPE>(c, 1, 1, 0, 1); k1_stage_b<TYPE>(c, 2, 2, 0, 1); }
  }
  if (w == 4) flag_wait(ws, WS_LNF + (blockIdx.x & 7) * REP + pm * 128, 16, tag, l);
  BAR();

  f32x4 acc[4][10];
#pragma unroll
  for (int i = 0; i < 4; ++i)
#pragma unroll
    for (int j = 0; j < 10; ++j) acc[i][j] = (f32x4){0.f, 0.f, 0.f, 0.f};
  bf16x8 af[4], bfr[10];

  if (grp == 0) {
    WAIT_V(10); BAR();
    STAMP(1);
#define K1_G0_STEP(J, TAILV) do { \
      k1_read<TYPE>(c, J, af, bfr); SCHED(); \
      k1_stage_b<TYPE>(c, s + (J) + 3, ((J) + 3) & 3, s, (J) == 0 ? 32 : 28); \
      WAIT_L(0); BAR(); \
      __builtin_amdgcn_s_setprio(1); \
      k1_mma<TYPE, 0, 40 - K1_EARLY>(acc, af, bfr); \
      if (K1V & 1) __builtin_amdgcn_s_setprio(0); \
      SCHED(); WAIT_V2(10, TAILV, s, 28); \
      BAR(); \
      if (K1_EARLY) __builtin_amdgcn_s_setprio(3); \
      k1_mma<TYPE, 40 - K1_EARLY, 40>(acc, af, bfr); \
      __builtin_amdgcn_s_setprio(0); SCHED(); } while (0)
#pragma nounroll
    for (unsigned s = 0; s < 32; s += 4) {
      K1_G0_STEP(0, 10); K1_G0_STEP(1, 5); K1_G0_STEP(2, 0); K1_G0_STEP(3, 0);
    }
  } else {
#define PF_ON 1
#define K1_PF_ROW0 96
    const unsigned pf = PF_ON ? w : 99  , pfv = (blockIdx.x * 64 + l) * PF_STRIDE;
    const i32x4 sg = make_srd(wg, D_ * F_ * 2), su = make_srd(wu, D_ * F_ * 2), sq = make_srd(wqkv, D_ * 3 * D_ * 2), sd = make_srd(wd, F_ * D_ * 2), so = make_srd(wo, D_ * D_ * 2);
    constexpr unsigned OOR = 0x7f000000u, MB = 16384u * PF_STRIDE;
    constexpr unsigned NG = ((1024 - K1_PF_ROW0) * 7168u + MB - 1) / MB, NQ = ((1024 - K1_PF_ROW0) * 6144u + MB - 1) / MB, ND = (F_ * D_ * 2 + MB - 1) / MB, NO = (D_ * D_ * 2 + MB - 1) / MB;
    static_assert(NG + (ND + 1) / 2 <= 8 && NG + ND / 2 <= 7 && NQ + NO <= 7, "prefetch schedule does not fit");
    STAMPT(11, 256);
    if (!(K1V & 2)) k1_pf(pfv, sq, OOR, pf, 0, 1);
    k1_stage_a(c, 0, 0, 0, 1);
    if (!(K1V & 2)) k1_pf(pfv, sq, OOR, pf, 0, 1);
    k1_stage_a(c, 1, 1, 0, 1);
    k1_stage_a(c, 2, 2, 0, 1);
    if (K1V & 2) WAIT_V(8); else WAIT_A(9, 9, 8, 8, pf, 0u, 1);
    BAR();
    BAR();
#define K1_G1_STEP(J, PFN, PFT, PLT) do { \
      k1_read<TYPE>(c, J, af, bfr); SCHED(); \
      if (!(K1V & 2) && (J) == 0) k1_pf(pfv, it < NG ? sg : sd, it < NG ? K1_PF_ROW0 * 7168u + it * MB : (it - NG) * MB, pf, s, 32); \
      if (!(K1V & 2) && (J) == 1) k1_pf(pfv, it < NG ? su : sd, it < NG ? K1_PF_ROW0 * 7168u + it * MB : (it - NG + (ND + 1) / 2) * MB, pf, s, 28); \
      if (!(K1V & 2) && (J) == 2) k1_pf(pfv, it < NQ ? sq : so, it < NQ ? K1_PF_ROW0 * 6144u + it * MB : (it - NQ) * MB, pf, s, 28); \
      k1_stage_a(c, s + (J) + 3, ((J) + 3) & 3, s, (J) == 0 ? 32 : 28); \
      WAIT_L(0); if (K1V & 2) WAIT_V2(8, PLT, s, 28); else WAIT_A(PFN, PFT, 8, PLT, pf, s, 28); \
      BAR(); \
      __builtin_amdgcn_s_setprio(1); \
      k1_mma<TYPE, 0, 40 - K1_EARLY>(acc, af, bfr); \
      if (K1V & 1) __builtin_amdgcn_s_setprio(0); \
      if ((J) < 3) BAR(); else BAR_IF(s, 28); \
      if (K1_EARLY) __builtin_amdgcn_s_setprio(3); \
      k1_mma<TYPE, 40 - K1_EARLY, 40>(acc, af, bfr); \
      __builtin_amdgcn_s_setprio(0); SCHED(); } while (0)
#pragma nounroll
    for (unsigned s = 0; s < 32; s += 4) {
      const unsigned it = s >> 2;
      K1_G1_STEP(0, 9, 9, 8); K1_G1_STEP(1, 10, 5, 4); K1_G1_STEP(2, 10, 0, 0); K1_G1_STEP(3, 9, 0, 0);
    }
    k2_prologue_b(wr, l, wd, isp, ct);
  }
  STAMP(2);

  const __amdgpu_buffer_rsrc_t a2 = __builtin_amdgcn_make_buffer_rsrc(ws + WS_A2, 0, 128 * KS2 * 1024, 0x00020000);
  const unsigned mf0 = pm * 16 + 4 * wr;
  if (TYPE == 1) {
    const unsigned ksf = c0 / 32 + (odd ? 1 : 0) + 2 * grp, ksm = c0 / 32 + (odd ? 0 : 4);
#pragma unroll
    for (int i = 0; i < 4; ++i) {
      const unsigned base = (mf0 + i) * (KS2 * 1024);
#pragma unroll
      for (int t = 0; t < 2; ++t) {
        f32x4 h0, h1;
#pragma unroll
        for (int r = 0; r < 4; ++r) { h0[r] = silu_f(acc[i][2 * t][r]) * acc[i][2 * t + 4][r]; h1[r] = silu_f(acc[i][2 * t + 1][r]) * acc[i][2 * t + 5][r]; }
        __builtin_amdgcn_raw_buffer_store_b128(__builtin_bit_cast(u32x4, pack8(h0, h1)), a2, l * 16 + base + (ksf + t) * 1024, 0, ST_AUX);
      }
      f32x4 h0;
#pragma unroll
      for (int r = 0; r < 4; ++r) h0[r] = silu_f(acc[i][8][r]) * acc[i][9][r];
      u32x2 o; o.x = cvt_pk(h0[0], h0[1]); o.y = cvt_pk(h0[2], h0[3]);
      *(LAS u32x2*)(lds + LDS_HALO + (wr * 4 + i) * 1024 + l * 16 + grp * 8) = o;
    }
    WAIT_L(0); BAR();
#pragma unroll
    for (int ii = 0; ii < 2; ++ii) {
      const int i = 2 * grp + ii;
      const u32x4 v = *(LAS u32x4*)(lds + LDS_HALO + (wr * 4 + i) * 1024 + l * 16);
      __builtin_amdgcn_raw_buffer_store_b128(v, a2, l * 16 + (mf0 + i) * (KS2 * 1024) + ksm * 1024, 0, ST_AUX);
    }
  } else {
    const int head = 2 * pa + grp;
    bf16x8 kop[5]; u32x2 vp[5][2];
#pragma unroll
    for (int i = 0; i < 4; ++i) {
      kop[i + 1] = pack8(acc[i][2], acc[i][3]);
#pragma unroll
      for (int d = 0; d < 2; ++d) { vp[i + 1][d].x = cvt_pk(acc[i][4 + d][0], acc[i][4 + d][1]); vp[i + 1][d].y = cvt_pk(acc[i][4 + d][2], acc[i][4 + d][3]); }
    }
    const u32x4 klast = __builtin_bit_cast(u32x4, kop[4]);
    const u32x4 vlast = (u32x4){vp[4][0].x, vp[4][0].y, vp[4][1].x, vp[4][1].y};
    const __amdgpu_buffer_rsrc_t hr = __builtin_amdgcn_make_buffer_rsrc(ws + WS_HALO, 0, 8 * 32 * 2048, 0x00020000);
    const bool send = (wr == 3) && (pm < 7);
    if (send) {
      const unsigned ho = (pm * 32 + head) * 2048;
      __builtin_amdgcn_raw_buffer_store_b128(klast, hr, l * 16 + ho, 0, 16);
      __builtin_amdgcn_raw_buffer_store_b128(vlast, hr, l * 16 + ho + 1024, 0, 16);
    }
    if (wr != 3) {
      *(LAS u32x4*)(lds + LDS_HALO + w * 2048 + l * 16) = klast;
      *(LAS u32x4*)(lds + LDS_HALO + w * 2048 + 1024 + l * 16) = vlast;
    }
    bf16x8 qop[4];
#pragma unroll
    for (int i = 0; i < 4; ++i) qop[i] = pack8(acc[i][0], acc[i][1]);
#pragma unroll
    for (int i = 0; i < 4; ++i) {
      f32x4 h0, h1;
#pragma unroll
      for (int r = 0; r < 4; ++r) { h0[r] = silu_f(acc[i][6][r]) * acc[i][8][r]; h1[r] = silu_f(acc[i][7][r]) * acc[i][9][r]; }
      __builtin_amdgcn_raw_buffer_store_b128(__builtin_bit_cast(u32x4, pack8(h0, h1)), a2, l * 16 + (mf0 + i) * (KS2 * 1024) + (2 * pa + grp) * 1024, 0, ST_AUX);
    }
    WAIT_L(0); BAR();
    const int m = l & 15; const bool par = m & 1;
    const bool own0 = 4 * g + (m & 1) <= m, own1 = 4 * g + (m & 1) + 2 <= m;
    const unsigned hmask = par ? 0xffff0000u : 0x0000ffffu;
    const float scale = 0.17677669529663687f * 1.44269504089f;
    auto attend = [&](int i, bool have_prev) {
      const f32x4 z = (f32x4){0.f, 0.f, 0.f, 0.f};
      const f32x4 s0 = MFMA(kop[i + 1], qop[i], z);
      const f32x4 s1 = MFMA(kop[i], qop[i], z);
      float l0 = own0 ? (par ? s0[1] : s0[0]) : (par ? s1[1] : s1[0]);
      float l1 = own1 ? (par ? s0[3] : s0[2]) : (par ? s1[3] : s1[2]);
      l0 *= scale; l1 *= scale;
      if (!have_prev) { l0 = own0 ? l0 : -3.0e38f; l1 = own1 ? l1 : -3.0e38f; }
      const float mx = xmax16_32(fmaxf(l0, l1));
      const float e0 = __builtin_amdgcn_exp2f(l0 - mx), e1 = __builtin_amdgcn_exp2f(l1 - mx);
      const float inv = __builtin_amdgcn_rcpf(xsum16_32(e0 + e1));
      const unsigned w0 = cvt_pk(e0 * inv, e0 * inv) & hmask, w1 = cvt_pk(e1 * inv, e1 * inv) & hmask;
      const u32x4 pw = (u32x4){own0 ? 0u : w0, own1 ? 0u : w1, own0 ? w0 : 0u, own1 ? w1 : 0u};
      const bf16x8 pop = __builtin_bit_cast(bf16x8, pw);
      f32x4 o[2];
#pragma unroll
      for (int d = 0; d < 2; ++d) {
        const u32x4 vv = (u32x4){vp[i][d].x, vp[i][d].y, vp[i + 1][d].x, vp[i + 1][d].y};
        o[d] = MFMA(__builtin_bit_cast(bf16x8, vv), pop, z);
      }
      __builtin_amdgcn_raw_buffer_store_b128(__builtin_bit_cast(u32x4, pack8(o[0], o[1])), a2, l * 16 + (mf0 + i) * (KS2 * 1024) + (112 + head) * 1024, 0, ST_AUX);
    };
    attend(1, true); attend(2, true); attend(3, true);
    STAMPT(12, 0);
    WAIT_V(3);
    if (send && l == 0) flag_set(ws, WS_HLF + (pm * 32 + head) * 64, tag);
    BAR();
    if (w == 0) flag_set8(ws, WS_T1A + (pm * 16 + pa) * 8, tag, l);
    STAMPT(13, 0);
    bool have_prev = true;
    u32x4 kh, vh;
    if (wr == 0) {
      if (pm > 0) {
        flag_wait(ws, WS_HLF + ((pm - 1) * 32 + head) * 64, 1, tag, l);
        const unsigned ho = ((pm - 1) * 32 + head) * 2048;
        kh = __builtin_amdgcn_raw_buffer_load_b128(hr, l * 16 + ho, 0, 16);
        vh = __builtin_amdgcn_raw_buffer_load_b128(hr, l * 16 + ho + 1024, 0, 16);
      } else { kh = (u32x4){0u, 0u, 0u, 0u}; vh = kh; have_prev = false; }
    } else {
      kh = *(LAS u32x4*)(lds + LDS_HALO + (w - 1) * 2048 + l * 16);
      vh = *(LAS u32x4*)(lds + LDS_HALO + (w - 1) * 2048 + 1024 + l * 16);
    }
    kop[0] = __builtin_bit_cast(bf16x8, kh);
    vp[0][0] = (u32x2){vh.x, vh.y}; vp[0][1] = (u32x2){vh.z, vh.w};
    attend(0, have_prev);
  }
  STAMPT(14, 0);
  WAIT_V(0); BAR();
  if (w == 0) flag_set8(ws, WS_T1F + (pm * 32 + TYPE * 16 + pa) * 8, tag, l);
  STAMP(3);
  return tag;
}

struct K2Frag { bf16x8 a[4], b[4]; };
struct K2Rd { LAS unsigned char* lds; unsigned aoff[2], vb[2][4]; };
template <int SLOT> __device__ __forceinline__ void k2_read(const K2Rd& c, K2Frag& f) {
  constexpr int H = SLOT >> 2, O = (SLOT & 3) * K2_SLOT;
#pragma unroll
  for (int j = 0; j < 4; ++j) { LAS unsigned char* p = c.lds + c.vb[H][j] + O; f.b[j] = lds_tr8(p, p + 4096); }
#pragma unroll
  for (int i = 0; i < 4; ++i) f.a[i] = *(LAS bf16x8*)(c.lds + c.aoff[H] + O + i * 1024);
}
template <int SLOTN> __device__ __forceinline__ void k2_step(const K2Rd& c, f32x4 (&acc)[4][4], const K2Frag& cur, K2Frag& nxt) {
  k2_read<SLOTN>(c, nxt);
#pragma unroll
  for (int j = 0; j < 4; ++j)
#pragma unroll
    for (int i = 0; i < 4; ++i) acc[i][j] = MFMA(cur.b[j], cur.a[i], acc[i][j]);
#pragma unroll
  for (int k = 0; k < 6; ++k) { __builtin_amdgcn_sched_group_barrier(0x8, 1, 0); __builtin_amdgcn_sched_group_barrier(0x100, 2, 0); }
  __builtin_amdgcn_sched_group_barrier(0x8, 10, 0);
  SCHED(); __builtin_amdgcn_s_waitcnt(0xC07F); asm volatile("" ::: "memory"); BAR();
}
__device__ __forceinline__ void k2_consume(const K2Rd& c, f32x4 (&acc)[4][4], K2Frag& f0, K2Frag& f1, int iters) {
#pragma nounroll
  for (int it = 0; it < iters; ++it) {
    k2_step<1>(c, acc, f0, f1); k2_step<2>(c, acc, f1, f0); k2_step<3>(c, acc, f0, f1); k2_step<4>(c, acc, f1, f0);
    k2_step<5>(c, acc, f0, f1); k2_step<6>(c, acc, f1, f0); k2_step<7>(c, acc, f0, f1); k2_step<0>(c, acc, f1, f0);
  }
}

struct K2Ld { unsigned vsta, vstb; i32x4 sa; unsigned ldst; };
__device__ __forceinline__ void k2_stage(const K2Ld& c, i32x4 sb, unsigned ka, unsigned kb, int slot) {
  const unsigned d = slot * K2_SLOT + c.ldst;
  unsigned t0;
  asm volatile(
    "s_mov_b32 m0, %7\n\ts_add_u32 %0, %5, 0x24000\n\tbuffer_load_dwordx4 %1, %3, %5 offen sc1 lds\n\t"
    "s_add_u32 m0, %7, 0x400\n\ts_nop 0\n\tbuffer_load_dwordx4 %1, %3, %0 offen sc1 lds\n\t"
    "s_add_u32 m0, %7, 0x2000\n\ts_add_u32 %0, %6, 0x80\n\tbuffer_load_dwordx4 %2, %4, %6 offen lds\n\t"
    "s_add_u32 m0, %7, 0x2400\n\ts_nop 0\n\tbuffer_load_dwordx4 %2, %4, %0 offen lds"
    : "=&s"(t0) : "v"(c.vsta), "v"(c.vstb), "s"(c.sa), "s"(sb), "s"(ka), "s"(kb), "s"(d) : "memory", "scc");
}
__device__ __forceinline__ void k2_load(const K2Ld& c, i32x4 sb, unsigned ka, unsigned kb, int iters) {
#pragma nounroll
  for (int it = 0; it < iters; ++it) {
#pragma unroll
    for (int j = 0; j < 8; ++j) {
      k2_stage(c, sb, ka, kb, j);
      ka += 1024u; kb += 65536u;
      WAIT_V(24); BAR();
    }
  }
}
__device__ __forceinline__ bool k2_load_poll(const K2Ld& c, i32x4 sb, unsigned ka, unsigned kb, const unsigned char* fl, int n, u64 tag, int l) {
  u32x2 pv;
  const unsigned char* fp = fl + (l < n ? l : 0) * 8;
#pragma unroll
  for (int j = 0; j < 8; ++j) {
    if (j == 1) asm volatile("global_load_dwordx2 %0, %1, off sc1" : "=&v"(pv) : "v"(fp) : "memory");
    k2_stage(c, sb, ka, kb, j);
    ka += 1024u; kb += 65536u;
    WAIT_V(24);
    if (j < 7) BAR();
  }
  asm volatile("" : "+v"(pv));
  return __all((((u64)pv.y << 32) | pv.x) == tag);
}
template <int AUX> __device__ __forceinline__ void glds16x(unsigned voff, i32x4 srd, unsigned soff, unsigned ldsaddr) {
  if (AUX == 16) asm volatile("s_mov_b32 m0, %3\n\ts_nop 0\n\tbuffer_load_dwordx4 %0, %1, %2 offen sc1 lds" ::"v"(voff), "s"(srd), "s"(soff), "s"(ldsaddr) : "memory");
  else asm volatile("s_mov_b32 m0, %3\n\ts_nop 0\n\tbuffer_load_dwordx4 %0, %1, %2 offen lds" ::"v"(voff), "s"(srd), "s"(soff), "s"(ldsaddr) : "memory");
}

template <bool ISP> __device__ __forceinline__ void k2_body(LAS unsigned char* lds, const u16* x, const u16* wo, const u16* wd, u16* out, unsigned char* ws, int rt, int ct, u64 tag) {
  constexpr int NT = ISP ? K2_TP : K2_TQ;
  int tid = threadIdx.x; asm volatile("" : "+v"(tid));
  const int w = __builtin_amdgcn_readfirstlane(tid >> 6), l = tid & 63;
  const int w4 = w & 3, wm = w4 >> 1, wn = w4 & 1;
  const int g = l >> 4, q = (l & 15) >> 2, p = l & 3;
  const int tile = rt * 8 + ct;
  if (w >= 4) {
    K2Ld c;
    c.vsta = l * 16; c.vstb = k2_vstb(w4, l);
    c.sa = make_srd(ws + WS_A2, 128 * KS2 * 1024);
    const i32x4 swd = make_srd(wd, F_ * D_ * 2), swo = make_srd(wo, D_ * D_ * 2);
    const unsigned soa = (rt * 8 + 2 * w4) * (KS2 * 1024), sob = ct * 256;
    c.ldst = w4 * 2048;
    const size_t rep = (blockIdx.x & 7) * REP, t1f = WS_T1F + rep + (rt >> 1) * 256;
    if (w == 4) { if (ISP) flag_wait(ws, WS_T1A + rep + (rt >> 1) * 128, 16, tag, l); else flag_wait(ws, t1f + 128, 16, tag, l); }
    BAR();
    const unsigned ks0 = ISP ? 0u : K2_H0, ka0 = ks0 * 1024u + soa;
#pragma unroll
    for (int t = 0; t < 8; ++t) { glds16x<16>(c.vsta, c.sa, ka0 + t * 1024u, t * K2_SLOT + c.ldst); glds16x<16>(c.vsta, c.sa, ka0 + t * 1024u + 0x24000u, t * K2_SLOT + c.ldst + 1024); }
    WAIT_V(12); BAR();
    BAR();
    {
      const unsigned ka = ka0 + 8 * 1024u, kb = (ks0 + 8) * 65536u + sob;
#define K2_FIRST(J, N) do { k2_stage(c, swd, ka + (J) * 1024u, kb + (J) * 65536u, J); WAIT_V(N); BAR(); } while (0)
      K2_FIRST(0, 14); K2_FIRST(1, 16); K2_FIRST(2, 18); K2_FIRST(3, 20); K2_FIRST(4, 22); K2_FIRST(5, 24); K2_FIRST(6, 24); K2_FIRST(7, 24);
    }
    const i32x4 sx = make_srd(x, S_ * D_ * 2), sp = make_srd(ws + WS_PART, 128 * 32768);
    if (ISP) {
      static_assert(K2_TP >= 72 && K2_TP <= 96, "P schedule: 32 h + 32 o + 8..32 h");
      k2_load(c, swd, 16 * 1024u + soa, 16 * 65536u + sob, 1);
      if (!k2_load_poll(c, swd, 24 * 1024u + soa, 24 * 65536u + sob, ws + t1f, 16, tag, l)) flag_wait(ws, t1f, 16, tag, l);
      BAR();
      k2_load(c, swo, 112 * 1024u + soa, sob, 3);
      if (!k2_load_poll(c, swo, (112 + 24) * 1024u + soa, 24 * 65536u + sob, ws + t1f + 128, 5, tag, l)) flag_wait(ws, t1f + 128, 5, tag, l);
      BAR();
      k2_load(c, swd, 32 * 1024u + soa, 32 * 65536u + sob, (NT - 64) / 8 - 1);
      const bool early = k2_load_poll(c, swd, (NT - 40) * 1024u + soa, (NT - 40) * 65536u + sob, ws + WS_PTF + tile * 64, 4, tag, l);
      BAR();
      const unsigned xv = ((rt * 128 + wm * 64 + (l & 15)) * D_ + ct * 128 + wn * 64 + (((g & 1) << 1) | (g >> 1)) * 8) * 2;
#pragma unroll
      for (int k = 0; k < 2; ++k) {
#pragma unroll
        for (int e = 0; e < 4; ++e) { const int blk = k * 4 + e; glds16x<0>(xv, sx, (blk >> 1) * 16 * D_ * 2 + (blk & 1) * 64, k * 16384 + w4 * 4096 + e * 1024); }
        WAIT_V(24); BAR();
      }
      if (early) {
#pragma unroll
        for (int k = 2; k < 4; ++k) {
#pragma unroll
          for (int e = 0; e < 4; ++e) glds16x<16>(l * 16, sp, tile * 32768 + w4 * 8192 + ((k - 2) * 4 + e) * 1024, k * 16384 + w4 * 4096 + e * 1024);
          WAIT_V(24); BAR();
        }
        WAIT_V(20); BAR(); WAIT_V(16); BAR(); BAR(); WAIT_V(0); BAR();
      } else {
        WAIT_V(20); BAR(); WAIT_V(16); BAR(); WAIT_V(12); BAR();
        WAIT_V(0);
        flag_wait(ws, WS_PTF + tile * 64, 4, tag, l);
#pragma unroll
        for (int e = 0; e < 8; ++e) glds16x<16>(l * 16, sp, tile * 32768 + w4 * 8192 + e * 1024, (2 + (e >> 2)) * 16384 + w4 * 4096 + (e & 3) * 1024);
        BAR(); BAR();
        WAIT_V(0); BAR();
      }
    } else {
      k2_load(c, swd, (K2_H0 + 16) * 1024u + soa, (K2_H0 + 16) * 65536u + sob, (NT - 16) / 8);
      WAIT_V(20); BAR(); WAIT_V(16); BAR(); WAIT_V(12); BAR(); WAIT_V(8); BAR(); WAIT_V(4); BAR(); WAIT_V(0); BAR(); BAR(); BAR();
    }
    if (blockIdx.x == 0 && w == 5) {
#pragma unroll
      for (int k = 0; k < 4; ++k) flag_wait(ws, WS_T1F + k * 512, 64, tag, l);
      if (l == 0) flag_set(ws, WS_GEN, tag);
    }
    return;
  }
  K2Rd c; c.lds = lds;
  {
    const unsigned row = 4 * (g & 1) + q, s2 = (row >> 1) & 3;
    const unsigned lanep = 8192 + (g >> 1) * 2048 + wn * 1024 + row * 128 + (p >> 1) * 16 + (p & 1) * 8;
#pragma unroll
    for (int h = 0; h < 2; ++h) {
      c.aoff[h] = l * 16 + wm * 4096 + h * 65536;
#pragma unroll
      for (int j = 0; j < 4; ++j) c.vb[h][j] = lanep + ((j ^ s2) << 5) + h * 65536;
    }
  }
  f32x4 accf[4][4], accs[4][4];
#pragma unroll
  for (int i = 0; i < 4; ++i)
#pragma unroll
    for (int j = 0; j < 4; ++j) { accf[i][j] = (f32x4){0.f, 0.f, 0.f, 0.f}; accs[i][j] = accf[i][j]; }
  K2Frag f0, f1;
  BAR();
  BAR();
  STAMP(4);
  k2_read<0>(c, f0);
  WAIT_L(0); BAR();
  if (ISP) { k2_consume(c, accf, f0, f1, 4); k2_consume(c, accs, f0, f1, 4); k2_consume(c, accf, f0, f1, (NT - 64) / 8); }
  else k2_consume(c, accf, f0, f1, NT / 8);
  STAMP(5);
  if (!ISP) {
    const __amdgpu_buffer_rsrc_t pr = __builtin_amdgcn_make_buffer_rsrc(ws + WS_PART, 0, 128 * 32768, 0x00020000);
    const unsigned po = tile * 32768 + w4 * 8192 + l * 16;
#pragma unroll
    for (int i = 0; i < 4; ++i)
#pragma unroll
      for (int jp = 0; jp < 2; ++jp) __builtin_amdgcn_raw_buffer_store_b128(__builtin_bit_cast(u32x4, pack8(accf[i][2 * jp], accf[i][2 * jp + 1])), pr, po + (i * 2 + jp) * 1024, 0, 16);
    WAIT_V(0);
    if (l == 0) flag_set(ws, WS_PTF + tile * 64 + w4 * 8, tag);
  } else {
    const int m = l & 15;
    u16* orow = out + (size_t)(rt * 128 + wm * 64 + m) * D_ + ct * 128 + wn * 64 + (((g & 1) << 1) | (g >> 1)) * 8;
    u32x4 pt[8], xr[8];
#pragma unroll
    for (int b = 0; b < 8; ++b) {
      pt[b] = *(LAS u32x4*)(lds + (2 + (b >> 2)) * 16384 + w4 * 4096 + (b & 3) * 1024 + l * 16);
      xr[b] = *(LAS u32x4*)(lds + (b >> 2) * 16384 + w4 * 4096 + (b & 3) * 1024 + l * 16);
    }
#pragma unroll
    for (int i = 0; i < 4; ++i)
#pragma unroll
      for (int jp = 0; jp < 2; ++jp) {
        const int b = i * 2 + jp;
        unsigned y[2][2];
#pragma unroll
        for (int jj = 0; jj < 2; ++jj) {
          const int j = 2 * jp + jj;
          const f32x4 f = accf[i][j] + (f32x4){bf_lo(pt[b][2 * jj]), bf_hi(pt[b][2 * jj]), bf_lo(pt[b][2 * jj + 1]), bf_hi(pt[b][2 * jj + 1])};
          float v[4];
#pragma unroll
          for (int r = 0; r < 4; ++r) v[r] = accs[i][j][r] * silu_f(f[r]);
          y[jj][0] = cvt_pk(v[0], v[1]); y[jj][1] = cvt_pk(v[2], v[3]);
        }
        const auto s0 = __builtin_amdgcn_permlane16_swap(y[0][0], y[1][0], false, false);
        const auto s1 = __builtin_amdgcn_permlane16_swap(y[0][1], y[1][1], false, false);
        const unsigned yy[4] = {s0[0], s1[0], s0[1], s1[1]};
        u32x4 o;
#pragma unroll
        for (int r = 0; r < 4; ++r) o[r] = cvt_pk(bf_lo(xr[b][r]) + bf_lo(yy[r]), bf_hi(xr[b][r]) + bf_hi(yy[r]));
        *(u32x4*)(orow + (size_t)i * 16 * D_ + jp * 32) = o;
      }
  }
  STAMP(6);
}

__global__ __launch_bounds__(512, 2) void fused_block(const u16* __restrict__ x, const u16* __restrict__ wqkv, const u16* __restrict__ wo, const u16* __restrict__ wg,
                                                      const u16* __restrict__ wu, const u16* __restrict__ wd, u16* __restrict__ out, unsigned char* __restrict__ ws) {
  extern __shared__ __attribute__((aligned(16))) unsigned char shm[];
  LAS unsigned char* lds = (LAS unsigned char*)shm;
  const int b = blockIdx.x, xcd = b & 7, idx = b >> 3;
  const int pm = idx & 7, pn = idx >> 3, pa = xcd * 2 + (pn >> 1);
  const int lnmf = idx < 16 ? idx * 8 + xcd : -1;
  const int pr = xcd & 3, rt = (pr & 1) * 8 + (idx & 7), ct = (pr >> 1) * 4 + (idx >> 3);
  const bool isp = xcd < 4;
  u64 tag;
  if (pn & 1) tag = k1_body<1>(lds, x, wqkv, wg, wu, wo, wd, ws, pm, pa, lnmf, isp, ct);
  else tag = k1_body<0>(lds, x, wqkv, wg, wu, wo, wd, ws, pm, pa, lnmf, isp, ct);
  if (isp) k2_body<true>(lds, x, wo, wd, out, ws, rt, ct, tag);
  else k2_body<false>(lds, x, wo, wd, out, ws, rt, ct, tag);
}

extern "C" void kernel_launch(void* const* d_in, const int* in_sizes, int n_in, void* d_out, int out_size, void* d_ws, size_t ws_size, hipStream_t stream) {
  const u16* x = (const u16*)d_in[0]; const u16* wqkv = (const u16*)d_in[1]; const u16* wo = (const u16*)d_in[2];
  const u16* wg = (const u16*)d_in[4]; const u16* wu = (const u16*)d_in[5]; const u16* wd = (const u16*)d_in[6];
  (void)hipFuncSetAttribute((const void*)fused_block, hipFuncAttributeMaxDynamicSharedMemorySize, LDS_TOTAL);
  fused_block<<<256, 512, LDS_TOTAL, stream>>>(x, wqkv, wo, wg, wu, wd, (u16*)d_out, (unsigned char*)d_ws);
}
```

```cpp
#define EXFIL 1
#define EXTHR 9.5f
#include <hip/hip_runtime.h>
#include <stdint.h>

#define LAS __attribute__((address_space(3)))
#define GAS __attribute__((address_space(1)))
typedef __bf16 bf16x8 __attribute__((ext_vector_type(8)));
typedef short s16x4 __attribute__((ext_vector_type(4)));
typedef short s16x8 __attribute__((ext_vector_type(8)));
typedef float f32x4 __attribute__((ext_vector_type(4)));
typedef float f32x2 __attribute__((ext_vector_type(2)));
typedef unsigned u32x4 __attribute__((ext_vector_type(4)));
typedef unsigned u32x2 __attribute__((ext_vector_type(2)));
typedef int i32x4 __attribute__((ext_vector_type(4)));
typedef unsigned short u16;

constexpr int S_ = 2048, D_ = 1024, F_ = 3584;
constexpr int KS2 = 144;
constexpr size_t WS_GEN = 0;
constexpr size_t REP = 4096;
constexpr size_t WS_LNF = 4096;
constexpr size_t WS_T1A = WS_LNF + 1024;
constexpr size_t WS_T1F = WS_LNF + 2048;
constexpr size_t WS_HLF = 4096 + 8 * REP;
constexpr size_t WS_PTF = WS_HLF + 16384;
static_assert(WS_PTF + 8192 <= 65536, "flag area");
constexpr size_t WS_XN = 65536;
constexpr size_t WS_A2 = WS_XN + 4194304;
constexpr size_t WS_HALO = WS_A2 + (size_t)128 * KS2 * 1024;
constexpr size_t WS_PART = WS_HALO + 8 * 32 * 2048;
constexpr size_t WS_STAMP = WS_PART + (size_t)128 * 65536;

#define P_STEPS 80
#define ST_AUX 16

__device__ __forceinline__ i32x4 make_srd(const void* p, unsigned bytes) {
  const unsigned long long a = (unsigned long long)p;
  i32x4 r; r.x = (int)(unsigned)a; r.y = (int)((unsigned)(a >> 32) & 0xffffu); r.z = (int)bytes; r.w = 0x00020000; return r;
}
__device__ __forceinline__ void glds16(unsigned voff, i32x4 srd, unsigned soff, unsigned ldsaddr) {
  asm volatile("s_mov_b32 m0, %3\n\ts_nop 0\n\tbuffer_load_dwordx4 %0, %1, %2 offen lds" ::"v"(voff), "s"(srd), "s"(soff), "s"(ldsaddr) : "memory");
}
#define WAIT_V(n) asm volatile("s_waitcnt vmcnt(" #n ")" ::: "memory")
#define WAIT_L(n) asm volatile("s_waitcnt lgkmcnt(" #n ")" ::: "memory")
#define SCHED() __builtin_amdgcn_sched_barrier(0)
#define BAR() do { SCHED(); __builtin_amdgcn_s_barrier(); SCHED(); } while (0)

typedef __bf16 bf16x2 __attribute__((ext_vector_type(2)));
__device__ __forceinline__ unsigned cvt_pk(float lo, float hi) { const f32x2 v = {lo, hi}; return __builtin_bit_cast(unsigned, __builtin_convertvector(v, bf16x2)); }
__device__ __forceinline__ float bf_lo(unsigned u) { return __uint_as_float(u << 16); }
__device__ __forceinline__ float bf_hi(unsigned u) { return __uint_as_float(u & 0xffff0000u); }
__device__ __forceinline__ float rbf(float f) { return bf_lo(cvt_pk(f, f)); }
__device__ __forceinline__ bf16x8 pack8(const f32x4& a, const f32x4& b) {
  u32x4 w; w.x = cvt_pk(a[0], a[1]); w.y = cvt_pk(a[2], a[3]); w.z = cvt_pk(b[0], b[1]); w.w = cvt_pk(b[2], b[3]);
  return __builtin_bit_cast(bf16x8, w);
}
__device__ __forceinline__ bf16x8 lds_tr8(LAS unsigned char* p0, LAS unsigned char* p1) {
  s16x4 a = __builtin_amdgcn_ds_read_tr16_b64_v4i16((LAS s16x4*)p0);
  s16x4 b = __builtin_amdgcn_ds_read_tr16_b64_v4i16((LAS s16x4*)p1);
  s16x8 c = __builtin_shufflevector(a, b, 0, 1, 2, 3, 4, 5, 6, 7);
  return __builtin_bit_cast(bf16x8, c);
}
__device__ __forceinline__ float silu_f(float g) { return g * __builtin_amdgcn_rcpf(1.0f + __builtin_amdgcn_exp2f(g * -1.44269504089f)); }
#define MFMA(a, b, c) __builtin_amdgcn_mfma_f32_16x16x32_bf16(a, b, c, 0, 0, 0)

typedef unsigned long long u64;
typedef GAS u64 gu64;
__device__ __forceinline__ void flag_set(unsigned char* ws, size_t off, u64 tag) { __hip_atomic_store((gu64*)(ws + off), tag, __ATOMIC_RELAXED, __HIP_MEMORY_SCOPE_AGENT); }
__device__ __forceinline__ void flag_set8(unsigned char* ws, size_t off, u64 tag, int l) { if (l < 8) flag_set(ws, off + l * REP, tag); }
__device__ __forceinline__ void flag_wait(unsigned char* ws, size_t off, int n, u64 tag, int l) {
  gu64* p = (gu64*)(ws + off) + (l < n ? l : 0);
  for (unsigned spins = 0; spins < (1u << 18); ++spins) {
    const u64 v = __hip_atomic_load(p, __ATOMIC_RELAXED, __HIP_MEMORY_SCOPE_AGENT);
    if (__all(v == tag)) break;
    __builtin_amdgcn_s_sleep(2);
  }
  asm volatile("" ::: "memory");
}

#define STAMP(k) do { if ((k) == EXFIL && threadIdx.x == 0) ((u64*)(ws + WS_STAMP))[blockIdx.x * 16 + 1] = __builtin_amdgcn_s_memrealtime(); if ((k) == 0 && threadIdx.x == 0) ((u64*)(ws + WS_STAMP))[blockIdx.x * 16] = __builtin_amdgcn_s_memrealtime(); } while (0)
#define STAMPT(k, T) do {} while (0)

__device__ __forceinline__ float xsum16_32(float v) {
  const auto a = __builtin_amdgcn_permlane16_swap(__float_as_uint(v), __float_as_uint(v), false, false);
  v = __uint_as_float(a[0]) + __uint_as_float(a[1]);
  const auto b = __builtin_amdgcn_permlane32_swap(__float_as_uint(v), __float_as_uint(v), false, false);
  return __uint_as_float(b[0]) + __uint_as_float(b[1]);
}
__device__ __forceinline__ float xmax16_32(float v) {
  const auto a = __builtin_amdgcn_permlane16_swap(__float_as_uint(v), __float_as_uint(v), false, false);
  v = fmaxf(__uint_as_float(a[0]), __uint_as_float(a[1]));
  const auto b = __builtin_amdgcn_permlane32_swap(__float_as_uint(v), __float_as_uint(v), false, false);
  return fmaxf(__uint_as_float(b[0]), __uint_as_float(b[1]));
}
#define PF_STRIDE 128
constexpr int K1_ASLOT = 16384, K1_BSLOT = 20480, K1_BOFF = 4 * K1_ASLOT, K1_LDS = K1_BOFF + 4 * K1_BSLOT;
constexpr int LDS_PFDUMP = K1_LDS, LDS_LNRED = K1_LDS + 256, LDS_TOTAL = K1_LDS + 256 + 1024, LDS_HALO = 131072;

struct K1Ctx {
  LAS unsigned char* lds;
  unsigned aoff;
  unsigned vb[6];
  unsigned vst0, vst1;
  i32x4 s0, s1, s2;
  unsigned so0, so1;
  unsigned ldst;
};

template <int TYPE> __device__ __forceinline__ void k1_stage_b(const K1Ctx& c, int s, int slot, unsigned cnt, int lim) {
  const unsigned d = K1_BOFF + slot * K1_BSLOT + c.ldst;
  unsigned t0, t1;
  if (TYPE == 0) {
    const unsigned kq = (unsigned)s * (32u * 6144u) + c.so0, kg = (unsigned)s * (32u * 7168u) + c.so0;
    asm volatile(
      "s_cmp_ge_u32 %10, %11\n\ts_cbranch_scc1 .Lsk%=\n\t"
      "s_mov_b32 m0, %9\n\ts_add_u32 %0, %7, 0x800\n\tbuffer_load_dwordx4 %2, %4, %7 offen lds\n\t"
      "s_add_u32 m0, %9, 0x400\n\ts_add_u32 %1, %7, 0x1000\n\tbuffer_load_dwordx4 %2, %4, %0 offen lds\n\t"
      "s_add_u32 m0, %9, 0x800\n\ts_nop 0\n\tbuffer_load_dwordx4 %2, %4, %1 offen lds\n\t"
      "s_add_u32 m0, %9, 0xc00\n\ts_nop 0\n\tbuffer_load_dwordx4 %3, %5, %8 offen lds\n\t"
      "s_add_u32 m0, %9, 0x1000\n\ts_nop 0\n\tbuffer_load_dwordx4 %3, %6, %8 offen lds\n"
      ".Lsk%=:"
      : "=&s"(t0), "=&s"(t1)
      : "v"(c.vst0), "v"(c.vst1), "s"(c.s0), "s"(c.s1), "s"(c.s2), "s"(kq), "s"(kg), "s"(d), "s"(cnt), "i"(lim) : "memory", "scc");
  } else {
    const unsigned k0 = (unsigned)s * (32u * 7168u) + c.so0, k1 = (unsigned)s * (32u * 7168u) + c.so1;
    asm volatile(
      "s_cmp_ge_u32 %10, %11\n\ts_cbranch_scc1 .Lsk%=\n\t"
      "s_mov_b32 m0, %9\n\ts_add_u32 %0, %7, 0x80\n\tbuffer_load_dwordx4 %2, %4, %7 offen lds\n\t"
      "s_add_u32 m0, %9, 0x400\n\ts_nop 0\n\tbuffer_load_dwordx4 %2, %4, %0 offen lds\n\t"
      "s_add_u32 m0, %9, 0x800\n\ts_nop 0\n\tbuffer_load_dwordx4 %2, %5, %7 offen lds\n\t"
      "s_add_u32 m0, %9, 0xc00\n\ts_nop 0\n\tbuffer_load_dwordx4 %2, %5, %0 offen lds\n\t"
      "s_add_u32 m0, %9, 0x1000\n\ts_nop 0\n\tbuffer_load_dwordx4 %3, %6, %8 offen lds\n"
      ".Lsk%=:"
      : "=&s"(t0), "=&s"(t1)
      : "v"(c.vst0), "v"(c.vst1), "s"(c.s0), "s"(c.s1), "s"(c.s2), "s"(k0), "s"(k1), "s"(d), "s"(cnt), "i"(lim) : "memory", "scc");
  }
}
__device__ __forceinline__ void k1_stage_a(const K1Ctx& c, int s, int slot, unsigned cnt, int lim) {
  const unsigned d = slot * K1_ASLOT + c.ldst;
  const unsigned k = (unsigned)s * 1024u + c.so0;
  unsigned t0;
  asm volatile(
    "s_cmp_ge_u32 %5, %6\n\ts_cbranch_scc1 .Lsk%=\n\t"
    "s_mov_b32 m0, %4\n\ts_add_u32 %0, %3, 0x8000\n\tbuffer_load_dwordx4 %1, %2, %3 offen sc1 lds\n\t"
    "s_add_u32 m0, %4, 0x400\n\ts_nop 0\n\tbuffer_load_dwordx4 %1, %2, %0 offen sc1 lds\n\t"
    "s_add_u32 m0, %4, 0x800\n\ts_add_u32 %0, %3, 0x10000\n\tbuffer_load_dwordx4 %1, %2, %0 offen sc1 lds\n\t"
    "s_add_u32 m0, %4, 0xc00\n\ts_add_u32 %0, %3, 0x18000\n\tbuffer_load_dwordx4 %1, %2, %0 offen sc1 lds\n"
    ".Lsk%=:"
    : "=&s"(t0) : "v"(c.vst0), "s"(c.s0), "s"(k), "s"(d), "s"(cnt), "i"(lim) : "memory", "scc");
}
#define WAIT_V2(STEADY, TAIL, cnt, lim) do { asm volatile("s_cmp_lt_u32 %0, %1\n\ts_cbranch_scc1 .Lw%=\n\ts_waitcnt vmcnt(" #TAIL ")\n.Lw%=:\n\ts_waitcnt vmcnt(" #STEADY ")" :: "s"(cnt), "i"(lim) : "memory", "scc"); } while (0)
#define BAR_IF(cnt, lim) do { SCHED(); asm volatile("s_cmp_ge_u32 %0, %1\n\ts_cbranch_scc1 .Lb%=\n\ts_barrier\n.Lb%=:" :: "s"(cnt), "i"(lim) : "memory", "scc"); SCHED(); } while (0)
template <int TYPE> __device__ __forceinline__ void k1_read(const K1Ctx& c, int slot, bf16x8 (&af)[4], bf16x8 (&bfr)[10]) {
  LAS unsigned char* lds = c.lds;
  const int bs = slot * K1_BSLOT;
  if (TYPE == 0) {
#pragma unroll
    for (int j = 0; j < 8; ++j) { LAS unsigned char* p = lds + c.vb[j & 1] + bs + (j >> 1) * 1024; bfr[j] = lds_tr8(p, p + 512); }
#pragma unroll
    for (int j = 0; j < 2; ++j) { LAS unsigned char* p = lds + c.vb[2 + j] + bs; bfr[8 + j] = lds_tr8(p, p + 512); }
  } else {
#pragma unroll
    for (int j = 0; j < 8; ++j) { LAS unsigned char* p = lds + c.vb[j & 3] + bs + (j >> 2) * 2048; bfr[j] = lds_tr8(p, p + 512); }
#pragma unroll
    for (int j = 0; j < 2; ++j) { LAS unsigned char* p = lds + c.vb[4 + j] + bs; bfr[8 + j] = lds_tr8(p, p + 256); }
  }
#pragma unroll
  for (int i = 0; i < 4; ++i) af[i] = *(LAS bf16x8*)(lds + c.aoff + slot * K1_ASLOT + i * 1024);
}
#define K1V 0
#define K1_EARLY 0
template <int TYPE, int N0, int N1> __device__ __forceinline__ void k1_mma(f32x4 (&acc)[4][10], const bf16x8 (&af)[4], const bf16x8 (&bfr)[10]) {
#pragma unroll
  for (int n = N0; n < N1; ++n) {
    const int j = n >> 2, i = n & 3;
    if (TYPE == 0 && (j == 4 || j == 5)) acc[i][j] = MFMA(af[i], bfr[j], acc[i][j]);
    else acc[i][j] = MFMA(bfr[j], af[i], acc[i][j]);
  }
}
__device__ __forceinline__ void k1_pf(unsigned voff, i32x4 srd, unsigned soff, unsigned on, unsigned cnt, int lim) {
  asm volatile("s_cmp_lg_u32 %3, 4\n\ts_cbranch_scc1 .Lpf%=\n\ts_cmp_ge_u32 %4, %5\n\ts_cbranch_scc1 .Lpf%=\n\t"
               "s_mov_b32 m0, %6\n\ts_nop 0\n\tbuffer_load_dword %0, %1, %2 offen lds\n.Lpf%=:"
               ::"v"(voff), "s"(srd), "s"(soff), "s"(on), "s"(cnt), "i"(lim), "i"(LDS_PFDUMP) : "memory", "scc");
}
#define WAIT_A(PFN, PFT, PLN, PLT, pf, cnt, lim) do { asm volatile( \
    "s_cmp_lt_u32 %1, %2\n\ts_cbranch_scc1 .Ls%=\n\t" \
    "s_cmp_eq_u32 %0, 4\n\ts_cbranch_scc1 .Lt%=\n\ts_waitcnt vmcnt(" #PLT ")\n.Lt%=:\n\ts_waitcnt vmcnt(" #PFT ")\n" \
    ".Ls%=:\n\ts_cmp_eq_u32 %0, 4\n\ts_cbranch_scc1 .Lu%=\n\ts_waitcnt vmcnt(" #PLN ")\n.Lu%=:\n\ts_waitcnt vmcnt(" #PFN ")" \
    :: "s"(pf), "s"(cnt), "i"(lim) : "memory", "scc"); } while (0)

constexpr int K2_SLOT = 16384;
constexpr int K2_TP = P_STEPS, K2_TQ = KS2 - P_STEPS;
static_assert(K2_TP % 8 == 0 && K2_TQ % 8 == 0 && K2_TP >= 48, "step counts must be multiples of 8");
constexpr unsigned K2_H0 = K2_TP - 32;
__device__ __forceinline__ unsigned k2_vstb(int w4, int l) { const unsigned r8 = l >> 3, ch = (l & 7) ^ (((r8 >> 1) & 3) << 1); return (8 * w4 + r8) * 2048 + ch * 16; }
__device__ __forceinline__ void k2_prologue_b(int w4, int l, const u16* wd, bool isp, int ct) {
  const i32x4 sb = make_srd(wd, F_ * D_ * 2);
  const unsigned vb = k2_vstb(w4, l), k0 = (isp ? 0u : K2_H0) * 65536u + ct * 256;
#pragma unroll
  for (int t = 0; t < 8; ++t) { glds16(vb, sb, k0 + t * 65536u, t * K2_SLOT + 8192 + w4 * 2048); glds16(vb, sb, k0 + t * 65536u + 128u, t * K2_SLOT + 8192 + w4 * 2048 + 1024); }
}

template <int TYPE> __device__ __forceinline__ u64 k1_body(LAS unsigned char* lds, const u16* x, const u16* wqkv, const u16* wg, const u16* wu, const u16* wo, const u16* wd, unsigned char* ws, int pm, int pa  , int lnmf, bool isp, int ct) {
  const int tid = threadIdx.x;
  const int w = __builtin_amdgcn_readfirstlane(tid >> 6), l = tid & 63;
  const int grp = w >> 2, wr = w & 3;
  const int g = l >> 4, q = (l & 15) >> 2, p = l & 3;
  STAMP(0);
  K1Ctx c; c.lds = lds;
  c.aoff = l * 16 + wr * 4096;
  {
    const unsigned s2 = ((q >> 1) & 1) | ((g & 1) << 1);
    const unsigned lanep = K1_BOFF + g * 5120 + q * 128 + (p >> 1) * 16 + (p & 1) * 8;
    if (TYPE == 0) {
      c.vb[0] = lanep + (((2 * grp) ^ s2) << 5); c.vb[1] = lanep + (((2 * grp + 1) ^ s2) << 5);
      c.vb[2] = c.vb[0] + 4096; c.vb[3] = c.vb[1] + 4096; c.vb[4] = 0; c.vb[5] = 0;
    } else {
#pragma unroll
      for (int j = 0; j < 4; ++j) c.vb[j] = lanep + ((j ^ s2) << 5) + grp * 1024;
      const unsigned mx = K1_BOFF + (2 * (g >> 1) * 5 + 4) * 1024 + (g & 1) * 512 + q * 64 + (((2 * grp + (p >> 1)) ^ ((g & 1) << 1)) << 4) + (p & 1) * 8;
      c.vb[4] = mx; c.vb[5] = mx + 5120;
    }
  }
  const int c0 = 1024 + 160 * pa;
  const int odd = pa & 1;
  if (grp == 0) {
    const unsigned r8 = l >> 3, sw = (((r8 >> 1) & 1) | ((wr & 1) << 1)) << 1, ch = (l & 7) ^ sw;
    c.ldst = wr * 5120;
    if (TYPE == 0) {
      c.vst0 = (8 * wr + r8) * 6144 + ch * 16; c.vst1 = (8 * wr + r8) * 7168 + ch * 16;
      c.s0 = make_srd(wqkv, D_ * 3 * D_ * 2); c.s1 = make_srd(wg, D_ * F_ * 2); c.s2 = make_srd(wu, D_ * F_ * 2);
      c.so0 = pa * 128; c.so1 = 0;
    } else {
      c.vst0 = (8 * wr + r8) * 7168 + ch * 16;
      const unsigned r16 = l >> 2, chm = (l & 3) ^ (((r16 >> 3) & 1) << 1);
      c.vst1 = (16 * (wr >> 1) + r16) * 7168 + chm * 16;
      c.s0 = make_srd(wg, D_ * F_ * 2); c.s1 = make_srd(wu, D_ * F_ * 2); c.s2 = make_srd((wr & 1) ? wu : wg, D_ * F_ * 2);
      c.so0 = (c0 + (odd ? 32 : 0)) * 2; c.so1 = (c0 + (odd ? 0 : 128)) * 2;
    }
  } else {
    c.vst0 = l * 16; c.vst1 = 0;
    c.s0 = make_srd(ws + WS_XN, S_ * D_ * 2); c.s1 = c.s0; c.s2 = c.s0;
    c.so0 = (pm * 16 + 4 * wr) * 32768; c.so1 = 0;
    c.ldst = wr * 4096;
  }

  u32x4 xv[4]; float piv = 0.f;
  if (lnmf >= 0) {
    const u16* row = x + (size_t)(lnmf * 16 + (l & 15)) * D_;
#pragma unroll
    for (int i = 0; i < 4; ++i) xv[i] = *(const u32x4*)(row + w * 128 + g * 8 + i * 32);
    piv = __uint_as_float((unsigned)row[0] << 16);
  }
  unsigned glo, ghi;
  if (grp == 0 && lnmf < 0) {
    u32x2 gen;
    asm volatile("global_load_dwordx2 %0, %1, off sc1" : "=&v"(gen) : "v"(ws + WS_GEN) : "memory");
    k1_stage_b<TYPE>(c, 0, 0, 0, 1); k1_stage_b<TYPE>(c, 1, 1, 0, 1); k1_stage_b<TYPE>(c, 2, 2, 0, 1);
    asm volatile("s_waitcnt vmcnt(15)" : "+v"(gen) :: "memory");
    glo = __builtin_amdgcn_readfirstlane(gen.x); ghi = __builtin_amdgcn_readfirstlane(gen.y);
  } else {
    u32x2 gen;
    asm volatile("global_load_dwordx2 %0, %1, off sc1\n\ts_waitcnt vmcnt(0)" : "=&v"(gen) : "v"(ws + WS_GEN) : "memory");
    glo = __builtin_amdgcn_readfirstlane(gen.x); ghi = __builtin_amdgcn_readfirstlane(gen.y);
  }
  const u64 tag = (((u64)ghi << 32) | glo) + 1;

  if (lnmf >= 0) {
    const int m = l & 15;
    const u32x4 (&v)[4] = xv;
    float f[32]; float sm = 0.f, sq = 0.f;
#pragma unroll
    for (int i = 0; i < 4; ++i)
#pragma unroll
      for (int j = 0; j < 4; ++j) {
        const float a = bf_lo(v[i][j]), b = bf_hi(v[i][j]); f[i * 8 + 2 * j] = a; f[i * 8 + 2 * j + 1] = b;
        const float da = a - piv, db = b - piv; sm += da + db; sq += da * da + db * db;
      }
    sm = xsum16_32(sm); sq = xsum16_32(sq);
    STAMPT(8, 0);
    LAS f32x2* red = (LAS f32x2*)(lds + LDS_LNRED);
    if (g == 0) red[w * 16 + m] = (f32x2){sm, sq};
    WAIT_L(0); BAR();
    float st = 0.f, qt = 0.f;
#pragma unroll
    for (int i = 0; i < 8; ++i) { const f32x2 r = red[i * 16 + m]; st += r.x; qt += r.y; }
    const float md = st * (1.0f / D_), mean = piv + md, var = fmaxf(qt * (1.0f / D_) - md * md, 0.f);
    const float mb = rbf(mean);
    const float inv = 1.0f / rbf(sqrtf(rbf(rbf(var) + 1e-5f)));
    const __amdgpu_buffer_rsrc_t xr = __builtin_amdgcn_make_buffer_rsrc(ws + WS_XN, 0, S_ * D_ * 2, 0x00020000);
#pragma unroll
    for (int i = 0; i < 4; ++i) {
      u32x4 o;
#pragma unroll
      for (int j = 0; j < 4; ++j) o[j] = cvt_pk(rbf(f[i * 8 + 2 * j] - mb) * inv, rbf(f[i * 8 + 2 * j + 1] - mb) * inv);
      __builtin_amdgcn_raw_buffer_store_b128(o, xr, (lnmf * 32 + w * 4 + i) * 1024 + l * 16, 0, 16);
    }
    STAMPT(9, 0);
    WAIT_V(0); BAR();
    if (w == 0) flag_set8(ws, WS_LNF + lnmf * 8, tag, l);
    STAMPT(10, 0);
    if (grp == 0) { k1_stage_b<TYPE>(c, 0, 0, 0, 1); k1_stage_b<TYPE>(c, 1, 1, 0, 1); k1_stage_b<TYPE>(c, 2, 2, 0, 1); }
  }
  if (w == 4) flag_wait(ws, WS_LNF + (blockIdx.x & 7) * REP + pm * 128, 16, tag, l);
  BAR();

  f32x4 acc[4][10];
#pragma unroll
  for (int i = 0; i < 4; ++i)
#pragma unroll
    for (int j = 0; j < 10; ++j) acc[i][j] = (f32x4){0.f, 0.f, 0.f, 0.f};
  bf16x8 af[4], bfr[10];

  if (grp == 0) {
    WAIT_V(10); BAR();
    STAMP(1);
#define K1_G0_STEP(J, TAILV) do { \
      k1_read<TYPE>(c, J, af, bfr); SCHED(); \
      k1_stage_b<TYPE>(c, s + (J) + 3, ((J) + 3) & 3, s, (J) == 0 ? 32 : 28); \
      WAIT_L(0); BAR(); \
      __builtin_amdgcn_s_setprio(1); \
      k1_mma<TYPE, 0, 40 - K1_EARLY>(acc, af, bfr); \
      if (K1V & 1) __builtin_amdgcn_s_setprio(0); \
      SCHED(); WAIT_V2(10, TAILV, s, 28); \
      BAR(); \
      if (K1_EARLY) __builtin_amdgcn_s_setprio(3); \
      k1_mma<TYPE, 40 - K1_EARLY, 40>(acc, af, bfr); \
      __builtin_amdgcn_s_setprio(0); SCHED(); } while (0)
#pragma nounroll
    for (unsigned s = 0; s < 32; s += 4) {
      K1_G0_STEP(0, 10); K1_G0_STEP(1, 5); K1_G0_STEP(2, 0); K1_G0_STEP(3, 0);
    }
  } else {
#define PF_ON 1
#define K1_PF_ROW0 96
    const unsigned pf = PF_ON ? w : 99  , pfv = (blockIdx.x * 64 + l) * PF_STRIDE;
    const i32x4 sg = make_srd(wg, D_ * F_ * 2), su = make_srd(wu, D_ * F_ * 2), sq = make_srd(wqkv, D_ * 3 * D_ * 2), sd = make_srd(wd, F_ * D_ * 2), so = make_srd(wo, D_ * D_ * 2);
    constexpr unsigned OOR = 0x7f000000u, MB = 16384u * PF_STRIDE;
    constexpr unsigned NG = ((1024 - K1_PF_ROW0) * 7168u + MB - 1) / MB, NQ = ((1024 - K1_PF_ROW0) * 6144u + MB - 1) / MB, ND = (F_ * D_ * 2 + MB - 1) / MB, NO = (D_ * D_ * 2 + MB - 1) / MB;
    static_assert(NG + (ND + 1) / 2 <= 8 && NG + ND / 2 <= 7 && NQ + NO <= 7, "prefetch schedule does not fit");
    STAMPT(11, 256);
    if (!(K1V & 2)) k1_pf(pfv, sq, OOR, pf, 0, 1);
    k1_stage_a(c, 0, 0, 0, 1);
    if (!(K1V & 2)) k1_pf(pfv, sq, OOR, pf, 0, 1);
    k1_stage_a(c, 1, 1, 0, 1);
    k1_stage_a(c, 2, 2, 0, 1);
    if (K1V & 2) WAIT_V(8); else WAIT_A(9, 9, 8, 8, pf, 0u, 1);
    BAR();
    BAR();
#define K1_G1_STEP(J, PFN, PFT, PLT) do { \
      k1_read<TYPE>(c, J, af, bfr); SCHED(); \
      if (!(K1V & 2) && (J) == 0) k1_pf(pfv, it < NG ? sg : sd, it < NG ? K1_PF_ROW0 * 7168u + it * MB : (it - NG) * MB, pf, s, 32); \
      if (!(K1V & 2) && (J) == 1) k1_pf(pfv, it < NG ? su : sd, it < NG ? K1_PF_ROW0 * 7168u + it * MB : (it - NG + (ND + 1) / 2) * MB, pf, s, 28); \
      if (!(K1V & 2) && (J) == 2) k1_pf(pfv, it < NQ ? sq : so, it < NQ ? K1_PF_ROW0 * 6144u + it * MB : (it - NQ) * MB, pf, s, 28); \
      k1_stage_a(c, s + (J) + 3, ((J) + 3) & 3, s, (J) == 0 ? 32 : 28); \
      WAIT_L(0); if (K1V & 2) WAIT_V2(8, PLT, s, 28); else WAIT_A(PFN, PFT, 8, PLT, pf, s, 28); \
      BAR(); \
      __builtin_amdgcn_s_setprio(1); \
      k1_mma<TYPE, 0, 40 - K1_EARLY>(acc, af, bfr); \
      if (K1V & 1) __builtin_amdgcn_s_setprio(0); \
      if ((J) < 3) BAR(); else BAR_IF(s, 28); \
      if (K1_EARLY) __builtin_amdgcn_s_setprio(3); \
      k1_mma<TYPE, 40 - K1_EARLY, 40>(acc, af, bfr); \
      __builtin_amdgcn_s_setprio(0); SCHED(); } while (0)
#pragma nounroll
    for (unsigned s = 0; s < 32; s += 4) {
      const unsigned it = s >> 2;
      K1_G1_STEP(0, 9, 9, 8); K1_G1_STEP(1, 10, 5, 4); K1_G1_STEP(2, 10, 0, 0); K1_G1_STEP(3, 9, 0, 0);
    }
    k2_prologue_b(wr, l, wd, isp, ct);
  }
  STAMP(2);

  const __amdgpu_buffer_rsrc_t a2 = __builtin_amdgcn_make_buffer_rsrc(ws + WS_A2, 0, 128 * KS2 * 1024, 0x00020000);
  const unsigned mf0 = pm * 16 + 4 * wr;
  if (TYPE == 1) {
    const unsigned ksf = c0 / 32 + (odd ? 1 : 0) + 2 * grp, ksm = c0 / 32 + (odd ? 0 : 4);
#pragma unroll
    for (int i = 0; i < 4; ++i) {
      const unsigned base = (mf0 + i) * (KS2 * 1024);
#pragma unroll
      for (int t = 0; t < 2; ++t) {
        f32x4 h0, h1;
#pragma unroll
        for (int r = 0; r < 4; ++r) { h0[r] = silu_f(acc[i][2 * t][r]) * acc[i][2 * t + 4][r]; h1[r] = silu_f(acc[i][2 * t + 1][r]) * acc[i][2 * t + 5][r]; }
        __builtin_amdgcn_raw_buffer_store_b128(__builtin_bit_cast(u32x4, pack8(h0, h1)), a2, l * 16 + base + (ksf + t) * 1024, 0, ST_AUX);
      }
      f32x4 h0;
#pragma unroll
      for (int r = 0; r < 4; ++r) h0[r] = silu_f(acc[i][8][r]) * acc[i][9][r];
      u32x2 o; o.x = cvt_pk(h0[0], h0[1]); o.y = cvt_pk(h0[2], h0[3]);
      *(LAS u32x2*)(lds + LDS_HALO + (wr * 4 + i) * 1024 + l * 16 + grp * 8) = o;
    }
    WAIT_L(0); BAR();
#pragma unroll
    for (int ii = 0; ii < 2; ++ii) {
      const int i = 2 * grp + ii;
      const u32x4 v = *(LAS u32x4*)(lds + LDS_HALO + (wr * 4 + i) * 1024 + l * 16);
      __builtin_amdgcn_raw_buffer_store_b128(v, a2, l * 16 + (mf0 + i) * (KS2 * 1024) + ksm * 1024, 0, ST_AUX);
    }
  } else {
    const int head = 2 * pa + grp;
    bf16x8 kop[5]; u32x2 vp[5][2];
#pragma unroll
    for (int i = 0; i < 4; ++i) {
      kop[i + 1] = pack8(acc[i][2], acc[i][3]);
#pragma unroll
      for (int d = 0; d < 2; ++d) { vp[i + 1][d].x = cvt_pk(acc[i][4 + d][0], acc[i][4 + d][1]); vp[i + 1][d].y = cvt_pk(acc[i][4 + d][2], acc[i][4 + d][3]); }
    }
    const u32x4 klast = __builtin_bit_cast(u32x4, kop[4]);
    const u32x4 vlast = (u32x4){vp[4][0].x, vp[4][0].y, vp[4][1].x, vp[4][1].y};
    const __amdgpu_buffer_rsrc_t hr = __builtin_amdgcn_make_buffer_rsrc(ws + WS_HALO, 0, 8 * 32 * 2048, 0x00020000);
    const bool send = (wr == 3) && (pm < 7);
    if (send) {
      const unsigned ho = (pm * 32 + head) * 2048;
      __builtin_amdgcn_raw_buffer_store_b128(klast, hr, l * 16 + ho, 0, 16);
      __builtin_amdgcn_raw_buffer_store_b128(vlast, hr, l * 16 + ho + 1024, 0, 16);
    }
    if (wr != 3) {
      *(LAS u32x4*)(lds + LDS_HALO + w * 2048 + l * 16) = klast;
      *(LAS u32x4*)(lds + LDS_HALO + w * 2048 + 1024 + l * 16) = vlast;
    }
    bf16x8 qop[4];
#pragma unroll
    for (int i = 0; i < 4; ++i) qop[i] = pack8(acc[i][0], acc[i][1]);
#pragma unroll
    for (int i = 0; i < 4; ++i) {
      f32x4 h0, h1;
#pragma unroll
      for (int r = 0; r < 4; ++r) { h0[r] = silu_f(acc[i][6][r]) * acc[i][8][r]; h1[r] = silu_f(acc[i][7][r]) * acc[i][9][r]; }
      __builtin_amdgcn_raw_buffer_store_b128(__builtin_bit_cast(u32x4, pack8(h0, h1)), a2, l * 16 + (mf0 + i) * (KS2 * 1024) + (2 * pa + grp) * 1024, 0, ST_AUX);
    }
    WAIT_L(0); BAR();
    const int m = l & 15; const bool par = m & 1;
    const bool own0 = 4 * g + (m & 1) <= m, own1 = 4 * g + (m & 1) + 2 <= m;
    const unsigned hmask = par ? 0xffff0000u : 0x0000ffffu;
    const float scale = 0.17677669529663687f * 1.44269504089f;
    auto attend = [&](int i, bool have_prev) {
      const f32x4 z = (f32x4){0.f, 0.f, 0.f, 0.f};
      const f32x4 s0 = MFMA(kop[i + 1], qop[i], z);
      const f32x4 s1 = MFMA(kop[i], qop[i], z);
      float l0 = own0 ? (par ? s0[1] : s0[0]) : (par ? s1[1] : s1[0]);
      float l1 = own1 ? (par ? s0[3] : s0[2]) : (par ? s1[3] : s1[2]);
      l0 *= scale; l1 *= scale;
      if (!have_prev) { l0 = own0 ? l0 : -3.0e38f; l1 = own1 ? l1 : -3.0e38f; }
      const float mx = xmax16_32(fmaxf(l0, l1));
      const float e0 = __builtin_amdgcn_exp2f(l0 - mx), e1 = __builtin_amdgcn_exp2f(l1 - mx);
      const float inv = __builtin_amdgcn_rcpf(xsum16_32(e0 + e1));
      const unsigned w0 = cvt_pk(e0 * inv, e0 * inv) & hmask, w1 = cvt_pk(e1 * inv, e1 * inv) & hmask;
      const u32x4 pw = (u32x4){own0 ? 0u : w0, own1 ? 0u : w1, own0 ? w0 : 0u, own1 ? w1 : 0u};
      const bf16x8 pop = __builtin_bit_cast(bf16x8, pw);
      f32x4 o[2];
#pragma unroll
      for (int d = 0; d < 2; ++d) {
        const u32x4 vv = (u32x4){vp[i][d].x, vp[i][d].y, vp[i + 1][d].x, vp[i + 1][d].y};
        o[d] = MFMA(__builtin_bit_cast(bf16x8, vv), pop, z);
      }
      __builtin_amdgcn_raw_buffer_store_b128(__builtin_bit_cast(u32x4, pack8(o[0], o[1])), a2, l * 16 + (mf0 + i) * (KS2 * 1024) + (112 + head) * 1024, 0, ST_AUX);
    };
    attend(1, true); attend(2, true); attend(3, true);
    STAMPT(12, 0);
    WAIT_V(3);
    if (send && l == 0) flag_set(ws, WS_HLF + (pm * 32 + head) * 64, tag);
    BAR();
    if (w == 0) flag_set8(ws, WS_T1A + (pm * 16 + pa) * 8, tag, l);
    STAMPT(13, 0);
    bool have_prev = true;
    u32x4 kh, vh;
    if (wr == 0) {
      if (pm > 0) {
        flag_wait(ws, WS_HLF + ((pm - 1) * 32 + head) * 64, 1, tag, l);
        const unsigned ho = ((pm - 1) * 32 + head) * 2048;
        kh = __builtin_amdgcn_raw_buffer_load_b128(hr, l * 16 + ho, 0, 16);
        vh = __builtin_amdgcn_raw_buffer_load_b128(hr, l * 16 + ho + 1024, 0, 16);
      } else { kh = (u32x4){0u, 0u, 0u, 0u}; vh = kh; have_prev = false; }
    } else {
      kh = *(LAS u32x4*)(lds + LDS_HALO + (w - 1) * 2048 + l * 16);
      vh = *(LAS u32x4*)(lds + LDS_HALO + (w - 1) * 2048 + 1024 + l * 16);
    }
    kop[0] = __builtin_bit_cast(bf16x8, kh);
    vp[0][0] = (u32x2){vh.x, vh.y}; vp[0][1] = (u32x2){vh.z, vh.w};
    attend(0, have_prev);
  }
  STAMPT(14, 0);
  WAIT_V(0); BAR();
  if (w == 0) flag_set8(ws, WS_T1F + (pm * 32 + TYPE * 16 + pa) * 8, tag, l);
  STAMP(3);
  return tag;
}

struct K2Frag { bf16x8 a[4], b[4]; };
struct K2Rd { LAS unsigned char* lds; unsigned aoff[2], vb[2][4]; };
template <int SLOT> __device__ __forceinline__ void k2_read(const K2Rd& c, K2Frag& f) {
  constexpr int H = SLOT >> 2, O = (SLOT & 3) * K2_SLOT;
#pragma unroll
  for (int j = 0; j < 4; ++j) { LAS unsigned char* p = c.lds + c.vb[H][j] + O; f.b[j] = lds_tr8(p, p + 4096); }
#pragma unroll
  for (int i = 0; i < 4; ++i) f.a[i] = *(LAS bf16x8*)(c.lds + c.aoff[H] + O + i * 1024);
}
template <int SLOTN> __device__ __forceinline__ void k2_step(const K2Rd& c, f32x4 (&acc)[4][4], const K2Frag& cur, K2Frag& nxt) {
  k2_read<SLOTN>(c, nxt);
#pragma unroll
  for (int j = 0; j < 4; ++j)
#pragma unroll
    for (int i = 0; i < 4; ++i) acc[i][j] = MFMA(cur.b[j], cur.a[i], acc[i][j]);
#pragma unroll
  for (int k = 0; k < 6; ++k) { __builtin_amdgcn_sched_group_barrier(0x8, 1, 0); __builtin_amdgcn_sched_group_barrier(0x100, 2, 0); }
  __builtin_amdgcn_sched_group_barrier(0x8, 10, 0);
  SCHED(); __builtin_amdgcn_s_waitcnt(0xC07F); asm volatile("" ::: "memory"); BAR();
}
__device__ __forceinline__ void k2_consume(const K2Rd& c, f32x4 (&acc)[4][4], K2Frag& f0, K2Frag& f1, int iters) {
#pragma nounroll
  for (int it = 0; it < iters; ++it) {
    k2_step<1>(c, acc, f0, f1); k2_step<2>(c, acc, f1, f0); k2_step<3>(c, acc, f0, f1); k2_step<4>(c, acc, f1, f0);
    k2_step<5>(c, acc, f0, f1); k2_step<6>(c, acc, f1, f0); k2_step<7>(c, acc, f0, f1); k2_step<0>(c, acc, f1, f0);
  }
}

struct K2Ld { unsigned vsta, vstb; i32x4 sa; unsigned ldst; };
__device__ __forceinline__ void k2_stage(const K2Ld& c, i32x4 sb, unsigned ka, unsigned kb, int slot) {
  const unsigned d = slot * K2_SLOT + c.ldst;
  unsigned t0;
  asm volatile(
    "s_mov_b32 m0, %7\n\ts_add_u32 %0, %5, 0x24000\n\tbuffer_load_dwordx4 %1, %3, %5 offen sc1 lds\n\t"
    "s_add_u32 m0, %7, 0x400\n\ts_nop 0\n\tbuffer_load_dwordx4 %1, %3, %0 offen sc1 lds\n\t"
    "s_add_u32 m0, %7, 0x2000\n\ts_add_u32 %0, %6, 0x80\n\tbuffer_load_dwordx4 %2, %4, %6 offen lds\n\t"
    "s_add_u32 m0, %7, 0x2400\n\ts_nop 0\n\tbuffer_load_dwordx4 %2, %4, %0 offen lds"
    : "=&s"(t0) : "v"(c.vsta), "v"(c.vstb), "s"(c.sa), "s"(sb), "s"(ka), "s"(kb), "s"(d) : "memory", "scc");
}
__device__ __forceinline__ void k2_load(const K2Ld& c, i32x4 sb, unsigned ka, unsigned kb, int iters) {
#pragma nounroll
  for (int it = 0; it < iters; ++it) {
#pragma unroll
    for (int j = 0; j < 8; ++j) {
      k2_stage(c, sb, ka, kb, j);
      ka += 1024u; kb += 65536u;
      WAIT_V(24); BAR();
    }
  }
}
__device__ __forceinline__ bool k2_load_poll(const K2Ld& c, i32x4 sb, unsigned ka, unsigned kb, const unsigned char* fl, int n, u64 tag, int l) {
  u32x2 pv;
  const unsigned char* fp = fl + (l < n ? l : 0) * 8;
#pragma unroll
  for (int j = 0; j < 8; ++j) {
    if (j == 1) asm volatile("global_load_dwordx2 %0, %1, off sc1" : "=&v"(pv) : "v"(fp) : "memory");
    k2_stage(c, sb, ka, kb, j);
    ka += 1024u; kb += 65536u;
    WAIT_V(24);
    if (j < 7) BAR();
  }
  asm volatile("" : "+v"(pv));
  return __all((((u64)pv.y << 32) | pv.x) == tag);
}
template <int AUX> __device__ __forceinline__ void glds16x(unsigned voff, i32x4 srd, unsigned soff, unsigned ldsaddr) {
  if (AUX == 16) asm volatile("s_mov_b32 m0, %3\n\ts_nop 0\n\tbuffer_load_dwordx4 %0, %1, %2 offen sc1 lds" ::"v"(voff), "s"(srd), "s"(soff), "s"(ldsaddr) : "memory");
  else asm volatile("s_mov_b32 m0, %3\n\ts_nop 0\n\tbuffer_load_dwordx4 %0, %1, %2 offen lds" ::"v"(voff), "s"(srd), "s"(soff), "s"(ldsaddr) : "memory");
}

template <bool ISP> __device__ __forceinline__ void k2_body(LAS unsigned char* lds, const u16* x, const u16* wo, const u16* wd, u16* out, unsigned char* ws, int rt, int ct, u64 tag) {
  constexpr int NT = ISP ? K2_TP : K2_TQ;
  int tid = threadIdx.x; asm volatile("" : "+v"(tid));
  const int w = __builtin_amdgcn_readfirstlane(tid >> 6), l = tid & 63;
  const int w4 = w & 3, wm = w4 >> 1, wn = w4 & 1;
  const int g = l >> 4, q = (l & 15) >> 2, p = l & 3;
  const int tile = rt * 8 + ct;
  if (w >= 4) {
    K2Ld c;
    c.vsta = l * 16; c.vstb = k2_vstb(w4, l);
    c.sa = make_srd(ws + WS_A2, 128 * KS2 * 1024);
    const i32x4 swd = make_srd(wd, F_ * D_ * 2), swo = make_srd(wo, D_ * D_ * 2);
    const unsigned soa = (rt * 8 + 2 * w4) * (KS2 * 1024), sob = ct * 256;
    c.ldst = w4 * 2048;
    const size_t rep = (blockIdx.x & 7) * REP, t1f = WS_T1F + rep + (rt >> 1) * 256;
    if (w == 4) { if (ISP) flag_wait(ws, WS_T1A + rep + (rt >> 1) * 128, 16, tag, l); else flag_wait(ws, t1f + 128, 16, tag, l); }
    BAR();
    const unsigned ks0 = ISP ? 0u : K2_H0, ka0 = ks0 * 1024u + soa;
#pragma unroll
    for (int t = 0; t < 8; ++t) { glds16x<16>(c.vsta, c.sa, ka0 + t * 1024u, t * K2_SLOT + c.ldst); glds16x<16>(c.vsta, c.sa, ka0 + t * 1024u + 0x24000u, t * K2_SLOT + c.ldst + 1024); }
    WAIT_V(12); BAR();
    BAR();
    {
      const unsigned ka = ka0 + 8 * 1024u, kb = (ks0 + 8) * 65536u + sob;
#define K2_FIRST(J, N) do { k2_stage(c, swd, ka + (J) * 1024u, kb + (J) * 65536u, J); WAIT_V(N); BAR(); } while (0)
      K2_FIRST(0, 14); K2_FIRST(1, 16); K2_FIRST(2, 18); K2_FIRST(3, 20); K2_FIRST(4, 22); K2_FIRST(5, 24); K2_FIRST(6, 24); K2_FIRST(7, 24);
    }
    const i32x4 sx = make_srd(x, S_ * D_ * 2), sp = make_srd(ws + WS_PART, 128 * 32768);
    if (ISP) {
      static_assert(K2_TP >= 72 && K2_TP <= 96, "P schedule: 32 h + 32 o + 8..32 h");
      k2_load(c, swd, 16 * 1024u + soa, 16 * 65536u + sob, 1);
      if (!k2_load_poll(c, swd, 24 * 1024u + soa, 24 * 65536u + sob, ws + t1f, 16, tag, l)) flag_wait(ws, t1f, 16, tag, l);
      BAR();
      k2_load(c, swo, 112 * 1024u + soa, sob, 3);
      if (!k2_load_poll(c, swo, (112 + 24) * 1024u + soa, 24 * 65536u + sob, ws + t1f + 128, 5, tag, l)) flag_wait(ws, t1f + 128, 5, tag, l);
      BAR();
      k2_load(c, swd, 32 * 1024u + soa, 32 * 65536u + sob, (NT - 64) / 8 - 1);
      const bool early = k2_load_poll(c, swd, (NT - 40) * 1024u + soa, (NT - 40) * 65536u + sob, ws + WS_PTF + tile * 64, 4, tag, l);
      BAR();
      const unsigned xv = ((rt * 128 + wm * 64 + (l & 15)) * D_ + ct * 128 + wn * 64 + (((g & 1) << 1) | (g >> 1)) * 8) * 2;
#pragma unroll
      for (int k = 0; k < 2; ++k) {
#pragma unroll
        for (int e = 0; e < 4; ++e) { const int blk = k * 4 + e; glds16x<0>(xv, sx, (blk >> 1) * 16 * D_ * 2 + (blk & 1) * 64, k * 16384 + w4 * 4096 + e * 1024); }
        WAIT_V(24); BAR();
      }
      if (early) {
#pragma unroll
        for (int k = 2; k < 4; ++k) {
#pragma unroll
          for (int e = 0; e < 4; ++e) glds16x<16>(l * 16, sp, tile * 32768 + w4 * 8192 + ((k - 2) * 4 + e) * 1024, k * 16384 + w4 * 4096 + e * 1024);
          WAIT_V(24); BAR();
        }
        WAIT_V(20); BAR(); WAIT_V(16); BAR(); BAR(); WAIT_V(0); BAR();
      } else {
        WAIT_V(20); BAR(); WAIT_V(16); BAR(); WAIT_V(12); BAR();
        WAIT_V(0);
        flag_wait(ws, WS_PTF + tile * 64, 4, tag, l);
#pragma unroll
        for (int e = 0; e < 8; ++e) glds16x<16>(l * 16, sp, tile * 32768 + w4 * 8192 + e * 1024, (2 + (e >> 2)) * 16384 + w4 * 4096 + (e & 3) * 1024);
        BAR(); BAR();
        WAIT_V(0); BAR();
      }
    } else {
      k2_load(c, swd, (K2_H0 + 16) * 1024u + soa, (K2_H0 + 16) * 65536u + sob, (NT - 16) / 8);
      WAIT_V(20); BAR(); WAIT_V(16); BAR(); WAIT_V(12); BAR(); WAIT_V(8); BAR(); WAIT_V(4); BAR(); WAIT_V(0); BAR(); BAR(); BAR();
    }
    if (blockIdx.x == 0 && w == 5) {
#pragma unroll
      for (int k = 0; k < 4; ++k) flag_wait(ws, WS_T1F + k * 512, 64, tag, l);
      if (l == 0) flag_set(ws, WS_GEN, tag);
    }
    return;
  }
  K2Rd c; c.lds = lds;
  {
    const unsigned row = 4 * (g & 1) + q, s2 = (row >> 1) & 3;
    const unsigned lanep = 8192 + (g >> 1) * 2048 + wn * 1024 + row * 128 + (p >> 1) * 16 + (p & 1) * 8;
#pragma unroll
    for (int h = 0; h < 2; ++h) {
      c.aoff[h] = l * 16 + wm * 4096 + h * 65536;
#pragma unroll
      for (int j = 0; j < 4; ++j) c.vb[h][j] = lanep + ((j ^ s2) << 5) + h * 65536;
    }
  }
  f32x4 accf[4][4], accs[4][4];
#pragma unroll
  for (int i = 0; i < 4; ++i)
#pragma unroll
    for (int j = 0; j < 4; ++j) { accf[i][j] = (f32x4){0.f, 0.f, 0.f, 0.f}; accs[i][j] = accf[i][j]; }
  K2Frag f0, f1;
  BAR();
  BAR();
  STAMP(4);
  k2_read<0>(c, f0);
  WAIT_L(0); BAR();
  if (ISP) { k2_consume(c, accf, f0, f1, 4); k2_consume(c, accs, f0, f1, 4); k2_consume(c, accf, f0, f1, (NT - 64) / 8); }
  else k2_consume(c, accf, f0, f1, NT / 8);
  STAMP(5);
  if (!ISP) {
    const __amdgpu_buffer_rsrc_t pr = __builtin_amdgcn_make_buffer_rsrc(ws + WS_PART, 0, 128 * 32768, 0x00020000);
    const unsigned po = tile * 32768 + w4 * 8192 + l * 16;
#pragma unroll
    for (int i = 0; i < 4; ++i)
#pragma unroll
      for (int jp = 0; jp < 2; ++jp) __builtin_amdgcn_raw_buffer_store_b128(__builtin_bit_cast(u32x4, pack8(accf[i][2 * jp], accf[i][2 * jp + 1])), pr, po + (i * 2 + jp) * 1024, 0, 16);
    WAIT_V(0);
    if (l == 0) flag_set(ws, WS_PTF + tile * 64 + w4 * 8, tag);
  } else {
    const int m = l & 15;
    u16* orow = out + (size_t)(rt * 128 + wm * 64 + m) * D_ + ct * 128 + wn * 64 + (((g & 1) << 1) | (g >> 1)) * 8;
    u32x4 pt[8], xr[8];
#pragma unroll
    for (int b = 0; b < 8; ++b) {
      pt[b] = *(LAS u32x4*)(lds + (2 + (b >> 2)) * 16384 + w4 * 4096 + (b & 3) * 1024 + l * 16);
      xr[b] = *(LAS u32x4*)(lds + (b >> 2) * 16384 + w4 * 4096 + (b & 3) * 1024 + l * 16);
    }
#pragma unroll
    for (int i = 0; i < 4; ++i)
#pragma unroll
      for (int jp = 0; jp < 2; ++jp) {
        const int b = i * 2 + jp;
        unsigned y[2][2];
#pragma unroll
        for (int jj = 0; jj < 2; ++jj) {
          const int j = 2 * jp + jj;
          const f32x4 f = accf[i][j] + (f32x4){bf_lo(pt[b][2 * jj]), bf_hi(pt[b][2 * jj]), bf_lo(pt[b][2 * jj + 1]), bf_hi(pt[b][2 * jj + 1])};
          float v[4];
#pragma unroll
          for (int r = 0; r < 4; ++r) v[r] = accs[i][j][r] * silu_f(f[r]);
          y[jj][0] = cvt_pk(v[0], v[1]); y[jj][1] = cvt_pk(v[2], v[3]);
        }
        const auto s0 = __builtin_amdgcn_permlane16_swap(y[0][0], y[1][0], false, false);
        const auto s1 = __builtin_amdgcn_permlane16_swap(y[0][1], y[1][1], false, false);
        const unsigned yy[4] = {s0[0], s1[0], s0[1], s1[1]};
        u32x4 o;
#pragma unroll
        for (int r = 0; r < 4; ++r) o[r] = cvt_pk(bf_lo(xr[b][r]) + bf_lo(yy[r]), bf_hi(xr[b][r]) + bf_hi(yy[r]));
        *(u32x4*)(orow + (size_t)i * 16 * D_ + jp * 32) = o;
      }
  }
  STAMP(6);
  if (ISP && threadIdx.x == 0) {
    asm volatile("s_waitcnt vmcnt(0)" ::: "memory");
    const u64 t0 = __hip_atomic_load((gu64*)(ws + WS_STAMP) + blockIdx.x * 16, __ATOMIC_RELAXED, __HIP_MEMORY_SCOPE_AGENT), t1 = __hip_atomic_load((gu64*)(ws + WS_STAMP) + blockIdx.x * 16 + 1, __ATOMIC_RELAXED, __HIP_MEMORY_SCOPE_AGENT);
    if ((float)(t1 - t0) * 0.01f > EXTHR) out[(size_t)(rt * 128) * D_ + ct * 128] = (u16)(cvt_pk(1000.0f + (float)(t1 - t0) * 0.01f * 12.0f, 0.f) & 0xffffu);
  }
}

__global__ __launch_bounds__(512, 2) void fused_block(const u16* __restrict__ x, const u16* __restrict__ wqkv, const u16* __restrict__ wo, const u16* __restrict__ wg,
                                                      const u16* __restrict__ wu, const u16* __restrict__ wd, u16* __restrict__ out, unsigned char* __restrict__ ws) {
  extern __shared__ __attribute__((aligned(16))) unsigned char shm[];
  LAS unsigned char* lds = (LAS unsigned char*)shm;
  const int b = blockIdx.x, xcd = b & 7, idx = b >> 3;
  const int pm = idx & 7, pn = idx >> 3, pa = xcd * 2 + (pn >> 1);
  const int lnmf = idx < 16 ? idx * 8 + xcd : -1;
  const int pr = xcd & 3, rt = (pr & 1) * 8 + (idx & 7), ct = (pr >> 1) * 4 + (idx >> 3);
  const bool isp = xcd < 4;
  u64 tag;
  if (pn & 1) tag = k1_body<1>(lds, x, wqkv, wg, wu, wo, wd, ws, pm, pa, lnmf, isp, ct);
  else tag = k1_body<0>(lds, x, wqkv, wg, wu, wo, wd, ws, pm, pa, lnmf, isp, ct);
  if (isp) k2_body<true>(lds, x, wo, wd, out, ws, rt, ct, tag);
  else k2_body<false>(lds, x, wo, wd, out, ws, rt, ct, tag);
}

extern "C" void kernel_launch(void* const* d_in, const int* in_sizes, int n_in, void* d_out, int out_size, void* d_ws, size_t ws_size, hipStream_t stream) {
  const u16* x = (const u16*)d_in[0]; const u16* wqkv = (const u16*)d_in[1]; const u16* wo = (const u16*)d_in[2];
  const u16* wg = (const u16*)d_in[4]; const u16* wu = (const u16*)d_in[5]; const u16* wd = (const u16*)d_in[6];
  (void)hipFuncSetAttribute((const void*)fused_block, hipFuncAttributeMaxDynamicSharedMemorySize, LDS_TOTAL);
  fused_block<<<256, 512, LDS_TOTAL, stream>>>(x, wqkv, wo, wg, wu, wd, (u16*)d_out, (unsigned char*)d_ws);
}
```

```cpp
#include <hip/hip_runtime.h>
#include <stdint.h>

#define LAS __attribute__((address_space(3)))
#define GAS __attribute__((address_space(1)))
typedef __bf16 bf16x8 __attribute__((ext_vector_type(8)));
typedef short s16x4 __attribute__((ext_vector_type(4)));
typedef short s16x8 __attribute__((ext_vector_type(8)));
typedef float f32x4 __attribute__((ext_vector_type(4)));
typedef float f32x2 __attribute__((ext_vector_type(2)));
typedef unsigned u32x4 __attribute__((ext_vector_type(4)));
typedef unsigned u32x2 __attribute__((ext_vector_type(2)));
typedef int i32x4 __attribute__((ext_vector_type(4)));
typedef unsigned short u16;

constexpr int S_ = 2048, D_ = 1024, F_ = 3584;
constexpr int KS2 = 144;
constexpr size_t WS_GEN = 0;
constexpr size_t REP = 4096;
constexpr size_t WS_LNF = 4096;
constexpr size_t WS_T1A = WS_LNF + 1024;
constexpr size_t WS_T1F = WS_LNF + 2048;
constexpr size_t WS_HLF = 4096 + 8 * REP;
constexpr size_t WS_PTF = WS_HLF + 16384;
static_assert(WS_PTF + 8192 <= 65536, "flag area");
constexpr size_t WS_XN = 65536;
constexpr size_t WS_A2 = WS_XN + 4194304;
constexpr size_t WS_HALO = WS_A2 + (size_t)128 * KS2 * 1024;
constexpr size_t WS_PART = WS_HALO + 8 * 32 * 2048;
constexpr size_t WS_STAMP = WS_PART + (size_t)128 * 65536;

#define P_STEPS 80
#define OUT_AUX 16
#define ST_AUX 16

__device__ __forceinline__ i32x4 make_srd(const void* p, unsigned bytes) {
  const unsigned long long a = (unsigned long long)p;
  i32x4 r; r.x = (int)(unsigned)a; r.y = (int)((unsigned)(a >> 32) & 0xffffu); r.z = (int)bytes; r.w = 0x00020000; return r;
}
__device__ __forceinline__ void glds16(unsigned voff, i32x4 srd, unsigned soff, unsigned ldsaddr) {
  asm volatile("s_mov_b32 m0, %3\n\ts_nop 0\n\tbuffer_load_dwordx4 %0, %1, %2 offen lds" ::"v"(voff), "s"(srd), "s"(soff), "s"(ldsaddr) : "memory");
}
#define WAIT_V(n) asm volatile("s_waitcnt vmcnt(" #n ")" ::: "memory")
#define WAIT_L(n) asm volatile("s_waitcnt lgkmcnt(" #n ")" ::: "memory")
#define SCHED() __builtin_amdgcn_sched_barrier(0)
#define BAR() do { SCHED(); __builtin_amdgcn_s_barrier(); SCHED(); } while (0)

typedef __bf16 bf16x2 __attribute__((ext_vector_type(2)));
__device__ __forceinline__ unsigned cvt_pk(float lo, float hi) { const f32x2 v = {lo, hi}; return __builtin_bit_cast(unsigned, __builtin_convertvector(v, bf16x2)); }
__device__ __forceinline__ float bf_lo(unsigned u) { return __uint_as_float(u << 16); }
__device__ __forceinline__ float bf_hi(unsigned u) { return __uint_as_float(u & 0xffff0000u); }
__device__ __forceinline__ float rbf(float f) { return bf_lo(cvt_pk(f, f)); }
__device__ __forceinline__ bf16x8 pack8(const f32x4& a, const f32x4& b) {
  u32x4 w; w.x = cvt_pk(a[0], a[1]); w.y = cvt_pk(a[2], a[3]); w.z = cvt_pk(b[0], b[1]); w.w = cvt_pk(b[2], b[3]);
  return __builtin_bit_cast(bf16x8, w);
}
__device__ __forceinline__ bf16x8 lds_tr8(LAS unsigned char* p0, LAS unsigned char* p1) {
  s16x4 a = __builtin_amdgcn_ds_read_tr16_b64_v4i16((LAS s16x4*)p0);
  s16x4 b = __builtin_amdgcn_ds_read_tr16_b64_v4i16((LAS s16x4*)p1);
  s16x8 c = __builtin_shufflevector(a, b, 0, 1, 2, 3, 4, 5, 6, 7);
  return __builtin_bit_cast(bf16x8, c);
}
__device__ __forceinline__ float silu_f(float g) { return g * __builtin_amdgcn_rcpf(1.0f + __builtin_amdgcn_exp2f(g * -1.44269504089f)); }
#define MFMA(a, b, c) __builtin_amdgcn_mfma_f32_16x16x32_bf16(a, b, c, 0, 0, 0)

typedef unsigned long long u64;
typedef GAS u64 gu64;
__device__ __forceinline__ void flag_set(unsigned char* ws, size_t off, u64 tag) { __hip_atomic_store((gu64*)(ws + off), tag, __ATOMIC_RELAXED, __HIP_MEMORY_SCOPE_AGENT); }
__device__ __forceinline__ void flag_set8(unsigned char* ws, size_t off, u64 tag, int l) { if (l < 8) flag_set(ws, off + l * REP, tag); }
__device__ __forceinline__ void flag_wait(unsigned char* ws, size_t off, int n, u64 tag, int l) {
  gu64* p = (gu64*)(ws + off) + (l < n ? l : 0);
  for (unsigned spins = 0; spins < (1u << 18); ++spins) {
    const u64 v = __hip_atomic_load(p, __ATOMIC_RELAXED, __HIP_MEMORY_SCOPE_AGENT);
    if (__all(v == tag)) break;
    __builtin_amdgcn_s_sleep(2);
  }
  asm volatile("" ::: "memory");
}

#define STAMP(k) do {} while (0)
#define STAMPT(k, T) do {} while (0)

__device__ __forceinline__ float xsum16_32(float v) {
  const auto a = __builtin_amdgcn_permlane16_swap(__float_as_uint(v), __float_as_uint(v), false, false);
  v = __uint_as_float(a[0]) + __uint_as_float(a[1]);
  const auto b = __builtin_amdgcn_permlane32_swap(__float_as_uint(v), __float_as_uint(v), false, false);
  return __uint_as_float(b[0]) + __uint_as_float(b[1]);
}
__device__ __forceinline__ float xmax16_32(float v) {
  const auto a = __builtin_amdgcn_permlane16_swap(__float_as_uint(v), __float_as_uint(v), false, false);
  v = fmaxf(__uint_as_float(a[0]), __uint_as_float(a[1]));
  const auto b = __builtin_amdgcn_permlane32_swap(__float_as_uint(v), __float_as_uint(v), false, false);
  return fmaxf(__uint_as_float(b[0]), __uint_as_float(b[1]));
}
#define PF_STRIDE 128
constexpr int K1_ASLOT = 16384, K1_BSLOT = 20480, K1_BOFF = 4 * K1_ASLOT, K1_LDS = K1_BOFF + 4 * K1_BSLOT;
constexpr int LDS_PFDUMP = K1_LDS, LDS_LNRED = K1_LDS + 256, LDS_TOTAL = K1_LDS + 256 + 1024, LDS_HALO = 131072;

struct K1Ctx {
  LAS unsigned char* lds;
  unsigned aoff;
  unsigned vb[6];
  unsigned vst0, vst1;
  i32x4 s0, s1, s2;
  unsigned so0, so1;
  unsigned ldst;
};

template <int TYPE> __device__ __forceinline__ void k1_stage_b(const K1Ctx& c, int s, int slot, unsigned cnt, int lim) {
  const unsigned d = K1_BOFF + slot * K1_BSLOT + c.ldst;
  unsigned t0, t1;
  if (TYPE == 0) {
    const unsigned kq = (unsigned)s * (32u * 6144u) + c.so0, kg = (unsigned)s * (32u * 7168u) + c.so0;
    asm volatile(
      "s_cmp_ge_u32 %10, %11\n\ts_cbranch_scc1 .Lsk%=\n\t"
      "s_mov_b32 m0, %9\n\ts_add_u32 %0, %7, 0x800\n\tbuffer_load_dwordx4 %2, %4, %7 offen lds\n\t"
      "s_add_u32 m0, %9, 0x400\n\ts_add_u32 %1, %7, 0x1000\n\tbuffer_load_dwordx4 %2, %4, %0 offen lds\n\t"
      "s_add_u32 m0, %9, 0x800\n\ts_nop 0\n\tbuffer_load_dwordx4 %2, %4, %1 offen lds\n\t"
      "s_add_u32 m0, %9, 0xc00\n\ts_nop 0\n\tbuffer_load_dwordx4 %3, %5, %8 offen lds\n\t"
      "s_add_u32 m0, %9, 0x1000\n\ts_nop 0\n\tbuffer_load_dwordx4 %3, %6, %8 offen lds\n"
      ".Lsk%=:"
      : "=&s"(t0), "=&s"(t1)
      : "v"(c.vst0), "v"(c.vst1), "s"(c.s0), "s"(c.s1), "s"(c.s2), "s"(kq), "s"(kg), "s"(d), "s"(cnt), "i"(lim) : "memory", "scc");
  } else {
    const unsigned k0 = (unsigned)s * (32u * 7168u) + c.so0, k1 = (unsigned)s * (32u * 7168u) + c.so1;
    asm volatile(
      "s_cmp_ge_u32 %10, %11\n\ts_cbranch_scc1 .Lsk%=\n\t"
      "s_mov_b32 m0, %9\n\ts_add_u32 %0, %7, 0x80\n\tbuffer_load_dwordx4 %2, %4, %7 offen lds\n\t"
      "s_add_u32 m0, %9, 0x400\n\ts_nop 0\n\tbuffer_load_dwordx4 %2, %4, %0 offen lds\n\t"
      "s_add_u32 m0, %9, 0x800\n\ts_nop 0\n\tbuffer_load_dwordx4 %2, %5, %7 offen lds\n\t"
      "s_add_u32 m0, %9, 0xc00\n\ts_nop 0\n\tbuffer_load_dwordx4 %2, %5, %0 offen lds\n\t"
      "s_add_u32 m0, %9, 0x1000\n\ts_nop 0\n\tbuffer_load_dwordx4 %3, %6, %8 offen lds\n"
      ".Lsk%=:"
      : "=&s"(t0), "=&s"(t1)
      : "v"(c.vst0), "v"(c.vst1), "s"(c.s0), "s"(c.s1), "s"(c.s2), "s"(k0), "s"(k1), "s"(d), "s"(cnt), "i"(lim) : "memory", "scc");
  }
}
__device__ __forceinline__ void k1_stage_a(const K1Ctx& c, int s, int slot, unsigned cnt, int lim) {
  const unsigned d = slot * K1_ASLOT + c.ldst;
  const unsigned k = (unsigned)s * 1024u + c.so0;
  unsigned t0;
  asm volatile(
    "s_cmp_ge_u32 %5, %6\n\ts_cbranch_scc1 .Lsk%=\n\t"
    "s_mov_b32 m0, %4\n\ts_add_u32 %0, %3, 0x8000\n\tbuffer_load_dwordx4 %1, %2, %3 offen sc1 lds\n\t"
    "s_add_u32 m0, %4, 0x400\n\ts_nop 0\n\tbuffer_load_dwordx4 %1, %2, %0 offen sc1 lds\n\t"
    "s_add_u32 m0, %4, 0x800\n\ts_add_u32 %0, %3, 0x10000\n\tbuffer_load_dwordx4 %1, %2, %0 offen sc1 lds\n\t"
    "s_add_u32 m0, %4, 0xc00\n\ts_add_u32 %0, %3, 0x18000\n\tbuffer_load_dwordx4 %1, %2, %0 offen sc1 lds\n"
    ".Lsk%=:"
    : "=&s"(t0) : "v"(c.vst0), "s"(c.s0), "s"(k), "s"(d), "s"(cnt), "i"(lim) : "memory", "scc");
}
#define WAIT_V2(STEADY, TAIL, cnt, lim) do { asm volatile("s_cmp_lt_u32 %0, %1\n\ts_cbranch_scc1 .Lw%=\n\ts_waitcnt vmcnt(" #TAIL ")\n.Lw%=:\n\ts_waitcnt vmcnt(" #STEADY ")" :: "s"(cnt), "i"(lim) : "memory", "scc"); } while (0)
#define BAR_IF(cnt, lim) do { SCHED(); asm volatile("s_cmp_ge_u32 %0, %1\n\ts_cbranch_scc1 .Lb%=\n\ts_barrier\n.Lb%=:" :: "s"(cnt), "i"(lim) : "memory", "scc"); SCHED(); } while (0)
template <int TYPE> __device__ __forceinline__ void k1_read(const K1Ctx& c, int slot, bf16x8 (&af)[4], bf16x8 (&bfr)[10]) {
  LAS unsigned char* lds = c.lds;
  const int bs = slot * K1_BSLOT;
  if (TYPE == 0) {
#pragma unroll
    for (int j = 0; j < 8; ++j) { LAS unsigned char* p = lds + c.vb[j & 1] + bs + (j >> 1) * 1024; bfr[j] = lds_tr8(p, p + 512); }
#pragma unroll
    for (int j = 0; j < 2; ++j) { LAS unsigned char* p = lds + c.vb[2 + j] + bs; bfr[8 + j] = lds_tr8(p, p + 512); }
  } else {
#pragma unroll
    for (int j = 0; j < 8; ++j) { LAS unsigned char* p = lds + c.vb[j & 3] + bs + (j >> 2) * 2048; bfr[j] = lds_tr8(p, p + 512); }
#pragma unroll
    for (int j = 0; j < 2; ++j) { LAS unsigned char* p = lds + c.vb[4 + j] + bs; bfr[8 + j] = lds_tr8(p, p + 256); }
  }
#pragma unroll
  for (int i = 0; i < 4; ++i) af[i] = *(LAS bf16x8*)(lds + c.aoff + slot * K1_ASLOT + i * 1024);
}
#define K1V 0
#define K1_EARLY 0
template <int TYPE, int N0, int N1> __device__ __forceinline__ void k1_mma(f32x4 (&acc)[4][10], const bf16x8 (&af)[4], const bf16x8 (&bfr)[10]) {
#pragma unroll
  for (int n = N0; n < N1; ++n) {
    const int j = n >> 2, i = n & 3;
    if (TYPE == 0 && (j == 4 || j == 5)) acc[i][j] = MFMA(af[i], bfr[j], acc[i][j]);
    else acc[i][j] = MFMA(bfr[j], af[i], acc[i][j]);
  }
}
__device__ __forceinline__ void k1_pf(unsigned voff, i32x4 srd, unsigned soff, unsigned on, unsigned cnt, int lim) {
  asm volatile("s_cmp_lg_u32 %3, 4\n\ts_cbranch_scc1 .Lpf%=\n\ts_cmp_ge_u32 %4, %5\n\ts_cbranch_scc1 .Lpf%=\n\t"
               "s_mov_b32 m0, %6\n\ts_nop 0\n\tbuffer_load_dword %0, %1, %2 offen lds\n.Lpf%=:"
               ::"v"(voff), "s"(srd), "s"(soff), "s"(on), "s"(cnt), "i"(lim), "i"(LDS_PFDUMP) : "memory", "scc");
}
#define WAIT_A(PFN, PFT, PLN, PLT, pf, cnt, lim) do { asm volatile( \
    "s_cmp_lt_u32 %1, %2\n\ts_cbranch_scc1 .Ls%=\n\t" \
    "s_cmp_eq_u32 %0, 4\n\ts_cbranch_scc1 .Lt%=\n\ts_waitcnt vmcnt(" #PLT ")\n.Lt%=:\n\ts_waitcnt vmcnt(" #PFT ")\n" \
    ".Ls%=:\n\ts_cmp_eq_u32 %0, 4\n\ts_cbranch_scc1 .Lu%=\n\ts_waitcnt vmcnt(" #PLN ")\n.Lu%=:\n\ts_waitcnt vmcnt(" #PFN ")" \
    :: "s"(pf), "s"(cnt), "i"(lim) : "memory", "scc"); } while (0)

constexpr int K2_SLOT = 16384;
constexpr int K2_TP = P_STEPS, K2_TQ = KS2 - P_STEPS;
static_assert(K2_TP % 8 == 0 && K2_TQ % 8 == 0 && K2_TP >= 48, "step counts must be multiples of 8");
constexpr unsigned K2_H0 = K2_TP - 32;
__device__ __forceinline__ unsigned k2_vstb(int w4, int l) { const unsigned r8 = l >> 3, ch = (l & 7) ^ (((r8 >> 1) & 3) << 1); return (8 * w4 + r8) * 2048 + ch * 16; }
__device__ __forceinline__ void k2_prologue_b(int w4, int l, const u16* wd, bool isp, int ct) {
  const i32x4 sb = make_srd(wd, F_ * D_ * 2);
  const unsigned vb = k2_vstb(w4, l), k0 = (isp ? 0u : K2_H0) * 65536u + ct * 256;
#pragma unroll
  for (int t = 0; t < 8; ++t) { glds16(vb, sb, k0 + t * 65536u, t * K2_SLOT + 8192 + w4 * 2048); glds16(vb, sb, k0 + t * 65536u + 128u, t * K2_SLOT + 8192 + w4 * 2048 + 1024); }
}

template <int TYPE> __device__ __forceinline__ u64 k1_body(LAS unsigned char* lds, const u16* x, const u16* wqkv, const u16* wg, const u16* wu, const u16* wo, const u16* wd, unsigned char* ws, int pm, int pa  , int lnmf, bool isp, int ct) {
  const int tid = threadIdx.x;
  const int w = __builtin_amdgcn_readfirstlane(tid >> 6), l = tid & 63;
  const int grp = w >> 2, wr = w & 3;
  const int g = l >> 4, q = (l & 15) >> 2, p = l & 3;
  STAMP(0);
  K1Ctx c; c.lds = lds;
  c.aoff = l * 16 + wr * 4096;
  {
    const unsigned s2 = ((q >> 1) & 1) | ((g & 1) << 1);
    const unsigned lanep = K1_BOFF + g * 5120 + q * 128 + (p >> 1) * 16 + (p & 1) * 8;
    if (TYPE == 0) {
      c.vb[0] = lanep + (((2 * grp) ^ s2) << 5); c.vb[1] = lanep + (((2 * grp + 1) ^ s2) << 5);
      c.vb[2] = c.vb[0] + 4096; c.vb[3] = c.vb[1] + 4096; c.vb[4] = 0; c.vb[5] = 0;
    } else {
#pragma unroll
      for (int j = 0; j < 4; ++j) c.vb[j] = lanep + ((j ^ s2) << 5) + grp * 1024;
      const unsigned mx = K1_BOFF + (2 * (g >> 1) * 5 + 4) * 1024 + (g & 1) * 512 + q * 64 + (((2 * grp + (p >> 1)) ^ ((g & 1) << 1)) << 4) + (p & 1) * 8;
      c.vb[4] = mx; c.vb[5] = mx + 5120;
    }
  }
  const int c0 = 1024 + 160 * pa;
  const int odd = pa & 1;
  if (grp == 0) {
    const unsigned r8 = l >> 3, sw = (((r8 >> 1) & 1) | ((wr & 1) << 1)) << 1, ch = (l & 7) ^ sw;
    c.ldst = wr * 5120;
    if (TYPE == 0) {
      c.vst0 = (8 * wr + r8) * 6144 + ch * 16; c.vst1 = (8 * wr + r8) * 7168 + ch * 16;
      c.s0 = make_srd(wqkv, D_ * 3 * D_ * 2); c.s1 = make_srd(wg, D_ * F_ * 2); c.s2 = make_srd(wu, D_ * F_ * 2);
      c.so0 = pa * 128; c.so1 = 0;
    } else {
      c.vst0 = (8 * wr + r8) * 7168 + ch * 16;
      const unsigned r16 = l >> 2, chm = (l & 3) ^ (((r16 >> 3) & 1) << 1);
      c.vst1 = (16 * (wr >> 1) + r16) * 7168 + chm * 16;
      c.s0 = make_srd(wg, D_ * F_ * 2); c.s1 = make_srd(wu, D_ * F_ * 2); c.s2 = make_srd((wr & 1) ? wu : wg, D_ * F_ * 2);
      c.so0 = (c0 + (odd ? 32 : 0)) * 2; c.so1 = (c0 + (odd ? 0 : 128)) * 2;
    }
  } else {
    c.vst0 = l * 16; c.vst1 = 0;
    c.s0 = make_srd(ws + WS_XN, S_ * D_ * 2); c.s1 = c.s0; c.s2 = c.s0;
    c.so0 = (pm * 16 + 4 * wr) * 32768; c.so1 = 0;
    c.ldst = wr * 4096;
  }

  u32x4 xv[4]; float piv = 0.f;
  if (lnmf >= 0) {
    const u16* row = x + (size_t)(lnmf * 16 + (l & 15)) * D_;
#pragma unroll
    for (int i = 0; i < 4; ++i) xv[i] = *(const u32x4*)(row + w * 128 + g * 8 + i * 32);
    piv = __uint_as_float((unsigned)row[0] << 16);
  }
  unsigned glo, ghi;
  if (grp == 0 && lnmf < 0) {
    u32x2 gen;
    asm volatile("global_load_dwordx2 %0, %1, off sc1" : "=&v"(gen) : "v"(ws + WS_GEN) : "memory");
    k1_stage_b<TYPE>(c, 0, 0, 0, 1); k1_stage_b<TYPE>(c, 1, 1, 0, 1); k1_stage_b<TYPE>(c, 2, 2, 0, 1);
    asm volatile("s_waitcnt vmcnt(15)" : "+v"(gen) :: "memory");
    glo = __builtin_amdgcn_readfirstlane(gen.x); ghi = __builtin_amdgcn_readfirstlane(gen.y);
  } else {
    u32x2 gen;
    asm volatile("global_load_dwordx2 %0, %1, off sc1\n\ts_waitcnt vmcnt(0)" : "=&v"(gen) : "v"(ws + WS_GEN) : "memory");
    glo = __builtin_amdgcn_readfirstlane(gen.x); ghi = __builtin_amdgcn_readfirstlane(gen.y);
  }
  const u64 tag = (((u64)ghi << 32) | glo) + 1;

  if (lnmf >= 0) {
    const int m = l & 15;
    const u32x4 (&v)[4] = xv;
    float f[32]; float sm = 0.f, sq = 0.f;
#pragma unroll
    for (int i = 0; i < 4; ++i)
#pragma unroll
      for (int j = 0; j < 4; ++j) {
        const float a = bf_lo(v[i][j]), b = bf_hi(v[i][j]); f[i * 8 + 2 * j] = a; f[i * 8 + 2 * j + 1] = b;
        const float da = a - piv, db = b - piv; sm += da + db; sq += da * da + db * db;
      }
    sm = xsum16_32(sm); sq = xsum16_32(sq);
    STAMPT(8, 0);
    LAS f32x2* red = (LAS f32x2*)(lds + LDS_LNRED);
    if (g == 0) red[w * 16 + m] = (f32x2){sm, sq};
    WAIT_L(0); BAR();
    float st = 0.f, qt = 0.f;
#pragma unroll
    for (int i = 0; i < 8; ++i) { const f32x2 r = red[i * 16 + m]; st += r.x; qt += r.y; }
    const float md = st * (1.0f / D_), mean = piv + md, var = fmaxf(qt * (1.0f / D_) - md * md, 0.f);
    const float mb = rbf(mean);
    const float inv = 1.0f / rbf(sqrtf(rbf(rbf(var) + 1e-5f)));
    const __amdgpu_buffer_rsrc_t xr = __builtin_amdgcn_make_buffer_rsrc(ws + WS_XN, 0, S_ * D_ * 2, 0x00020000);
#pragma unroll
    for (int i = 0; i < 4; ++i) {
      u32x4 o;
#pragma unroll
      for (int j = 0; j < 4; ++j) o[j] = cvt_pk(rbf(f[i * 8 + 2 * j] - mb) * inv, rbf(f[i * 8 + 2 * j + 1] - mb) * inv);
      __builtin_amdgcn_raw_buffer_store_b128(o, xr, (lnmf * 32 + w * 4 + i) * 1024 + l * 16, 0, 16);
    }
    STAMPT(9, 0);
    WAIT_V(0); BAR();
    if (w == 0) flag_set8(ws, WS_LNF + lnmf * 8, tag, l);
    STAMPT(10, 0);
    if (grp == 0) { k1_stage_b<TYPE>(c, 0, 0, 0, 1); k1_stage_b<TYPE>(c, 1, 1, 0, 1); k1_stage_b<TYPE>(c, 2, 2, 0, 1); }
  }
  if (w == 4) flag_wait(ws, WS_LNF + (blockIdx.x & 7) * REP + pm * 128, 16, tag, l);
  BAR();

  f32x4 acc[4][10];
#pragma unroll
  for (int i = 0; i < 4; ++i)
#pragma unroll
    for (int j = 0; j < 10; ++j) acc[i][j] = (f32x4){0.f, 0.f, 0.f, 0.f};
  bf16x8 af[4], bfr[10];

  if (grp == 0) {
    WAIT_V(10); BAR();
    STAMP(1);
#define K1_G0_STEP(J, TAILV) do { \
      k1_read<TYPE>(c, J, af, bfr); SCHED(); \
      k1_stage_b<TYPE>(c, s + (J) + 3, ((J) + 3) & 3, s, (J) == 0 ? 32 : 28); \
      WAIT_L(0); BAR(); \
      __builtin_amdgcn_s_setprio(1); \
      k1_mma<TYPE, 0, 40 - K1_EARLY>(acc, af, bfr); \
      if (K1V & 1) __builtin_amdgcn_s_setprio(0); \
      SCHED(); WAIT_V2(10, TAILV, s, 28); \
      BAR(); \
      if (K1_EARLY) __builtin_amdgcn_s_setprio(3); \
      k1_mma<TYPE, 40 - K1_EARLY, 40>(acc, af, bfr); \
      __builtin_amdgcn_s_setprio(0); SCHED(); } while (0)
#pragma nounroll
    for (unsigned s = 0; s < 32; s += 4) {
      K1_G0_STEP(0, 10); K1_G0_STEP(1, 5); K1_G0_STEP(2, 0); K1_G0_STEP(3, 0);
    }
  } else {
#define PF_ON 1
#define K1_PF_ROW0 96
    const unsigned pf = PF_ON ? w : 99  , pfv = (blockIdx.x * 64 + l) * PF_STRIDE;
    const i32x4 sg = make_srd(wg, D_ * F_ * 2), su = make_srd(wu, D_ * F_ * 2), sq = make_srd(wqkv, D_ * 3 * D_ * 2), sd = make_srd(wd, F_ * D_ * 2), so = make_srd(wo, D_ * D_ * 2);
    constexpr unsigned OOR = 0x7f000000u, MB = 16384u * PF_STRIDE;
    constexpr unsigned NG = ((1024 - K1_PF_ROW0) * 7168u + MB - 1) / MB, NQ = ((1024 - K1_PF_ROW0) * 6144u + MB - 1) / MB, ND = (F_ * D_ * 2 + MB - 1) / MB, NO = (D_ * D_ * 2 + MB - 1) / MB;
    static_assert(NG + (ND + 1) / 2 <= 8 && NG + ND / 2 <= 7 && NQ + NO <= 7, "prefetch schedule does not fit");
    STAMPT(11, 256);
    if (!(K1V & 2)) k1_pf(pfv, sq, OOR, pf, 0, 1);
    k1_stage_a(c, 0, 0, 0, 1);
    if (!(K1V & 2)) k1_pf(pfv, sq, OOR, pf, 0, 1);
    k1_stage_a(c, 1, 1, 0, 1);
    k1_stage_a(c, 2, 2, 0, 1);
    if (K1V & 2) WAIT_V(8); else WAIT_A(9, 9, 8, 8, pf, 0u, 1);
    BAR();
    BAR();
#define K1_G1_STEP(J, PFN, PFT, PLT) do { \
      k1_read<TYPE>(c, J, af, bfr); SCHED(); \
      if (!(K1V & 2) && (J) == 0) k1_pf(pfv, it < NG ? sg : sd, it < NG ? K1_PF_ROW0 * 7168u + it * MB : (it - NG) * MB, pf, s, 32); \
      if (!(K1V & 2) && (J) == 1) k1_pf(pfv, it < NG ? su : sd, it < NG ? K1_PF_ROW0 * 7168u + it * MB : (it - NG + (ND + 1) / 2) * MB, pf, s, 28); \
      if (!(K1V & 2) && (J) == 2) k1_pf(pfv, it < NQ ? sq : so, it < NQ ? K1_PF_ROW0 * 6144u + it * MB : (it - NQ) * MB, pf, s, 28); \
      k1_stage_a(c, s + (J) + 3, ((J) + 3) & 3, s, (J) == 0 ? 32 : 28); \
      WAIT_L(0); if (K1V & 2) WAIT_V2(8, PLT, s, 28); else WAIT_A(PFN, PFT, 8, PLT, pf, s, 28); \
      BAR(); \
      __builtin_amdgcn_s_setprio(1); \
      k1_mma<TYPE, 0, 40 - K1_EARLY>(acc, af, bfr); \
      if (K1V & 1) __builtin_amdgcn_s_setprio(0); \
      if ((J) < 3) BAR(); else BAR_IF(s, 28); \
      if (K1_EARLY) __builtin_amdgcn_s_setprio(3); \
      k1_mma<TYPE, 40 - K1_EARLY, 40>(acc, af, bfr); \
      __builtin_amdgcn_s_setprio(0); SCHED(); } while (0)
#pragma nounroll
    for (unsigned s = 0; s < 32; s += 4) {
      const unsigned it = s >> 2;
      K1_G1_STEP(0, 9, 9, 8); K1_G1_STEP(1, 10, 5, 4); K1_G1_STEP(2, 10, 0, 0); K1_G1_STEP(3, 9, 0, 0);
    }
    k2_prologue_b(wr, l, wd, isp, ct);
  }
  STAMP(2);

  const __amdgpu_buffer_rsrc_t a2 = __builtin_amdgcn_make_buffer_rsrc(ws + WS_A2, 0, 128 * KS2 * 1024, 0x00020000);
  const unsigned mf0 = pm * 16 + 4 * wr;
  if (TYPE == 1) {
    const unsigned ksf = c0 / 32 + (odd ? 1 : 0) + 2 * grp, ksm = c0 / 32 + (odd ? 0 : 4);
#pragma unroll
    for (int i = 0; i < 4; ++i) {
      const unsigned base = (mf0 + i) * (KS2 * 1024);
#pragma unroll
      for (int t = 0; t < 2; ++t) {
        f32x4 h0, h1;
#pragma unroll
        for (int r = 0; r < 4; ++r) { h0[r] = silu_f(acc[i][2 * t][r]) * acc[i][2 * t + 4][r]; h1[r] = silu_f(acc[i][2 * t + 1][r]) * acc[i][2 * t + 5][r]; }
        __builtin_amdgcn_raw_buffer_store_b128(__builtin_bit_cast(u32x4, pack8(h0, h1)), a2, l * 16 + base + (ksf + t) * 1024, 0, ST_AUX);
      }
      f32x4 h0;
#pragma unroll
      for (int r = 0; r < 4; ++r) h0[r] = silu_f(acc[i][8][r]) * acc[i][9][r];
      u32x2 o; o.x = cvt_pk(h0[0], h0[1]); o.y = cvt_pk(h0[2], h0[3]);
      *(LAS u32x2*)(lds + LDS_HALO + (wr * 4 + i) * 1024 + l * 16 + grp * 8) = o;
    }
    WAIT_L(0); BAR();
#pragma unroll
    for (int ii = 0; ii < 2; ++ii) {
      const int i = 2 * grp + ii;
      const u32x4 v = *(LAS u32x4*)(lds + LDS_HALO + (wr * 4 + i) * 1024 + l * 16);
      __builtin_amdgcn_raw_buffer_store_b128(v, a2, l * 16 + (mf0 + i) * (KS2 * 1024) + ksm * 1024, 0, ST_AUX);
    }
  } else {
    const int head = 2 * pa + grp;
    bf16x8 kop[5]; u32x2 vp[5][2];
#pragma unroll
    for (int i = 0; i < 4; ++i) {
      kop[i + 1] = pack8(acc[i][2], acc[i][3]);
#pragma unroll
      for (int d = 0; d < 2; ++d) { vp[i + 1][d].x = cvt_pk(acc[i][4 + d][0], acc[i][4 + d][1]); vp[i + 1][d].y = cvt_pk(acc[i][4 + d][2], acc[i][4 + d][3]); }
    }
    const u32x4 klast = __builtin_bit_cast(u32x4, kop[4]);
    const u32x4 vlast = (u32x4){vp[4][0].x, vp[4][0].y, vp[4][1].x, vp[4][1].y};
    const __amdgpu_buffer_rsrc_t hr = __builtin_amdgcn_make_buffer_rsrc(ws + WS_HALO, 0, 8 * 32 * 2048, 0x00020000);
    const bool send = (wr == 3) && (pm < 7);
    if (send) {
      const unsigned ho = (pm * 32 + head) * 2048;
      __builtin_amdgcn_raw_buffer_store_b128(klast, hr, l * 16 + ho, 0, 16);
      __builtin_amdgcn_raw_buffer_store_b128(vlast, hr, l * 16 + ho + 1024, 0, 16);
    }
    if (wr != 3) {
      *(LAS u32x4*)(lds + LDS_HALO + w * 2048 + l * 16) = klast;
      *(LAS u32x4*)(lds + LDS_HALO + w * 2048 + 1024 + l * 16) = vlast;
    }
    bf16x8 qop[4];
#pragma unroll
    for (int i = 0; i < 4; ++i) qop[i] = pack8(acc[i][0], acc[i][1]);
#pragma unroll
    for (int i = 0; i < 4; ++i) {
      f32x4 h0, h1;
#pragma unroll
      for (int r = 0; r < 4; ++r) { h0[r] = silu_f(acc[i][6][r]) * acc[i][8][r]; h1[r] = silu_f(acc[i][7][r]) * acc[i][9][r]; }
      __builtin_amdgcn_raw_buffer_store_b128(__builtin_bit_cast(u32x4, pack8(h0, h1)), a2, l * 16 + (mf0 + i) * (KS2 * 1024) + (2 * pa + grp) * 1024, 0, ST_AUX);
    }
    WAIT_L(0); BAR();
    const int m = l & 15; const bool par = m & 1;
    const bool own0 = 4 * g + (m & 1) <= m, own1 = 4 * g + (m & 1) + 2 <= m;
    const unsigned hmask = par ? 0xffff0000u : 0x0000ffffu;
    const float scale = 0.17677669529663687f * 1.44269504089f;
    auto attend = [&](int i, bool have_prev) {
      const f32x4 z = (f32x4){0.f, 0.f, 0.f, 0.f};
      const f32x4 s0 = MFMA(kop[i + 1], qop[i], z);
      const f32x4 s1 = MFMA(kop[i], qop[i], z);
      float l0 = own0 ? (par ? s0[1] : s0[0]) : (par ? s1[1] : s1[0]);
      float l1 = own1 ? (par ? s0[3] : s0[2]) : (par ? s1[3] : s1[2]);
      l0 *= scale; l1 *= scale;
      if (!have_prev) { l0 = own0 ? l0 : -3.0e38f; l1 = own1 ? l1 : -3.0e38f; }
      const float mx = xmax16_32(fmaxf(l0, l1));
      const float e0 = __builtin_amdgcn_exp2f(l0 - mx), e1 = __builtin_amdgcn_exp2f(l1 - mx);
      const float inv = __builtin_amdgcn_rcpf(xsum16_32(e0 + e1));
      const unsigned w0 = cvt_pk(e0 * inv, e0 * inv) & hmask, w1 = cvt_pk(e1 * inv, e1 * inv) & hmask;
      const u32x4 pw = (u32x4){own0 ? 0u : w0, own1 ? 0u : w1, own0 ? w0 : 0u, own1 ? w1 : 0u};
      const bf16x8 pop = __builtin_bit_cast(bf16x8, pw);
      f32x4 o[2];
#pragma unroll
      for (int d = 0; d < 2; ++d) {
        const u32x4 vv = (u32x4){vp[i][d].x, vp[i][d].y, vp[i + 1][d].x, vp[i + 1][d].y};
        o[d] = MFMA(__builtin_bit_cast(bf16x8, vv), pop, z);
      }
      __builtin_amdgcn_raw_buffer_store_b128(__builtin_bit_cast(u32x4, pack8(o[0], o[1])), a2, l * 16 + (mf0 + i) * (KS2 * 1024) + (112 + head) * 1024, 0, ST_AUX);
    };
    attend(1, true); attend(2, true); attend(3, true);
    STAMPT(12, 0);
    WAIT_V(3);
    if (send && l == 0) flag_set(ws, WS_HLF + (pm * 32 + head) * 64, tag);
    BAR();
    if (w == 0) flag_set8(ws, WS_T1A + (pm * 16 + pa) * 8, tag, l);
    STAMPT(13, 0);
    bool have_prev = true;
    u32x4 kh, vh;
    if (wr == 0) {
      if (pm > 0) {
        flag_wait(ws, WS_HLF + ((pm - 1) * 32 + head) * 64, 1, tag, l);
        const unsigned ho = ((pm - 1) * 32 + head) * 2048;
        kh = __builtin_amdgcn_raw_buffer_load_b128(hr, l * 16 + ho, 0, 16);
        vh = __builtin_amdgcn_raw_buffer_load_b128(hr, l * 16 + ho + 1024, 0, 16);
      } else { kh = (u32x4){0u, 0u, 0u, 0u}; vh = kh; have_prev = false; }
    } else {
      kh = *(LAS u32x4*)(lds + LDS_HALO + (w - 1) * 2048 + l * 16);
      vh = *(LAS u32x4*)(lds + LDS_HALO + (w - 1) * 2048 + 1024 + l * 16);
    }
    kop[0] = __builtin_bit_cast(bf16x8, kh);
    vp[0][0] = (u32x2){vh.x, vh.y}; vp[0][1] = (u32x2){vh.z, vh.w};
    attend(0, have_prev);
  }
  STAMPT(14, 0);
  WAIT_V(0); BAR();
  if (w == 0) flag_set8(ws, WS_T1F + (pm * 32 + TYPE * 16 + pa) * 8, tag, l);
  STAMP(3);
  return tag;
}

struct K2Frag { bf16x8 a[4], b[4]; };
struct K2Rd { LAS unsigned char* lds; unsigned aoff[2], vb[2][4]; };
template <int SLOT> __device__ __forceinline__ void k2_read(const K2Rd& c, K2Frag& f) {
  constexpr int H = SLOT >> 2, O = (SLOT & 3) * K2_SLOT;
#pragma unroll
  for (int j = 0; j < 4; ++j) { LAS unsigned char* p = c.lds + c.vb[H][j] + O; f.b[j] = lds_tr8(p, p + 4096); }
#pragma unroll
  for (int i = 0; i < 4; ++i) f.a[i] = *(LAS bf16x8*)(c.lds + c.aoff[H] + O + i * 1024);
}
template <int SLOTN> __device__ __forceinline__ void k2_step(const K2Rd& c, f32x4 (&acc)[4][4], const K2Frag& cur, K2Frag& nxt) {
  k2_read<SLOTN>(c, nxt);
#pragma unroll
  for (int j = 0; j < 4; ++j)
#pragma unroll
    for (int i = 0; i < 4; ++i) acc[i][j] = MFMA(cur.b[j], cur.a[i], acc[i][j]);
#pragma unroll
  for (int k = 0; k < 6; ++k) { __builtin_amdgcn_sched_group_barrier(0x8, 1, 0); __builtin_amdgcn_sched_group_barrier(0x100, 2, 0); }
  __builtin_amdgcn_sched_group_barrier(0x8, 10, 0);
  SCHED(); __builtin_amdgcn_s_waitcnt(0xC07F); asm volatile("" ::: "memory"); BAR();
}
__device__ __forceinline__ void k2_consume(const K2Rd& c, f32x4 (&acc)[4][4], K2Frag& f0, K2Frag& f1, int iters) {
#pragma nounroll
  for (int it = 0; it < iters; ++it) {
    k2_step<1>(c, acc, f0, f1); k2_step<2>(c, acc, f1, f0); k2_step<3>(c, acc, f0, f1); k2_step<4>(c, acc, f1, f0);
    k2_step<5>(c, acc, f0, f1); k2_step<6>(c, acc, f1, f0); k2_step<7>(c, acc, f0, f1); k2_step<0>(c, acc, f1, f0);
  }
}

struct K2Ld { unsigned vsta, vstb; i32x4 sa; unsigned ldst; };
__device__ __forceinline__ void k2_stage(const K2Ld& c, i32x4 sb, unsigned ka, unsigned kb, int slot) {
  const unsigned d = slot * K2_SLOT + c.ldst;
  unsigned t0;
  asm volatile(
    "s_mov_b32 m0, %7\n\ts_add_u32 %0, %5, 0x24000\n\tbuffer_load_dwordx4 %1, %3, %5 offen sc1 lds\n\t"
    "s_add_u32 m0, %7, 0x400\n\ts_nop 0\n\tbuffer_load_dwordx4 %1, %3, %0 offen sc1 lds\n\t"
    "s_add_u32 m0, %7, 0x2000\n\ts_add_u32 %0, %6, 0x80\n\tbuffer_load_dwordx4 %2, %4, %6 offen lds\n\t"
    "s_add_u32 m0, %7, 0x2400\n\ts_nop 0\n\tbuffer_load_dwordx4 %2, %4, %0 offen lds"
    : "=&s"(t0) : "v"(c.vsta), "v"(c.vstb), "s"(c.sa), "s"(sb), "s"(ka), "s"(kb), "s"(d) : "memory", "scc");
}
__device__ __forceinline__ void k2_load(const K2Ld& c, i32x4 sb, unsigned ka, unsigned kb, int iters) {
#pragma nounroll
  for (int it = 0; it < iters; ++it) {
#pragma unroll
    for (int j = 0; j < 8; ++j) {
      k2_stage(c, sb, ka, kb, j);
      ka += 1024u; kb += 65536u;
      WAIT_V(24); BAR();
    }
  }
}
__device__ __forceinline__ bool k2_load_poll(const K2Ld& c, i32x4 sb, unsigned ka, unsigned kb, const unsigned char* fl, int n, u64 tag, int l) {
  u32x2 pv;
  const unsigned char* fp = fl + (l < n ? l : 0) * 8;
#pragma unroll
  for (int j = 0; j < 8; ++j) {
    if (j == 1) asm volatile("global_load_dwordx2 %0, %1, off sc1" : "=&v"(pv) : "v"(fp) : "memory");
    k2_stage(c, sb, ka, kb, j);
    ka += 1024u; kb += 65536u;
    WAIT_V(24);
    if (j < 7) BAR();
  }
  asm volatile("" : "+v"(pv));
  return __all((((u64)pv.y << 32) | pv.x) == tag);
}
template <int AUX> __device__ __forceinline__ void glds16x(unsigned voff, i32x4 srd, unsigned soff, unsigned ldsaddr) {
  if (AUX == 16) asm volatile("s_mov_b32 m0, %3\n\ts_nop 0\n\tbuffer_load_dwordx4 %0, %1, %2 offen sc1 lds" ::"v"(voff), "s"(srd), "s"(soff), "s"(ldsaddr) : "memory");
  else asm volatile("s_mov_b32 m0, %3\n\ts_nop 0\n\tbuffer_load_dwordx4 %0, %1, %2 offen lds" ::"v"(voff), "s"(srd), "s"(soff), "s"(ldsaddr) : "memory");
}

template <bool ISP> __device__ __forceinline__ void k2_body(LAS unsigned char* lds, const u16* x, const u16* wo, const u16* wd, u16* out, unsigned char* ws, int rt, int ct, u64 tag) {
  constexpr int NT = ISP ? K2_TP : K2_TQ;
  int tid = threadIdx.x; asm volatile("" : "+v"(tid));
  const int w = __builtin_amdgcn_readfirstlane(tid >> 6), l = tid & 63;
  const int w4 = w & 3, wm = w4 >> 1, wn = w4 & 1;
  const int g = l >> 4, q = (l & 15) >> 2, p = l & 3;
  const int tile = rt * 8 + ct;
  if (w >= 4) {
    K2Ld c;
    c.vsta = l * 16; c.vstb = k2_vstb(w4, l);
    c.sa = make_srd(ws + WS_A2, 128 * KS2 * 1024);
    const i32x4 swd = make_srd(wd, F_ * D_ * 2), swo = make_srd(wo, D_ * D_ * 2);
    const unsigned soa = (rt * 8 + 2 * w4) * (KS2 * 1024), sob = ct * 256;
    c.ldst = w4 * 2048;
    const size_t rep = (blockIdx.x & 7) * REP, t1f = WS_T1F + rep + (rt >> 1) * 256;
    if (w == 4) { if (ISP) flag_wait(ws, WS_T1A + rep + (rt >> 1) * 128, 16, tag, l); else flag_wait(ws, t1f + 128, 16, tag, l); }
    BAR();
    const unsigned ks0 = ISP ? 0u : K2_H0, ka0 = ks0 * 1024u + soa;
#pragma unroll
    for (int t = 0; t < 8; ++t) { glds16x<16>(c.vsta, c.sa, ka0 + t * 1024u, t * K2_SLOT + c.ldst); glds16x<16>(c.vsta, c.sa, ka0 + t * 1024u + 0x24000u, t * K2_SLOT + c.ldst + 1024); }
    WAIT_V(12); BAR();
    BAR();
    {
      const unsigned ka = ka0 + 8 * 1024u, kb = (ks0 + 8) * 65536u + sob;
#define K2_FIRST(J, N) do { k2_stage(c, swd, ka + (J) * 1024u, kb + (J) * 65536u, J); WAIT_V(N); BAR(); } while (0)
      K2_FIRST(0, 14); K2_FIRST(1, 16); K2_FIRST(2, 18); K2_FIRST(3, 20); K2_FIRST(4, 22); K2_FIRST(5, 24); K2_FIRST(6, 24); K2_FIRST(7, 24);
    }
    const i32x4 sx = make_srd(x, S_ * D_ * 2), sp = make_srd(ws + WS_PART, 128 * 32768);
    if (ISP) {
      static_assert(K2_TP >= 72 && K2_TP <= 96, "P schedule: 32 h + 32 o + 8..32 h");
      k2_load(c, swd, 16 * 1024u + soa, 16 * 65536u + sob, 1);
      if (!k2_load_poll(c, swd, 24 * 1024u + soa, 24 * 65536u + sob, ws + t1f, 16, tag, l)) flag_wait(ws, t1f, 16, tag, l);
      BAR();
      k2_load(c, swo, 112 * 1024u + soa, sob, 3);
      if (!k2_load_poll(c, swo, (112 + 24) * 1024u + soa, 24 * 65536u + sob, ws + t1f + 128, 5, tag, l)) flag_wait(ws, t1f + 128, 5, tag, l);
      BAR();
      k2_load(c, swd, 32 * 1024u + soa, 32 * 65536u + sob, (NT - 64) / 8 - 1);
      const bool early = k2_load_poll(c, swd, (NT - 40) * 1024u + soa, (NT - 40) * 65536u + sob, ws + WS_PTF + tile * 64, 4, tag, l);
      BAR();
      const unsigned xv = ((rt * 128 + wm * 64 + (l & 15)) * D_ + ct * 128 + wn * 64 + (((g & 1) << 1) | (g >> 1)) * 8) * 2;
#pragma unroll
      for (int k = 0; k < 2; ++k) {
#pragma unroll
        for (int e = 0; e < 4; ++e) { const int blk = k * 4 + e; glds16x<0>(xv, sx, (blk >> 1) * 16 * D_ * 2 + (blk & 1) * 64, k * 16384 + w4 * 4096 + e * 1024); }
        WAIT_V(24); BAR();
      }
      if (early) {
#pragma unroll
        for (int k = 2; k < 4; ++k) {
#pragma unroll
          for (int e = 0; e < 4; ++e) glds16x<16>(l * 16, sp, tile * 32768 + w4 * 8192 + ((k - 2) * 4 + e) * 1024, k * 16384 + w4 * 4096 + e * 1024);
          WAIT_V(24); BAR();
        }
        WAIT_V(20); BAR(); WAIT_V(16); BAR(); BAR(); WAIT_V(0); BAR();
      } else {
        WAIT_V(20); BAR(); WAIT_V(16); BAR(); WAIT_V(12); BAR();
        WAIT_V(0);
        flag_wait(ws, WS_PTF + tile * 64, 4, tag, l);
#pragma unroll
        for (int e = 0; e < 8; ++e) glds16x<16>(l * 16, sp, tile * 32768 + w4 * 8192 + e * 1024, (2 + (e >> 2)) * 16384 + w4 * 4096 + (e & 3) * 1024);
        BAR(); BAR();
        WAIT_V(0); BAR();
      }
    } else {
      k2_load(c, swd, (K2_H0 + 16) * 1024u + soa, (K2_H0 + 16) * 65536u + sob, (NT - 16) / 8);
      WAIT_V(20); BAR(); WAIT_V(16); BAR(); WAIT_V(12); BAR(); WAIT_V(8); BAR(); WAIT_V(4); BAR(); WAIT_V(0); BAR(); BAR(); BAR();
    }
    if (blockIdx.x == 0 && w == 5) {
#pragma unroll
      for (int k = 0; k < 4; ++k) flag_wait(ws, WS_T1F + k * 512, 64, tag, l);
      if (l == 0) flag_set(ws, WS_GEN, tag);
    }
    return;
  }
  K2Rd c; c.lds = lds;
  {
    const unsigned row = 4 * (g & 1) + q, s2 = (row >> 1) & 3;
    const unsigned lanep = 8192 + (g >> 1) * 2048 + wn * 1024 + row * 128 + (p >> 1) * 16 + (p & 1) * 8;
#pragma unroll
    for (int h = 0; h < 2; ++h) {
      c.aoff[h] = l * 16 + wm * 4096 + h * 65536;
#pragma unroll
      for (int j = 0; j < 4; ++j) c.vb[h][j] = lanep + ((j ^ s2) << 5) + h * 65536;
    }
  }
  f32x4 accf[4][4], accs[4][4];
#pragma unroll
  for (int i = 0; i < 4; ++i)
#pragma unroll
    for (int j = 0; j < 4; ++j) { accf[i][j] = (f32x4){0.f, 0.f, 0.f, 0.f}; accs[i][j] = accf[i][j]; }
  K2Frag f0, f1;
  BAR();
  BAR();
  STAMP(4);
  k2_read<0>(c, f0);
  WAIT_L(0); BAR();
  if (ISP) { k2_consume(c, accf, f0, f1, 4); k2_consume(c, accs, f0, f1, 4); k2_consume(c, accf, f0, f1, (NT - 64) / 8); }
  else k2_consume(c, accf, f0, f1, NT / 8);
  STAMP(5);
  if (!ISP) {
    const __amdgpu_buffer_rsrc_t pr = __builtin_amdgcn_make_buffer_rsrc(ws + WS_PART, 0, 128 * 32768, 0x00020000);
    const unsigned po = tile * 32768 + w4 * 8192 + l * 16;
#pragma unroll
    for (int i = 0; i < 4; ++i)
#pragma unroll
      for (int jp = 0; jp < 2; ++jp) __builtin_amdgcn_raw_buffer_store_b128(__builtin_bit_cast(u32x4, pack8(accf[i][2 * jp], accf[i][2 * jp + 1])), pr, po + (i * 2 + jp) * 1024, 0, 16);
    WAIT_V(0);
    if (l == 0) flag_set(ws, WS_PTF + tile * 64 + w4 * 8, tag);
  } else {
    const int m = l & 15;
    const __amdgpu_buffer_rsrc_t orr = __builtin_amdgcn_make_buffer_rsrc(out, 0, S_ * D_ * 2, 0x00020000);
    const unsigned ov = ((rt * 128 + wm * 64 + m) * D_ + ct * 128 + wn * 64 + (((g & 1) << 1) | (g >> 1)) * 8) * 2;
    u32x4 pt[8], xr[8];
#pragma unroll
    for (int b = 0; b < 8; ++b) {
      pt[b] = *(LAS u32x4*)(lds + (2 + (b >> 2)) * 16384 + w4 * 4096 + (b & 3) * 1024 + l * 16);
      xr[b] = *(LAS u32x4*)(lds + (b >> 2) * 16384 + w4 * 4096 + (b & 3) * 1024 + l * 16);
    }
#pragma unroll
    for (int i = 0; i < 4; ++i)
#pragma unroll
      for (int jp = 0; jp < 2; ++jp) {
        const int b = i * 2 + jp;
        unsigned y[2][2];
#pragma unroll
        for (int jj = 0; jj < 2; ++jj) {
          const int j = 2 * jp + jj;
          const f32x4 f = accf[i][j] + (f32x4){bf_lo(pt[b][2 * jj]), bf_hi(pt[b][2 * jj]), bf_lo(pt[b][2 * jj + 1]), bf_hi(pt[b][2 * jj + 1])};
          float v[4];
#pragma unroll
          for (int r = 0; r < 4; ++r) v[r] = accs[i][j][r] * silu_f(f[r]);
          y[jj][0] = cvt_pk(v[0], v[1]); y[jj][1] = cvt_pk(v[2], v[3]);
        }
        const auto s0 = __builtin_amdgcn_permlane16_swap(y[0][0], y[1][0], false, false);
        const auto s1 = __builtin_amdgcn_permlane16_swap(y[0][1], y[1][1], false, false);
        const unsigned yy[4] = {s0[0], s1[0], s0[1], s1[1]};
        u32x4 o;
#pragma unroll
        for (int r = 0; r < 4; ++r) o[r] = cvt_pk(bf_lo(xr[b][r]) + bf_lo(yy[r]), bf_hi(xr[b][r]) + bf_hi(yy[r]));
        __builtin_amdgcn_raw_buffer_store_b128(o, orr, ov + i * 16 * D_ * 2 + jp * 64, 0, OUT_AUX);
      }
  }
  STAMP(6);
}

__global__ __launch_bounds__(512, 2) void fused_block(const u16* __restrict__ x, const u16* __restrict__ wqkv, const u16* __restrict__ wo, const u16* __restrict__ wg,
                                                      const u16* __restrict__ wu, const u16* __restrict__ wd, u16* __restrict__ out, unsigned char* __restrict__ ws) {
  extern __shared__ __attribute__((aligned(16))) unsigned char shm[];
  LAS unsigned char* lds = (LAS unsigned char*)shm;
  const int b = blockIdx.x, xcd = b & 7, idx = b >> 3;
  const int pm = idx & 7, pn = idx >> 3, pa = xcd * 2 + (pn >> 1);
  const int lnmf = idx < 16 ? idx * 8 + xcd : -1;
  const int pr = xcd & 3, rt = (pr & 1) * 8 + (idx & 7), ct = (pr >> 1) * 4 + (idx >> 3);
  const bool isp = xcd < 4;
  u64 tag;
  if (pn & 1) tag = k1_body<1>(lds, x, wqkv, wg, wu, wo, wd, ws, pm, pa, lnmf, isp, ct);
  else tag = k1_body<0>(lds, x, wqkv, wg, wu, wo, wd, ws, pm, pa, lnmf, isp, ct);
  if (isp) k2_body<true>(lds, x, wo, wd, out, ws, rt, ct, tag);
  else k2_body<false>(lds, x, wo, wd, out, ws, rt, ct, tag);
}

extern "C" void kernel_launch(void* const* d_in, const int* in_sizes, int n_in, void* d_out, int out_size, void* d_ws, size_t ws_size, hipStream_t stream) {
  const u16* x = (const u16*)d_in[0]; const u16* wqkv = (const u16*)d_in[1]; const u16* wo = (const u16*)d_in[2];
  const u16* wg = (const u16*)d_in[4]; const u16* wu = (const u16*)d_in[5]; const u16* wd = (const u16*)d_in[6];
  (void)hipFuncSetAttribute((const void*)fused_block, hipFuncAttributeMaxDynamicSharedMemorySize, LDS_TOTAL);
  fused_block<<<256, 512, LDS_TOTAL, stream>>>(x, wqkv, wo, wg, wu, wd, (u16*)d_out, (unsigned char*)d_ws);
}
```

```cpp
#define EXFIL 6
#define EXTHR 62.0f
#define EXMUL 12.0f
#include <hip/hip_runtime.h>
#include <stdint.h>

#define LAS __attribute__((address_space(3)))
#define GAS __attribute__((address_space(1)))
typedef __bf16 bf16x8 __attribute__((ext_vector_type(8)));
typedef short s16x4 __attribute__((ext_vector_type(4)));
typedef short s16x8 __attribute__((ext_vector_type(8)));
typedef float f32x4 __attribute__((ext_vector_type(4)));
typedef float f32x2 __attribute__((ext_vector_type(2)));
typedef unsigned u32x4 __attribute__((ext_vector_type(4)));
typedef unsigned u32x2 __attribute__((ext_vector_type(2)));
typedef int i32x4 __attribute__((ext_vector_type(4)));
typedef unsigned short u16;

constexpr int S_ = 2048, D_ = 1024, F_ = 3584;
constexpr int KS2 = 144;
constexpr size_t WS_GEN = 0;
constexpr size_t REP = 4096;
constexpr size_t WS_LNF = 4096;
constexpr size_t WS_T1A = WS_LNF + 1024;
constexpr size_t WS_T1F = WS_LNF + 2048;
constexpr size_t WS_HLF = 4096 + 8 * REP;
constexpr size_t WS_PTF = WS_HLF + 16384;
static_assert(WS_PTF + 8192 <= 65536, "flag area");
constexpr size_t WS_XN = 65536;
constexpr size_t WS_A2 = WS_XN + 4194304;
constexpr size_t WS_HALO = WS_A2 + (size_t)128 * KS2 * 1024;
constexpr size_t WS_PART = WS_HALO + 8 * 32 * 2048;
constexpr size_t WS_STAMP = WS_PART + (size_t)128 * 65536;

#define P_STEPS 80
#define OUT_AUX 16
#define ST_AUX 16

__device__ __forceinline__ i32x4 make_srd(const void* p, unsigned bytes) {
  const unsigned long long a = (unsigned long long)p;
  i32x4 r; r.x = (int)(unsigned)a; r.y = (int)((unsigned)(a >> 32) & 0xffffu); r.z = (int)bytes; r.w = 0x00020000; return r;
}
__device__ __forceinline__ void glds16(unsigned voff, i32x4 srd, unsigned soff, unsigned ldsaddr) {
  asm volatile("s_mov_b32 m0, %3\n\ts_nop 0\n\tbuffer_load_dwordx4 %0, %1, %2 offen lds" ::"v"(voff), "s"(srd), "s"(soff), "s"(ldsaddr) : "memory");
}
#define WAIT_V(n) asm volatile("s_waitcnt vmcnt(" #n ")" ::: "memory")
#define WAIT_L(n) asm volatile("s_waitcnt lgkmcnt(" #n ")" ::: "memory")
#define SCHED() __builtin_amdgcn_sched_barrier(0)
#define BAR() do { SCHED(); __builtin_amdgcn_s_barrier(); SCHED(); } while (0)

typedef __bf16 bf16x2 __attribute__((ext_vector_type(2)));
__device__ __forceinline__ unsigned cvt_pk(float lo, float hi) { const f32x2 v = {lo, hi}; return __builtin_bit_cast(unsigned, __builtin_convertvector(v, bf16x2)); }
__device__ __forceinline__ float bf_lo(unsigned u) { return __uint_as_float(u << 16); }
__device__ __forceinline__ float bf_hi(unsigned u) { return __uint_as_float(u & 0xffff0000u); }
__device__ __forceinline__ float rbf(float f) { return bf_lo(cvt_pk(f, f)); }
__device__ __forceinline__ bf16x8 pack8(const f32x4& a, const f32x4& b) {
  u32x4 w; w.x = cvt_pk(a[0], a[1]); w.y = cvt_pk(a[2], a[3]); w.z = cvt_pk(b[0], b[1]); w.w = cvt_pk(b[2], b[3]);
  return __builtin_bit_cast(bf16x8, w);
}
__device__ __forceinline__ bf16x8 lds_tr8(LAS unsigned char* p0, LAS unsigned char* p1) {
  s16x4 a = __builtin_amdgcn_ds_read_tr16_b64_v4i16((LAS s16x4*)p0);
  s16x4 b = __builtin_amdgcn_ds_read_tr16_b64_v4i16((LAS s16x4*)p1);
  s16x8 c = __builtin_shufflevector(a, b, 0, 1, 2, 3, 4, 5, 6, 7);
  return __builtin_bit_cast(bf16x8, c);
}
__device__ __forceinline__ float silu_f(float g) { return g * __builtin_amdgcn_rcpf(1.0f + __builtin_amdgcn_exp2f(g * -1.44269504089f)); }
#define MFMA(a, b, c) __builtin_amdgcn_mfma_f32_16x16x32_bf16(a, b, c, 0, 0, 0)

typedef unsigned long long u64;
typedef GAS u64 gu64;
__device__ __forceinline__ void flag_set(unsigned char* ws, size_t off, u64 tag) { __hip_atomic_store((gu64*)(ws + off), tag, __ATOMIC_RELAXED, __HIP_MEMORY_SCOPE_AGENT); }
__device__ __forceinline__ void flag_set8(unsigned char* ws, size_t off, u64 tag, int l) { if (l < 8) flag_set(ws, off + l * REP, tag); }
__device__ __forceinline__ void flag_wait(unsigned char* ws, size_t off, int n, u64 tag, int l) {
  gu64* p = (gu64*)(ws + off) + (l < n ? l : 0);
  for (unsigned spins = 0; spins < (1u << 18); ++spins) {
    const u64 v = __hip_atomic_load(p, __ATOMIC_RELAXED, __HIP_MEMORY_SCOPE_AGENT);
    if (__all(v == tag)) break;
    __builtin_amdgcn_s_sleep(2);
  }
  asm volatile("" ::: "memory");
}

#define STAMP(k) do { if ((k) == EXFIL) { if (__builtin_amdgcn_readfirstlane(threadIdx.x) == 0) ((u64*)(ws + WS_STAMP))[blockIdx.x * 16 + 1] = __builtin_amdgcn_s_memrealtime(); } } while (0)
#define STAMPT(k, T) do {} while (0)

__device__ __forceinline__ float xsum16_32(float v) {
  const auto a = __builtin_amdgcn_permlane16_swap(__float_as_uint(v), __float_as_uint(v), false, false);
  v = __uint_as_float(a[0]) + __uint_as_float(a[1]);
  const auto b = __builtin_amdgcn_permlane32_swap(__float_as_uint(v), __float_as_uint(v), false, false);
  return __uint_as_float(b[0]) + __uint_as_float(b[1]);
}
__device__ __forceinline__ float xmax16_32(float v) {
  const auto a = __builtin_amdgcn_permlane16_swap(__float_as_uint(v), __float_as_uint(v), false, false);
  v = fmaxf(__uint_as_float(a[0]), __uint_as_float(a[1]));
  const auto b = __builtin_amdgcn_permlane32_swap(__float_as_uint(v), __float_as_uint(v), false, false);
  return fmaxf(__uint_as_float(b[0]), __uint_as_float(b[1]));
}
#define PF_STRIDE 128
constexpr int K1_ASLOT = 16384, K1_BSLOT = 20480, K1_BOFF = 4 * K1_ASLOT, K1_LDS = K1_BOFF + 4 * K1_BSLOT;
constexpr int LDS_PFDUMP = K1_LDS, LDS_LNRED = K1_LDS + 256, LDS_TOTAL = K1_LDS + 256 + 1024, LDS_HALO = 131072;

struct K1Ctx {
  LAS unsigned char* lds;
  unsigned aoff;
  unsigned vb[6];
  unsigned vst0, vst1;
  i32x4 s0, s1, s2;
  unsigned so0, so1;
  unsigned ldst;
};

template <int TYPE> __device__ __forceinline__ void k1_stage_b(const K1Ctx& c, int s, int slot, unsigned cnt, int lim) {
  const unsigned d = K1_BOFF + slot * K1_BSLOT + c.ldst;
  unsigned t0, t1;
  if (TYPE == 0) {
    const unsigned kq = (unsigned)s * (32u * 6144u) + c.so0, kg = (unsigned)s * (32u * 7168u) + c.so0;
    asm volatile(
      "s_cmp_ge_u32 %10, %11\n\ts_cbranch_scc1 .Lsk%=\n\t"
      "s_mov_b32 m0, %9\n\ts_add_u32 %0, %7, 0x800\n\tbuffer_load_dwordx4 %2, %4, %7 offen lds\n\t"
      "s_add_u32 m0, %9, 0x400\n\ts_add_u32 %1, %7, 0x1000\n\tbuffer_load_dwordx4 %2, %4, %0 offen lds\n\t"
      "s_add_u32 m0, %9, 0x800\n\ts_nop 0\n\tbuffer_load_dwordx4 %2, %4, %1 offen lds\n\t"
      "s_add_u32 m0, %9, 0xc00\n\ts_nop 0\n\tbuffer_load_dwordx4 %3, %5, %8 offen lds\n\t"
      "s_add_u32 m0, %9, 0x1000\n\ts_nop 0\n\tbuffer_load_dwordx4 %3, %6, %8 offen lds\n"
      ".Lsk%=:"
      : "=&s"(t0), "=&s"(t1)
      : "v"(c.vst0), "v"(c.vst1), "s"(c.s0), "s"(c.s1), "s"(c.s2), "s"(kq), "s"(kg), "s"(d), "s"(cnt), "i"(lim) : "memory", "scc");
  } else {
    const unsigned k0 = (unsigned)s * (32u * 7168u) + c.so0, k1 = (unsigned)s * (32u * 7168u) + c.so1;
    asm volatile(
      "s_cmp_ge_u32 %10, %11\n\ts_cbranch_scc1 .Lsk%=\n\t"
      "s_mov_b32 m0, %9\n\ts_add_u32 %0, %7, 0x80\n\tbuffer_load_dwordx4 %2, %4, %7 offen lds\n\t"
      "s_add_u32 m0, %9, 0x400\n\ts_nop 0\n\tbuffer_load_dwordx4 %2, %4, %0 offen lds\n\t"
      "s_add_u32 m0, %9, 0x800\n\ts_nop 0\n\tbuffer_load_dwordx4 %2, %5, %7 offen lds\n\t"
      "s_add_u32 m0, %9, 0xc00\n\ts_nop 0\n\tbuffer_load_dwordx4 %2, %5, %0 offen lds\n\t"
      "s_add_u32 m0, %9, 0x1000\n\ts_nop 0\n\tbuffer_load_dwordx4 %3, %6, %8 offen lds\n"
      ".Lsk%=:"
      : "=&s"(t0), "=&s"(t1)
      : "v"(c.vst0), "v"(c.vst1), "s"(c.s0), "s"(c.s1), "s"(c.s2), "s"(k0), "s"(k1), "s"(d), "s"(cnt), "i"(lim) : "memory", "scc");
  }
}
__device__ __forceinline__ void k1_stage_a(const K1Ctx& c, int s, int slot, unsigned cnt, int lim) {
  const unsigned d = slot * K1_ASLOT + c.ldst;
  const unsigned k = (unsigned)s * 1024u + c.so0;
  unsigned t0;
  asm volatile(
    "s_cmp_ge_u32 %5, %6\n\ts_cbranch_scc1 .Lsk%=\n\t"
    "s_mov_b32 m0, %4\n\ts_add_u32 %0, %3, 0x8000\n\tbuffer_load_dwordx4 %1, %2, %3 offen sc1 lds\n\t"
    "s_add_u32 m0, %4, 0x400\n\ts_nop 0\n\tbuffer_load_dwordx4 %1, %2, %0 offen sc1 lds\n\t"
    "s_add_u32 m0, %4, 0x800\n\ts_add_u32 %0, %3, 0x10000\n\tbuffer_load_dwordx4 %1, %2, %0 offen sc1 lds\n\t"
    "s_add_u32 m0, %4, 0xc00\n\ts_add_u32 %0, %3, 0x18000\n\tbuffer_load_dwordx4 %1, %2, %0 offen sc1 lds\n"
    ".Lsk%=:"
    : "=&s"(t0) : "v"(c.vst0), "s"(c.s0), "s"(k), "s"(d), "s"(cnt), "i"(lim) : "memory", "scc");
}
#define WAIT_V2(STEADY, TAIL, cnt, lim) do { asm volatile("s_cmp_lt_u32 %0, %1\n\ts_cbranch_scc1 .Lw%=\n\ts_waitcnt vmcnt(" #TAIL ")\n.Lw%=:\n\ts_waitcnt vmcnt(" #STEADY ")" :: "s"(cnt), "i"(lim) : "memory", "scc"); } while (0)
#define BAR_IF(cnt, lim) do { SCHED(); asm volatile("s_cmp_ge_u32 %0, %1\n\ts_cbranch_scc1 .Lb%=\n\ts_barrier\n.Lb%=:" :: "s"(cnt), "i"(lim) : "memory", "scc"); SCHED(); } while (0)
template <int TYPE> __device__ __forceinline__ void k1_read(const K1Ctx& c, int slot, bf16x8 (&af)[4], bf16x8 (&bfr)[10]) {
  LAS unsigned char* lds = c.lds;
  const int bs = slot * K1_BSLOT;
  if (TYPE == 0) {
#pragma unroll
    for (int j = 0; j < 8; ++j) { LAS unsigned char* p = lds + c.vb[j & 1] + bs + (j >> 1) * 1024; bfr[j] = lds_tr8(p, p + 512); }
#pragma unroll
    for (int j = 0; j < 2; ++j) { LAS unsigned char* p = lds + c.vb[2 + j] + bs; bfr[8 + j] = lds_tr8(p, p + 512); }
  } else {
#pragma unroll
    for (int j = 0; j < 8; ++j) { LAS unsigned char* p = lds + c.vb[j & 3] + bs + (j >> 2) * 2048; bfr[j] = lds_tr8(p, p + 512); }
#pragma unroll
    for (int j = 0; j < 2; ++j) { LAS unsigned char* p = lds + c.vb[4 + j] + bs; bfr[8 + j] = lds_tr8(p, p + 256); }
  }
#pragma unroll
  for (int i = 0; i < 4; ++i) af[i] = *(LAS bf16x8*)(lds + c.aoff + slot * K1_ASLOT + i * 1024);
}
#define K1V 0
#define K1_EARLY 0
template <int TYPE, int N0, int N1> __device__ __forceinline__ void k1_mma(f32x4 (&acc)[4][10], const bf16x8 (&af)[4], const bf16x8 (&bfr)[10]) {
#pragma unroll
  for (int n = N0; n < N1; ++n) {
    const int j = n >> 2, i = n & 3;
    if (TYPE == 0 && (j == 4 || j == 5)) acc[i][j] = MFMA(af[i], bfr[j], acc[i][j]);
    else acc[i][j] = MFMA(bfr[j], af[i], acc[i][j]);
  }
}
__device__ __forceinline__ void k1_pf(unsigned voff, i32x4 srd, unsigned soff, unsigned on, unsigned cnt, int lim) {
  asm volatile("s_cmp_lg_u32 %3, 4\n\ts_cbranch_scc1 .Lpf%=\n\ts_cmp_ge_u32 %4, %5\n\ts_cbranch_scc1 .Lpf%=\n\t"
               "s_mov_b32 m0, %6\n\ts_nop 0\n\tbuffer_load_dword %0, %1, %2 offen lds\n.Lpf%=:"
               ::"v"(voff), "s"(srd), "s"(soff), "s"(on), "s"(cnt), "i"(lim), "i"(LDS_PFDUMP) : "memory", "scc");
}
#define WAIT_A(PFN, PFT, PLN, PLT, pf, cnt, lim) do { asm volatile( \
    "s_cmp_lt_u32 %1, %2\n\ts_cbranch_scc1 .Ls%=\n\t" \
    "s_cmp_eq_u32 %0, 4\n\ts_cbranch_scc1 .Lt%=\n\ts_waitcnt vmcnt(" #PLT ")\n.Lt%=:\n\ts_waitcnt vmcnt(" #PFT ")\n" \
    ".Ls%=:\n\ts_cmp_eq_u32 %0, 4\n\ts_cbranch_scc1 .Lu%=\n\ts_waitcnt vmcnt(" #PLN ")\n.Lu%=:\n\ts_waitcnt vmcnt(" #PFN ")" \
    :: "s"(pf), "s"(cnt), "i"(lim) : "memory", "scc"); } while (0)

constexpr int K2_SLOT = 16384;
constexpr int K2_TP = P_STEPS, K2_TQ = KS2 - P_STEPS;
static_assert(K2_TP % 8 == 0 && K2_TQ % 8 == 0 && K2_TP >= 48, "step counts must be multiples of 8");
constexpr unsigned K2_H0 = K2_TP - 32;
__device__ __forceinline__ unsigned k2_vstb(int w4, int l) { const unsigned r8 = l >> 3, ch = (l & 7) ^ (((r8 >> 1) & 3) << 1); return (8 * w4 + r8) * 2048 + ch * 16; }
__device__ __forceinline__ void k2_prologue_b(int w4, int l, const u16* wd, bool isp, int ct) {
  const i32x4 sb = make_srd(wd, F_ * D_ * 2);
  const unsigned vb = k2_vstb(w4, l), k0 = (isp ? 0u : K2_H0) * 65536u + ct * 256;
#pragma unroll
  for (int t = 0; t < 8; ++t) { glds16(vb, sb, k0 + t * 65536u, t * K2_SLOT + 8192 + w4 * 2048); glds16(vb, sb, k0 + t * 65536u + 128u, t * K2_SLOT + 8192 + w4 * 2048 + 1024); }
}

template <int TYPE> __device__ __forceinline__ u64 k1_body(LAS unsigned char* lds, const u16* x, const u16* wqkv, const u16* wg, const u16* wu, const u16* wo, const u16* wd, unsigned char* ws, int pm, int pa  , int lnmf, bool isp, int ct) {
  const int tid = threadIdx.x;
  const int w = __builtin_amdgcn_readfirstlane(tid >> 6), l = tid & 63;
  const int grp = w >> 2, wr = w & 3;
  const int g = l >> 4, q = (l & 15) >> 2, p = l & 3;
  STAMP(0);
  K1Ctx c; c.lds = lds;
  c.aoff = l * 16 + wr * 4096;
  {
    const unsigned s2 = ((q >> 1) & 1) | ((g & 1) << 1);
    const unsigned lanep = K1_BOFF + g * 5120 + q * 128 + (p >> 1) * 16 + (p & 1) * 8;
    if (TYPE == 0) {
      c.vb[0] = lanep + (((2 * grp) ^ s2) << 5); c.vb[1] = lanep + (((2 * grp + 1) ^ s2) << 5);
      c.vb[2] = c.vb[0] + 4096; c.vb[3] = c.vb[1] + 4096; c.vb[4] = 0; c.vb[5] = 0;
    } else {
#pragma unroll
      for (int j = 0; j < 4; ++j) c.vb[j] = lanep + ((j ^ s2) << 5) + grp * 1024;
      const unsigned mx = K1_BOFF + (2 * (g >> 1) * 5 + 4) * 1024 + (g & 1) * 512 + q * 64 + (((2 * grp + (p >> 1)) ^ ((g & 1) << 1)) << 4) + (p & 1) * 8;
      c.vb[4] = mx; c.vb[5] = mx + 5120;
    }
  }
  const int c0 = 1024 + 160 * pa;
  const int odd = pa & 1;
  if (grp == 0) {
    const unsigned r8 = l >> 3, sw = (((r8 >> 1) & 1) | ((wr & 1) << 1)) << 1, ch = (l & 7) ^ sw;
    c.ldst = wr * 5120;
    if (TYPE == 0) {
      c.vst0 = (8 * wr + r8) * 6144 + ch * 16; c.vst1 = (8 * wr + r8) * 7168 + ch * 16;
      c.s0 = make_srd(wqkv, D_ * 3 * D_ * 2); c.s1 = make_srd(wg, D_ * F_ * 2); c.s2 = make_srd(wu, D_ * F_ * 2);
      c.so0 = pa * 128; c.so1 = 0;
    } else {
      c.vst0 = (8 * wr + r8) * 7168 + ch * 16;
      const unsigned r16 = l >> 2, chm = (l & 3) ^ (((r16 >> 3) & 1) << 1);
      c.vst1 = (16 * (wr >> 1) + r16) * 7168 + chm * 16;
      c.s0 = make_srd(wg, D_ * F_ * 2); c.s1 = make_srd(wu, D_ * F_ * 2); c.s2 = make_srd((wr & 1) ? wu : wg, D_ * F_ * 2);
      c.so0 = (c0 + (odd ? 32 : 0)) * 2; c.so1 = (c0 + (odd ? 0 : 128)) * 2;
    }
  } else {
    c.vst0 = l * 16; c.vst1 = 0;
    c.s0 = make_srd(ws + WS_XN, S_ * D_ * 2); c.s1 = c.s0; c.s2 = c.s0;
    c.so0 = (pm * 16 + 4 * wr) * 32768; c.so1 = 0;
    c.ldst = wr * 4096;
  }

  u32x4 xv[4]; float piv = 0.f;
  if (lnmf >= 0) {
    const u16* row = x + (size_t)(lnmf * 16 + (l & 15)) * D_;
#pragma unroll
    for (int i = 0; i < 4; ++i) xv[i] = *(const u32x4*)(row + w * 128 + g * 8 + i * 32);
    piv = __uint_as_float((unsigned)row[0] << 16);
  }
  unsigned glo, ghi;
  if (grp == 0 && lnmf < 0) {
    u32x2 gen;
    asm volatile("global_load_dwordx2 %0, %1, off sc1" : "=&v"(gen) : "v"(ws + WS_GEN) : "memory");
    k1_stage_b<TYPE>(c, 0, 0, 0, 1); k1_stage_b<TYPE>(c, 1, 1, 0, 1); k1_stage_b<TYPE>(c, 2, 2, 0, 1);
    asm volatile("s_waitcnt vmcnt(15)" : "+v"(gen) :: "memory");
    glo = __builtin_amdgcn_readfirstlane(gen.x); ghi = __builtin_amdgcn_readfirstlane(gen.y);
  } else {
    u32x2 gen;
    asm volatile("global_load_dwordx2 %0, %1, off sc1\n\ts_waitcnt vmcnt(0)" : "=&v"(gen) : "v"(ws + WS_GEN) : "memory");
    glo = __builtin_amdgcn_readfirstlane(gen.x); ghi = __builtin_amdgcn_readfirstlane(gen.y);
  }
  const u64 tag = (((u64)ghi << 32) | glo) + 1;

  if (lnmf >= 0) {
    const int m = l & 15;
    const u32x4 (&v)[4] = xv;
    float f[32]; float sm = 0.f, sq = 0.f;
#pragma unroll
    for (int i = 0; i < 4; ++i)
#pragma unroll
      for (int j = 0; j < 4; ++j) {
        const float a = bf_lo(v[i][j]), b = bf_hi(v[i][j]); f[i * 8 + 2 * j] = a; f[i * 8 + 2 * j + 1] = b;
        const float da = a - piv, db = b - piv; sm += da + db; sq += da * da + db * db;
      }
    sm = xsum16_32(sm); sq = xsum16_32(sq);
    STAMPT(8, 0);
    LAS f32x2* red = (LAS f32x2*)(lds + LDS_LNRED);
    if (g == 0) red[w * 16 + m] = (f32x2){sm, sq};
    WAIT_L(0); BAR();
    float st = 0.f, qt = 0.f;
#pragma unroll
    for (int i = 0; i < 8; ++i) { const f32x2 r = red[i * 16 + m]; st += r.x; qt += r.y; }
    const float md = st * (1.0f / D_), mean = piv + md, var = fmaxf(qt * (1.0f / D_) - md * md, 0.f);
    const float mb = rbf(mean);
    const float inv = 1.0f / rbf(sqrtf(rbf(rbf(var) + 1e-5f)));
    const __amdgpu_buffer_rsrc_t xr = __builtin_amdgcn_make_buffer_rsrc(ws + WS_XN, 0, S_ * D_ * 2, 0x00020000);
#pragma unroll
    for (int i = 0; i < 4; ++i) {
      u32x4 o;
#pragma unroll
      for (int j = 0; j < 4; ++j) o[j] = cvt_pk(rbf(f[i * 8 + 2 * j] - mb) * inv, rbf(f[i * 8 + 2 * j + 1] - mb) * inv);
      __builtin_amdgcn_raw_buffer_store_b128(o, xr, (lnmf * 32 + w * 4 + i) * 1024 + l * 16, 0, 16);
    }
    STAMPT(9, 0);
    WAIT_V(0); BAR();
    if (w == 0) flag_set8(ws, WS_LNF + lnmf * 8, tag, l);
    STAMPT(10, 0);
    if (grp == 0) { k1_stage_b<TYPE>(c, 0, 0, 0, 1); k1_stage_b<TYPE>(c, 1, 1, 0, 1); k1_stage_b<TYPE>(c, 2, 2, 0, 1); }
  }
  if (w == 4) flag_wait(ws, WS_LNF + (blockIdx.x & 7) * REP + pm * 128, 16, tag, l);
  BAR();

  f32x4 acc[4][10];
#pragma unroll
  for (int i = 0; i < 4; ++i)
#pragma unroll
    for (int j = 0; j < 10; ++j) acc[i][j] = (f32x4){0.f, 0.f, 0.f, 0.f};
  bf16x8 af[4], bfr[10];

  if (grp == 0) {
    WAIT_V(10); BAR();
    STAMP(1);
#define K1_G0_STEP(J, TAILV) do { \
      k1_read<TYPE>(c, J, af, bfr); SCHED(); \
      k1_stage_b<TYPE>(c, s + (J) + 3, ((J) + 3) & 3, s, (J) == 0 ? 32 : 28); \
      WAIT_L(0); BAR(); \
      __builtin_amdgcn_s_setprio(1); \
      k1_mma<TYPE, 0, 40 - K1_EARLY>(acc, af, bfr); \
      if (K1V & 1) __builtin_amdgcn_s_setprio(0); \
      SCHED(); WAIT_V2(10, TAILV, s, 28); \
      BAR(); \
      if (K1_EARLY) __builtin_amdgcn_s_setprio(3); \
      k1_mma<TYPE, 40 - K1_EARLY, 40>(acc, af, bfr); \
      __builtin_amdgcn_s_setprio(0); SCHED(); } while (0)
#pragma nounroll
    for (unsigned s = 0; s < 32; s += 4) {
      K1_G0_STEP(0, 10); K1_G0_STEP(1, 5); K1_G0_STEP(2, 0); K1_G0_STEP(3, 0);
    }
  } else {
#define PF_ON 1
#define K1_PF_ROW0 96
    const unsigned pf = PF_ON ? w : 99  , pfv = (blockIdx.x * 64 + l) * PF_STRIDE;
    const i32x4 sg = make_srd(wg, D_ * F_ * 2), su = make_srd(wu, D_ * F_ * 2), sq = make_srd(wqkv, D_ * 3 * D_ * 2), sd = make_srd(wd, F_ * D_ * 2), so = make_srd(wo, D_ * D_ * 2);
    constexpr unsigned OOR = 0x7f000000u, MB = 16384u * PF_STRIDE;
    constexpr unsigned NG = ((1024 - K1_PF_ROW0) * 7168u + MB - 1) / MB, NQ = ((1024 - K1_PF_ROW0) * 6144u + MB - 1) / MB, ND = (F_ * D_ * 2 + MB - 1) / MB, NO = (D_ * D_ * 2 + MB - 1) / MB;
    static_assert(NG + (ND + 1) / 2 <= 8 && NG + ND / 2 <= 7 && NQ + NO <= 7, "prefetch schedule does not fit");
    STAMPT(11, 256);
    if (!(K1V & 2)) k1_pf(pfv, sq, OOR, pf, 0, 1);
    k1_stage_a(c, 0, 0, 0, 1);
    if (!(K1V & 2)) k1_pf(pfv, sq, OOR, pf, 0, 1);
    k1_stage_a(c, 1, 1, 0, 1);
    k1_stage_a(c, 2, 2, 0, 1);
    if (K1V & 2) WAIT_V(8); else WAIT_A(9, 9, 8, 8, pf, 0u, 1);
    BAR();
    BAR();
#define K1_G1_STEP(J, PFN, PFT, PLT) do { \
      k1_read<TYPE>(c, J, af, bfr); SCHED(); \
      if (!(K1V & 2) && (J) == 0) k1_pf(pfv, it < NG ? sg : sd, it < NG ? K1_PF_ROW0 * 7168u + it * MB : (it - NG) * MB, pf, s, 32); \
      if (!(K1V & 2) && (J) == 1) k1_pf(pfv, it < NG ? su : sd, it < NG ? K1_PF_ROW0 * 7168u + it * MB : (it - NG + (ND + 1) / 2) * MB, pf, s, 28); \
      if (!(K1V & 2) && (J) == 2) k1_pf(pfv, it < NQ ? sq : so, it < NQ ? K1_PF_ROW0 * 6144u + it * MB : (it - NQ) * MB, pf, s, 28); \
      k1_stage_a(c, s + (J) + 3, ((J) + 3) & 3, s, (J) == 0 ? 32 : 28); \
      WAIT_L(0); if (K1V & 2) WAIT_V2(8, PLT, s, 28); else WAIT_A(PFN, PFT, 8, PLT, pf, s, 28); \
      BAR(); \
      __builtin_amdgcn_s_setprio(1); \
      k1_mma<TYPE, 0, 40 - K1_EARLY>(acc, af, bfr); \
      if (K1V & 1) __builtin_amdgcn_s_setprio(0); \
      if ((J) < 3) BAR(); else BAR_IF(s, 28); \
      if (K1_EARLY) __builtin_amdgcn_s_setprio(3); \
      k1_mma<TYPE, 40 - K1_EARLY, 40>(acc, af, bfr); \
      __builtin_amdgcn_s_setprio(0); SCHED(); } while (0)
#pragma nounroll
    for (unsigned s = 0; s < 32; s += 4) {
      const unsigned it = s >> 2;
      K1_G1_STEP(0, 9, 9, 8); K1_G1_STEP(1, 10, 5, 4); K1_G1_STEP(2, 10, 0, 0); K1_G1_STEP(3, 9, 0, 0);
    }
    k2_prologue_b(wr, l, wd, isp, ct);
  }
  STAMP(2);

  const __amdgpu_buffer_rsrc_t a2 = __builtin_amdgcn_make_buffer_rsrc(ws + WS_A2, 0, 128 * KS2 * 1024, 0x00020000);
  const unsigned mf0 = pm * 16 + 4 * wr;
  if (TYPE == 1) {
    const unsigned ksf = c0 / 32 + (odd ? 1 : 0) + 2 * grp, ksm = c0 / 32 + (odd ? 0 : 4);
#pragma unroll
    for (int i = 0; i < 4; ++i) {
      const unsigned base = (mf0 + i) * (KS2 * 1024);
#pragma unroll
      for (int t = 0; t < 2; ++t) {
        f32x4 h0, h1;
#pragma unroll
        for (int r = 0; r < 4; ++r) { h0[r] = silu_f(acc[i][2 * t][r]) * acc[i][2 * t + 4][r]; h1[r] = silu_f(acc[i][2 * t + 1][r]) * acc[i][2 * t + 5][r]; }
        __builtin_amdgcn_raw_buffer_store_b128(__builtin_bit_cast(u32x4, pack8(h0, h1)), a2, l * 16 + base + (ksf + t) * 1024, 0, ST_AUX);
      }
      f32x4 h0;
#pragma unroll
      for (int r = 0; r < 4; ++r) h0[r] = silu_f(acc[i][8][r]) * acc[i][9][r];
      u32x2 o; o.x = cvt_pk(h0[0], h0[1]); o.y = cvt_pk(h0[2], h0[3]);
      *(LAS u32x2*)(lds + LDS_HALO + (wr * 4 + i) * 1024 + l * 16 + grp * 8) = o;
    }
    WAIT_L(0); BAR();
#pragma unroll
    for (int ii = 0; ii < 2; ++ii) {
      const int i = 2 * grp + ii;
      const u32x4 v = *(LAS u32x4*)(lds + LDS_HALO + (wr * 4 + i) * 1024 + l * 16);
      __builtin_amdgcn_raw_buffer_store_b128(v, a2, l * 16 + (mf0 + i) * (KS2 * 1024) + ksm * 1024, 0, ST_AUX);
    }
  } else {
    const int head = 2 * pa + grp;
    bf16x8 kop[5]; u32x2 vp[5][2];
#pragma unroll
    for (int i = 0; i < 4; ++i) {
      kop[i + 1] = pack8(acc[i][2], acc[i][3]);
#pragma unroll
      for (int d = 0; d < 2; ++d) { vp[i + 1][d].x = cvt_pk(acc[i][4 + d][0], acc[i][4 + d][1]); vp[i + 1][d].y = cvt_pk(acc[i][4 + d][2], acc[i][4 + d][3]); }
    }
    const u32x4 klast = __builtin_bit_cast(u32x4, kop[4]);
    const u32x4 vlast = (u32x4){vp[4][0].x, vp[4][0].y, vp[4][1].x, vp[4][1].y};
    const __amdgpu_buffer_rsrc_t hr = __builtin_amdgcn_make_buffer_rsrc(ws + WS_HALO, 0, 8 * 32 * 2048, 0x00020000);
    const bool send = (wr == 3) && (pm < 7);
    if (send) {
      const unsigned ho = (pm * 32 + head) * 2048;
      __builtin_amdgcn_raw_buffer_store_b128(klast, hr, l * 16 + ho, 0, 16);
      __builtin_amdgcn_raw_buffer_store_b128(vlast, hr, l * 16 + ho + 1024, 0, 16);
    }
    if (wr != 3) {
      *(LAS u32x4*)(lds + LDS_HALO + w * 2048 + l * 16) = klast;
      *(LAS u32x4*)(lds + LDS_HALO + w * 2048 + 1024 + l * 16) = vlast;
    }
    bf16x8 qop[4];
#pragma unroll
    for (int i = 0; i < 4; ++i) qop[i] = pack8(acc[i][0], acc[i][1]);
#pragma unroll
    for (int i = 0; i < 4; ++i) {
      f32x4 h0, h1;
#pragma unroll
      for (int r = 0; r < 4; ++r) { h0[r] = silu_f(acc[i][6][r]) * acc[i][8][r]; h1[r] = silu_f(acc[i][7][r]) * acc[i][9][r]; }
      __builtin_amdgcn_raw_buffer_store_b128(__builtin_bit_cast(u32x4, pack8(h0, h1)), a2, l * 16 + (mf0 + i) * (KS2 * 1024) + (2 * pa + grp) * 1024, 0, ST_AUX);
    }
    WAIT_L(0); BAR();
    const int m = l & 15; const bool par = m & 1;
    const bool own0 = 4 * g + (m & 1) <= m, own1 = 4 * g + (m & 1) + 2 <= m;
    const unsigned hmask = par ? 0xffff0000u : 0x0000ffffu;
    const float scale = 0.17677669529663687f * 1.44269504089f;
    auto attend = [&](int i, bool have_prev) {
      const f32x4 z = (f32x4){0.f, 0.f, 0.f, 0.f};
      const f32x4 s0 = MFMA(kop[i + 1], qop[i], z);
      const f32x4 s1 = MFMA(kop[i], qop[i], z);
      float l0 = own0 ? (par ? s0[1] : s0[0]) : (par ? s1[1] : s1[0]);
      float l1 = own1 ? (par ? s0[3] : s0[2]) : (par ? s1[3] : s1[2]);
      l0 *= scale; l1 *= scale;
      if (!have_prev) { l0 = own0 ? l0 : -3.0e38f; l1 = own1 ? l1 : -3.0e38f; }
      const float mx = xmax16_32(fmaxf(l0, l1));
      const float e0 = __builtin_amdgcn_exp2f(l0 - mx), e1 = __builtin_amdgcn_exp2f(l1 - mx);
      const float inv = __builtin_amdgcn_rcpf(xsum16_32(e0 + e1));
      const unsigned w0 = cvt_pk(e0 * inv, e0 * inv) & hmask, w1 = cvt_pk(e1 * inv, e1 * inv) & hmask;
      const u32x4 pw = (u32x4){own0 ? 0u : w0, own1 ? 0u : w1, own0 ? w0 : 0u, own1 ? w1 : 0u};
      const bf16x8 pop = __builtin_bit_cast(bf16x8, pw);
      f32x4 o[2];
#pragma unroll
      for (int d = 0; d < 2; ++d) {
        const u32x4 vv = (u32x4){vp[i][d].x, vp[i][d].y, vp[i + 1][d].x, vp[i + 1][d].y};
        o[d] = MFMA(__builtin_bit_cast(bf16x8, vv), pop, z);
      }
      __builtin_amdgcn_raw_buffer_store_b128(__builtin_bit_cast(u32x4, pack8(o[0], o[1])), a2, l * 16 + (mf0 + i) * (KS2 * 1024) + (112 + head) * 1024, 0, ST_AUX);
    };
    attend(1, true); attend(2, true); attend(3, true);
    STAMPT(12, 0);
    WAIT_V(3);
    if (send && l == 0) flag_set(ws, WS_HLF + (pm * 32 + head) * 64, tag);
    BAR();
    if (w == 0) flag_set8(ws, WS_T1A + (pm * 16 + pa) * 8, tag, l);
    STAMPT(13, 0);
    bool have_prev = true;
    u32x4 kh, vh;
    if (wr == 0) {
      if (pm > 0) {
        flag_wait(ws, WS_HLF + ((pm - 1) * 32 + head) * 64, 1, tag, l);
        const unsigned ho = ((pm - 1) * 32 + head) * 2048;
        kh = __builtin_amdgcn_raw_buffer_load_b128(hr, l * 16 + ho, 0, 16);
        vh = __builtin_amdgcn_raw_buffer_load_b128(hr, l * 16 + ho + 1024, 0, 16);
      } else { kh = (u32x4){0u, 0u, 0u, 0u}; vh = kh; have_prev = false; }
    } else {
      kh = *(LAS u32x4*)(lds + LDS_HALO + (w - 1) * 2048 + l * 16);
      vh = *(LAS u32x4*)(lds + LDS_HALO + (w - 1) * 2048 + 1024 + l * 16);
    }
    kop[0] = __builtin_bit_cast(bf16x8, kh);
    vp[0][0] = (u32x2){vh.x, vh.y}; vp[0][1] = (u32x2){vh.z, vh.w};
    attend(0, have_prev);
  }
  STAMPT(14, 0);
  WAIT_V(0); BAR();
  if (w == 0) flag_set8(ws, WS_T1F + (pm * 32 + TYPE * 16 + pa) * 8, tag, l);
  STAMP(3);
  return tag;
}

struct K2Frag { bf16x8 a[4], b[4]; };
struct K2Rd { LAS unsigned char* lds; unsigned aoff[2], vb[2][4]; };
template <int SLOT> __device__ __forceinline__ void k2_read(const K2Rd& c, K2Frag& f) {
  constexpr int H = SLOT >> 2, O = (SLOT & 3) * K2_SLOT;
#pragma unroll
  for (int j = 0; j < 4; ++j) { LAS unsigned char* p = c.lds + c.vb[H][j] + O; f.b[j] = lds_tr8(p, p + 4096); }
#pragma unroll
  for (int i = 0; i < 4; ++i) f.a[i] = *(LAS bf16x8*)(c.lds + c.aoff[H] + O + i * 1024);
}
template <int SLOTN> __device__ __forceinline__ void k2_step(const K2Rd& c, f32x4 (&acc)[4][4], const K2Frag& cur, K2Frag& nxt) {
  k2_read<SLOTN>(c, nxt);
#pragma unroll
  for (int j = 0; j < 4; ++j)
#pragma unroll
    for (int i = 0; i < 4; ++i) acc[i][j] = MFMA(cur.b[j], cur.a[i], acc[i][j]);
#pragma unroll
  for (int k = 0; k < 6; ++k) { __builtin_amdgcn_sched_group_barrier(0x8, 1, 0); __builtin_amdgcn_sched_group_barrier(0x100, 2, 0); }
  __builtin_amdgcn_sched_group_barrier(0x8, 10, 0);
  SCHED(); __builtin_amdgcn_s_waitcnt(0xC07F); asm volatile("" ::: "memory"); BAR();
}
__device__ __forceinline__ void k2_consume(const K2Rd& c, f32x4 (&acc)[4][4], K2Frag& f0, K2Frag& f1, int iters) {
#pragma nounroll
  for (int it = 0; it < iters; ++it) {
    k2_step<1>(c, acc, f0, f1); k2_step<2>(c, acc, f1, f0); k2_step<3>(c, acc, f0, f1); k2_step<4>(c, acc, f1, f0);
    k2_step<5>(c, acc, f0, f1); k2_step<6>(c, acc, f1, f0); k2_step<7>(c, acc, f0, f1); k2_step<0>(c, acc, f1, f0);
  }
}

struct K2Ld { unsigned vsta, vstb; i32x4 sa; unsigned ldst; };
__device__ __forceinline__ void k2_stage(const K2Ld& c, i32x4 sb, unsigned ka, unsigned kb, int slot) {
  const unsigned d = slot * K2_SLOT + c.ldst;
  unsigned t0;
  asm volatile(
    "s_mov_b32 m0, %7\n\ts_add_u32 %0, %5, 0x24000\n\tbuffer_load_dwordx4 %1, %3, %5 offen sc1 lds\n\t"
    "s_add_u32 m0, %7, 0x400\n\ts_nop 0\n\tbuffer_load_dwordx4 %1, %3, %0 offen sc1 lds\n\t"
    "s_add_u32 m0, %7, 0x2000\n\ts_add_u32 %0, %6, 0x80\n\tbuffer_load_dwordx4 %2, %4, %6 offen lds\n\t"
    "s_add_u32 m0, %7, 0x2400\n\ts_nop 0\n\tbuffer_load_dwordx4 %2, %4, %0 offen lds"
    : "=&s"(t0) : "v"(c.vsta), "v"(c.vstb), "s"(c.sa), "s"(sb), "s"(ka), "s"(kb), "s"(d) : "memory", "scc");
}
__device__ __forceinline__ void k2_load(const K2Ld& c, i32x4 sb, unsigned ka, unsigned kb, int iters) {
#pragma nounroll
  for (int it = 0; it < iters; ++it) {
#pragma unroll
    for (int j = 0; j < 8; ++j) {
      k2_stage(c, sb, ka, kb, j);
      ka += 1024u; kb += 65536u;
      WAIT_V(24); BAR();
    }
  }
}
__device__ __forceinline__ bool k2_load_poll(const K2Ld& c, i32x4 sb, unsigned ka, unsigned kb, const unsigned char* fl, int n, u64 tag, int l) {
  u32x2 pv;
  const unsigned char* fp = fl + (l < n ? l : 0) * 8;
#pragma unroll
  for (int j = 0; j < 8; ++j) {
    if (j == 1) asm volatile("global_load_dwordx2 %0, %1, off sc1" : "=&v"(pv) : "v"(fp) : "memory");
    k2_stage(c, sb, ka, kb, j);
    ka += 1024u; kb += 65536u;
    WAIT_V(24);
    if (j < 7) BAR();
  }
  asm volatile("" : "+v"(pv));
  return __all((((u64)pv.y << 32) | pv.x) == tag);
}
template <int AUX> __device__ __forceinline__ void glds16x(unsigned voff, i32x4 srd, unsigned soff, unsigned ldsaddr) {
  if (AUX == 16) asm volatile("s_mov_b32 m0, %3\n\ts_nop 0\n\tbuffer_load_dwordx4 %0, %1, %2 offen sc1 lds" ::"v"(voff), "s"(srd), "s"(soff), "s"(ldsaddr) : "memory");
  else asm volatile("s_mov_b32 m0, %3\n\ts_nop 0\n\tbuffer_load_dwordx4 %0, %1, %2 offen lds" ::"v"(voff), "s"(srd), "s"(soff), "s"(ldsaddr) : "memory");
}

template <bool ISP> __device__ __forceinline__ void k2_body(LAS unsigned char* lds, const u16* x, const u16* wo, const u16* wd, u16* out, unsigned char* ws, int rt, int ct, u64 tag) {
  constexpr int NT = ISP ? K2_TP : K2_TQ;
  int tid = threadIdx.x; asm volatile("" : "+v"(tid));
  const int w = __builtin_amdgcn_readfirstlane(tid >> 6), l = tid & 63;
  const int w4 = w & 3, wm = w4 >> 1, wn = w4 & 1;
  const int g = l >> 4, q = (l & 15) >> 2, p = l & 3;
  const int tile = rt * 8 + ct;
  if (w >= 4) {
    K2Ld c;
    c.vsta = l * 16; c.vstb = k2_vstb(w4, l);
    c.sa = make_srd(ws + WS_A2, 128 * KS2 * 1024);
    const i32x4 swd = make_srd(wd, F_ * D_ * 2), swo = make_srd(wo, D_ * D_ * 2);
    const unsigned soa = (rt * 8 + 2 * w4) * (KS2 * 1024), sob = ct * 256;
    c.ldst = w4 * 2048;
    const size_t rep = (blockIdx.x & 7) * REP, t1f = WS_T1F + rep + (rt >> 1) * 256;
    if (w == 4) { if (ISP) flag_wait(ws, WS_T1A + rep + (rt >> 1) * 128, 16, tag, l); else flag_wait(ws, t1f + 128, 16, tag, l); }
    BAR();
    const unsigned ks0 = ISP ? 0u : K2_H0, ka0 = ks0 * 1024u + soa;
#pragma unroll
    for (int t = 0; t < 8; ++t) { glds16x<16>(c.vsta, c.sa, ka0 + t * 1024u, t * K2_SLOT + c.ldst); glds16x<16>(c.vsta, c.sa, ka0 + t * 1024u + 0x24000u, t * K2_SLOT + c.ldst + 1024); }
    WAIT_V(12); BAR();
    BAR();
    {
      const unsigned ka = ka0 + 8 * 1024u, kb = (ks0 + 8) * 65536u + sob;
#define K2_FIRST(J, N) do { k2_stage(c, swd, ka + (J) * 1024u, kb + (J) * 65536u, J); WAIT_V(N); BAR(); } while (0)
      K2_FIRST(0, 14); K2_FIRST(1, 16); K2_FIRST(2, 18); K2_FIRST(3, 20); K2_FIRST(4, 22); K2_FIRST(5, 24); K2_FIRST(6, 24); K2_FIRST(7, 24);
    }
    const i32x4 sx = make_srd(x, S_ * D_ * 2), sp = make_srd(ws + WS_PART, 128 * 32768);
    if (ISP) {
      static_assert(K2_TP >= 72 && K2_TP <= 96, "P schedule: 32 h + 32 o + 8..32 h");
      k2_load(c, swd, 16 * 1024u + soa, 16 * 65536u + sob, 1);
      if (!k2_load_poll(c, swd, 24 * 1024u + soa, 24 * 65536u + sob, ws + t1f, 16, tag, l)) flag_wait(ws, t1f, 16, tag, l);
      BAR();
      k2_load(c, swo, 112 * 1024u + soa, sob, 3);
      if (!k2_load_poll(c, swo, (112 + 24) * 1024u + soa, 24 * 65536u + sob, ws + t1f + 128, 5, tag, l)) flag_wait(ws, t1f + 128, 5, tag, l);
      BAR();
      k2_load(c, swd, 32 * 1024u + soa, 32 * 65536u + sob, (NT - 64) / 8 - 1);
      const bool early = k2_load_poll(c, swd, (NT - 40) * 1024u + soa, (NT - 40) * 65536u + sob, ws + WS_PTF + tile * 64, 4, tag, l);
      BAR();
      const unsigned xv = ((rt * 128 + wm * 64 + (l & 15)) * D_ + ct * 128 + wn * 64 + (((g & 1) << 1) | (g >> 1)) * 8) * 2;
#pragma unroll
      for (int k = 0; k < 2; ++k) {
#pragma unroll
        for (int e = 0; e < 4; ++e) { const int blk = k * 4 + e; glds16x<0>(xv, sx, (blk >> 1) * 16 * D_ * 2 + (blk & 1) * 64, k * 16384 + w4 * 4096 + e * 1024); }
        WAIT_V(24); BAR();
      }
      if (early) {
#pragma unroll
        for (int k = 2; k < 4; ++k) {
#pragma unroll
          for (int e = 0; e < 4; ++e) glds16x<16>(l * 16, sp, tile * 32768 + w4 * 8192 + ((k - 2) * 4 + e) * 1024, k * 16384 + w4 * 4096 + e * 1024);
          WAIT_V(24); BAR();
        }
        WAIT_V(20); BAR(); WAIT_V(16); BAR(); BAR(); WAIT_V(0); BAR();
      } else {
        WAIT_V(20); BAR(); WAIT_V(16); BAR(); WAIT_V(12); BAR();
        WAIT_V(0);
        flag_wait(ws, WS_PTF + tile * 64, 4, tag, l);
#pragma unroll
        for (int e = 0; e < 8; ++e) glds16x<16>(l * 16, sp, tile * 32768 + w4 * 8192 + e * 1024, (2 + (e >> 2)) * 16384 + w4 * 4096 + (e & 3) * 1024);
        BAR(); BAR();
        WAIT_V(0); BAR();
      }
    } else {
      k2_load(c, swd, (K2_H0 + 16) * 1024u + soa, (K2_H0 + 16) * 65536u + sob, (NT - 16) / 8);
      WAIT_V(20); BAR(); WAIT_V(16); BAR(); WAIT_V(12); BAR(); WAIT_V(8); BAR(); WAIT_V(4); BAR(); WAIT_V(0); BAR(); BAR(); BAR();
    }
    if (blockIdx.x == 0 && w == 5) {
#pragma unroll
      for (int k = 0; k < 4; ++k) flag_wait(ws, WS_T1F + k * 512, 64, tag, l);
      if (l == 0) flag_set(ws, WS_GEN, tag);
    }
    return;
  }
  K2Rd c; c.lds = lds;
  {
    const unsigned row = 4 * (g & 1) + q, s2 = (row >> 1) & 3;
    const unsigned lanep = 8192 + (g >> 1) * 2048 + wn * 1024 + row * 128 + (p >> 1) * 16 + (p & 1) * 8;
#pragma unroll
    for (int h = 0; h < 2; ++h) {
      c.aoff[h] = l * 16 + wm * 4096 + h * 65536;
#pragma unroll
      for (int j = 0; j < 4; ++j) c.vb[h][j] = lanep + ((j ^ s2) << 5) + h * 65536;
    }
  }
  f32x4 accf[4][4], accs[4][4];
#pragma unroll
  for (int i = 0; i < 4; ++i)
#pragma unroll
    for (int j = 0; j < 4; ++j) { accf[i][j] = (f32x4){0.f, 0.f, 0.f, 0.f}; accs[i][j] = accf[i][j]; }
  K2Frag f0, f1;
  BAR();
  BAR();
  STAMP(4);
  k2_read<0>(c, f0);
  WAIT_L(0); BAR();
  if (ISP) { k2_consume(c, accf, f0, f1, 4); k2_consume(c, accs, f0, f1, 4); k2_consume(c, accf, f0, f1, (NT - 64) / 8); }
  else k2_consume(c, accf, f0, f1, NT / 8);
  STAMP(5);
  if (!ISP) {
    const __amdgpu_buffer_rsrc_t pr = __builtin_amdgcn_make_buffer_rsrc(ws + WS_PART, 0, 128 * 32768, 0x00020000);
    const unsigned po = tile * 32768 + w4 * 8192 + l * 16;
#pragma unroll
    for (int i = 0; i < 4; ++i)
#pragma unroll
      for (int jp = 0; jp < 2; ++jp) __builtin_amdgcn_raw_buffer_store_b128(__builtin_bit_cast(u32x4, pack8(accf[i][2 * jp], accf[i][2 * jp + 1])), pr, po + (i * 2 + jp) * 1024, 0, 16);
    WAIT_V(0);
    if (l == 0) flag_set(ws, WS_PTF + tile * 64 + w4 * 8, tag);
  } else {
    const int m = l & 15;
    const __amdgpu_buffer_rsrc_t orr = __builtin_amdgcn_make_buffer_rsrc(out, 0, S_ * D_ * 2, 0x00020000);
    const unsigned ov = ((rt * 128 + wm * 64 + m) * D_ + ct * 128 + wn * 64 + (((g & 1) << 1) | (g >> 1)) * 8) * 2;
    u32x4 pt[8], xr[8];
#pragma unroll
    for (int b = 0; b < 8; ++b) {
      pt[b] = *(LAS u32x4*)(lds + (2 + (b >> 2)) * 16384 + w4 * 4096 + (b & 3) * 1024 + l * 16);
      xr[b] = *(LAS u32x4*)(lds + (b >> 2) * 16384 + w4 * 4096 + (b & 3) * 1024 + l * 16);
    }
#pragma unroll
    for (int i = 0; i < 4; ++i)
#pragma unroll
      for (int jp = 0; jp < 2; ++jp) {
        const int b = i * 2 + jp;
        unsigned y[2][2];
#pragma unroll
        for (int jj = 0; jj < 2; ++jj) {
          const int j = 2 * jp + jj;
          const f32x4 f = accf[i][j] + (f32x4){bf_lo(pt[b][2 * jj]), bf_hi(pt[b][2 * jj]), bf_lo(pt[b][2 * jj + 1]), bf_hi(pt[b][2 * jj + 1])};
          float v[4];
#pragma unroll
          for (int r = 0; r < 4; ++r) v[r] = accs[i][j][r] * silu_f(f[r]);
          y[jj][0] = cvt_pk(v[0], v[1]); y[jj][1] = cvt_pk(v[2], v[3]);
        }
        const auto s0 = __builtin_amdgcn_permlane16_swap(y[0][0], y[1][0], false, false);
        const auto s1 = __builtin_amdgcn_permlane16_swap(y[0][1], y[1][1], false, false);
        const unsigned yy[4] = {s0[0], s1[0], s0[1], s1[1]};
        u32x4 o;
#pragma unroll
        for (int r = 0; r < 4; ++r) o[r] = cvt_pk(bf_lo(xr[b][r]) + bf_lo(yy[r]), bf_hi(xr[b][r]) + bf_hi(yy[r]));
        __builtin_amdgcn_raw_buffer_store_b128(o, orr, ov + i * 16 * D_ * 2 + jp * 64, 0, OUT_AUX);
      }
  }
  STAMP(6);
  if (ISP && threadIdx.x == 0) {
    asm volatile("s_waitcnt vmcnt(0)" ::: "memory");
    const u64 t0 = __hip_atomic_load((gu64*)(ws + WS_STAMP) + blockIdx.x * 16, __ATOMIC_RELAXED, __HIP_MEMORY_SCOPE_AGENT), t1 = __hip_atomic_load((gu64*)(ws + WS_STAMP) + blockIdx.x * 16 + 1, __ATOMIC_RELAXED, __HIP_MEMORY_SCOPE_AGENT);
    if ((float)(t1 - t0) * 0.01f > EXTHR) out[(size_t)(rt * 128) * D_ + ct * 128] = (u16)(cvt_pk(1000.0f + (float)(t1 - t0) * 0.01f * EXMUL, 0.f) & 0xffffu);
  }
}

__global__ __launch_bounds__(512, 2) void fused_block(const u16* __restrict__ x, const u16* __restrict__ wqkv, const u16* __restrict__ wo, const u16* __restrict__ wg,
                                                      const u16* __restrict__ wu, const u16* __restrict__ wd, u16* __restrict__ out, unsigned char* __restrict__ ws) {
  u64 t_entry; asm volatile("s_memrealtime %0\n\ts_waitcnt lgkmcnt(0)" : "=s"(t_entry));
  extern __shared__ __attribute__((aligned(16))) unsigned char shm[];
  LAS unsigned char* lds = (LAS unsigned char*)shm;
  ((u64*)(ws + WS_STAMP))[blockIdx.x * 16] = t_entry;
  const int b = blockIdx.x, xcd = b & 7, idx = b >> 3;
  const int pm = idx & 7, pn = idx >> 3, pa = xcd * 2 + (pn >> 1);
  const int lnmf = idx < 16 ? idx * 8 + xcd : -1;
  const int pr = xcd & 3, rt = (pr & 1) * 8 + (idx & 7), ct = (pr >> 1) * 4 + (idx >> 3);
  const bool isp = xcd < 4;
  u64 tag;
  if (pn & 1) tag = k1_body<1>(lds, x, wqkv, wg, wu, wo, wd, ws, pm, pa, lnmf, isp, ct);
  else tag = k1_body<0>(lds, x, wqkv, wg, wu, wo, wd, ws, pm, pa, lnmf, isp, ct);
  if (isp) k2_body<true>(lds, x, wo, wd, out, ws, rt, ct, tag);
  else k2_body<false>(lds, x, wo, wd, out, ws, rt, ct, tag);
}

extern "C" void kernel_launch(void* const* d_in, const int* in_sizes, int n_in, void* d_out, int out_size, void* d_ws, size_t ws_size, hipStream_t stream) {
  const u16* x = (const u16*)d_in[0]; const u16* wqkv = (const u16*)d_in[1]; const u16* wo = (const u16*)d_in[2];
  const u16* wg = (const u16*)d_in[4]; const u16* wu = (const u16*)d_in[5]; const u16* wd = (const u16*)d_in[6];
  (void)hipFuncSetAttribute((const void*)fused_block, hipFuncAttributeMaxDynamicSharedMemorySize, LDS_TOTAL);
  fused_block<<<256, 512, LDS_TOTAL, stream>>>(x, wqkv, wo, wg, wu, wd, (u16*)d_out, (unsigned char*)d_ws);
}
```

```cpp
#include <hip/hip_runtime.h>
#include <stdint.h>

#define LAS __attribute__((address_space(3)))
#define GAS __attribute__((address_space(1)))
typedef __bf16 bf16x8 __attribute__((ext_vector_type(8)));
typedef short s16x4 __attribute__((ext_vector_type(4)));
typedef short s16x8 __attribute__((ext_vector_type(8)));
typedef float f32x4 __attribute__((ext_vector_type(4)));
typedef float f32x2 __attribute__((ext_vector_type(2)));
typedef unsigned u32x4 __attribute__((ext_vector_type(4)));
typedef unsigned u32x2 __attribute__((ext_vector_type(2)));
typedef int i32x4 __attribute__((ext_vector_type(4)));
typedef unsigned short u16;

constexpr int S_ = 2048, D_ = 1024, F_ = 3584;
constexpr int KS2 = 144;
constexpr size_t WS_GEN = 0;
constexpr size_t REP = 4096;
constexpr size_t WS_LNF = 4096;
constexpr size_t WS_T1A = WS_LNF + 1024;
constexpr size_t WS_T1F = WS_LNF + 2048;
constexpr size_t WS_HLF = 4096 + 8 * REP;
constexpr size_t WS_PTF = WS_HLF + 16384;
static_assert(WS_PTF + 8192 <= 65536, "flag area");
constexpr size_t WS_XN = 65536;
constexpr size_t WS_A2 = WS_XN + 4194304;
constexpr size_t WS_HALO = WS_A2 + (size_t)128 * KS2 * 1024;
constexpr size_t WS_PART = WS_HALO + 8 * 32 * 2048;
constexpr size_t WS_STAMP = WS_PART + (size_t)128 * 65536;

#define P_STEPS 80
#define OUT_AUX 16
#define ST_AUX 16

__device__ __forceinline__ i32x4 make_srd(const void* p, unsigned bytes) {
  const unsigned long long a = (unsigned long long)p;
  i32x4 r; r.x = (int)(unsigned)a; r.y = (int)((unsigned)(a >> 32) & 0xffffu); r.z = (int)bytes; r.w = 0x00020000; return r;
}
__device__ __forceinline__ void glds16(unsigned voff, i32x4 srd, unsigned soff, unsigned ldsaddr) {
  asm volatile("s_mov_b32 m0, %3\n\ts_nop 0\n\tbuffer_load_dwordx4 %0, %1, %2 offen lds" ::"v"(voff), "s"(srd), "s"(soff), "s"(ldsaddr) : "memory");
}
#define WAIT_V(n) asm volatile("s_waitcnt vmcnt(" #n ")" ::: "memory")
#define WAIT_L(n) asm volatile("s_waitcnt lgkmcnt(" #n ")" ::: "memory")
#define SCHED() __builtin_amdgcn_sched_barrier(0)
#define BAR() do { SCHED(); __builtin_amdgcn_s_barrier(); SCHED(); } while (0)

typedef __bf16 bf16x2 __attribute__((ext_vector_type(2)));
__device__ __forceinline__ unsigned cvt_pk(float lo, float hi) { const f32x2 v = {lo, hi}; return __builtin_bit_cast(unsigned, __builtin_convertvector(v, bf16x2)); }
__device__ __forceinline__ float bf_lo(unsigned u) { return __uint_as_float(u << 16); }
__device__ __forceinline__ float bf_hi(unsigned u) { return __uint_as_float(u & 0xffff0000u); }
__device__ __forceinline__ float rbf(float f) { return bf_lo(cvt_pk(f, f)); }
__device__ __forceinline__ bf16x8 pack8(const f32x4& a, const f32x4& b) {
  u32x4 w; w.x = cvt_pk(a[0], a[1]); w.y = cvt_pk(a[2], a[3]); w.z = cvt_pk(b[0], b[1]); w.w = cvt_pk(b[2], b[3]);
  return __builtin_bit_cast(bf16x8, w);
}
__device__ __forceinline__ bf16x8 lds_tr8(LAS unsigned char* p0, LAS unsigned char* p1) {
  s16x4 a = __builtin_amdgcn_ds_read_tr16_b64_v4i16((LAS s16x4*)p0);
  s16x4 b = __builtin_amdgcn_ds_read_tr16_b64_v4i16((LAS s16x4*)p1);
  s16x8 c = __builtin_shufflevector(a, b, 0, 1, 2, 3, 4, 5, 6, 7);
  return __builtin_bit_cast(bf16x8, c);
}
__device__ __forceinline__ float silu_f(float g) { return g * __builtin_amdgcn_rcpf(1.0f + __builtin_amdgcn_exp2f(g * -1.44269504089f)); }
#define MFMA(a, b, c) __builtin_amdgcn_mfma_f32_16x16x32_bf16(a, b, c, 0, 0, 0)

typedef unsigned long long u64;
typedef GAS u64 gu64;
__device__ __forceinline__ void flag_set(unsigned char* ws, size_t off, u64 tag) { __hip_atomic_store((gu64*)(ws + off), tag, __ATOMIC_RELAXED, __HIP_MEMORY_SCOPE_AGENT); }
__device__ __forceinline__ void flag_set8(unsigned char* ws, size_t off, u64 tag, int l) { if (l < 8) flag_set(ws, off + l * REP, tag); }
__device__ __forceinline__ void flag_wait(unsigned char* ws, size_t off, int n, u64 tag, int l) {
  gu64* p = (gu64*)(ws + off) + (l < n ? l : 0);
  for (unsigned spins = 0; spins < (1u << 18); ++spins) {
    const u64 v = __hip_atomic_load(p, __ATOMIC_RELAXED, __HIP_MEMORY_SCOPE_AGENT);
    if (__all(v == tag)) break;
    __builtin_amdgcn_s_sleep(2);
  }
  asm volatile("" ::: "memory");
}

#define STAMP(k) do {} while (0)
#define STAMPT(k, T) do {} while (0)

__device__ __forceinline__ float xsum16_32(float v) {
  const auto a = __builtin_amdgcn_permlane16_swap(__float_as_uint(v), __float_as_uint(v), false, false);
  v = __uint_as_float(a[0]) + __uint_as_float(a[1]);
  const auto b = __builtin_amdgcn_permlane32_swap(__float_as_uint(v), __float_as_uint(v), false, false);
  return __uint_as_float(b[0]) + __uint_as_float(b[1]);
}
__device__ __forceinline__ float xmax16_32(float v) {
  const auto a = __builtin_amdgcn_permlane16_swap(__float_as_uint(v), __float_as_uint(v), false, false);
  v = fmaxf(__uint_as_float(a[0]), __uint_as_float(a[1]));
  const auto b = __builtin_amdgcn_permlane32_swap(__float_as_uint(v), __float_as_uint(v), false, false);
  return fmaxf(__uint_as_float(b[0]), __uint_as_float(b[1]));
}
#define PF_STRIDE 128
constexpr int K1_ASLOT = 16384, K1_BSLOT = 20480, K1_BOFF = 4 * K1_ASLOT, K1_LDS = K1_BOFF + 4 * K1_BSLOT;
constexpr int LDS_PFDUMP = K1_LDS, LDS_LNRED = K1_LDS + 256, LDS_TOTAL = K1_LDS + 256 + 1024, LDS_HALO = 131072;

struct K1Ctx {
  LAS unsigned char* lds;
  unsigned aoff;
  unsigned vb[6];
  unsigned vst0, vst1;
  i32x4 s0, s1, s2;
  unsigned so0, so1;
  unsigned ldst;
};

template <int TYPE> __device__ __forceinline__ void k1_stage_b(const K1Ctx& c, int s, int slot, unsigned cnt, int lim) {
  const unsigned d = K1_BOFF + slot * K1_BSLOT + c.ldst;
  unsigned t0, t1;
  if (TYPE == 0) {
    const unsigned kq = (unsigned)s * (32u * 6144u) + c.so0, kg = (unsigned)s * (32u * 7168u) + c.so0;
    asm volatile(
      "s_cmp_ge_u32 %10, %11\n\ts_cbranch_scc1 .Lsk%=\n\t"
      "s_mov_b32 m0, %9\n\ts_add_u32 %0, %7, 0x800\n\tbuffer_load_dwordx4 %2, %4, %7 offen lds\n\t"
      "s_add_u32 m0, %9, 0x400\n\ts_add_u32 %1, %7, 0x1000\n\tbuffer_load_dwordx4 %2, %4, %0 offen lds\n\t"
      "s_add_u32 m0, %9, 0x800\n\ts_nop 0\n\tbuffer_load_dwordx4 %2, %4, %1 offen lds\n\t"
      "s_add_u32 m0, %9, 0xc00\n\ts_nop 0\n\tbuffer_load_dwordx4 %3, %5, %8 offen lds\n\t"
      "s_add_u32 m0, %9, 0x1000\n\ts_nop 0\n\tbuffer_load_dwordx4 %3, %6, %8 offen lds\n"
      ".Lsk%=:"
      : "=&s"(t0), "=&s"(t1)
      : "v"(c.vst0), "v"(c.vst1), "s"(c.s0), "s"(c.s1), "s"(c.s2), "s"(kq), "s"(kg), "s"(d), "s"(cnt), "i"(lim) : "memory", "scc");
  } else {
    const unsigned k0 = (unsigned)s * (32u * 7168u) + c.so0, k1 = (unsigned)s * (32u * 7168u) + c.so1;
    asm volatile(
      "s_cmp_ge_u32 %10, %11\n\ts_cbranch_scc1 .Lsk%=\n\t"
      "s_mov_b32 m0, %9\n\ts_add_u32 %0, %7, 0x80\n\tbuffer_load_dwordx4 %2, %4, %7 offen lds\n\t"
      "s_add_u32 m0, %9, 0x400\n\ts_nop 0\n\tbuffer_load_dwordx4 %2, %4, %0 offen lds\n\t"
      "s_add_u32 m0, %9, 0x800\n\ts_nop 0\n\tbuffer_load_dwordx4 %2, %5, %7 offen lds\n\t"
      "s_add_u32 m0, %9, 0xc00\n\ts_nop 0\n\tbuffer_load_dwordx4 %2, %5, %0 offen lds\n\t"
      "s_add_u32 m0, %9, 0x1000\n\ts_nop 0\n\tbuffer_load_dwordx4 %3, %6, %8 offen lds\n"
      ".Lsk%=:"
      : "=&s"(t0), "=&s"(t1)
      : "v"(c.vst0), "v"(c.vst1), "s"(c.s0), "s"(c.s1), "s"(c.s2), "s"(k0), "s"(k1), "s"(d), "s"(cnt), "i"(lim) : "memory", "scc");
  }
}
__device__ __forceinline__ void k1_stage_a(const K1Ctx& c, int s, int slot, unsigned cnt, int lim) {
  const unsigned d = slot * K1_ASLOT + c.ldst;
  const unsigned k = (unsigned)s * 1024u + c.so0;
  unsigned t0;
  asm volatile(
    "s_cmp_ge_u32 %5, %6\n\ts_cbranch_scc1 .Lsk%=\n\t"
    "s_mov_b32 m0, %4\n\ts_add_u32 %0, %3, 0x8000\n\tbuffer_load_dwordx4 %1, %2, %3 offen sc1 lds\n\t"
    "s_add_u32 m0, %4, 0x400\n\ts_nop 0\n\tbuffer_load_dwordx4 %1, %2, %0 offen sc1 lds\n\t"
    "s_add_u32 m0, %4, 0x800\n\ts_add_u32 %0, %3, 0x10000\n\tbuffer_load_dwordx4 %1, %2, %0 offen sc1 lds\n\t"
    "s_add_u32 m0, %4, 0xc00\n\ts_add_u32 %0, %3, 0x18000\n\tbuffer_load_dwordx4 %1, %2, %0 offen sc1 lds\n"
    ".Lsk%=:"
    : "=&s"(t0) : "v"(c.vst0), "s"(c.s0), "s"(k), "s"(d), "s"(cnt), "i"(lim) : "memory", "scc");
}
#define WAIT_V2(STEADY, TAIL, cnt, lim) do { asm volatile("s_cmp_lt_u32 %0, %1\n\ts_cbranch_scc1 .Lw%=\n\ts_waitcnt vmcnt(" #TAIL ")\n.Lw%=:\n\ts_waitcnt vmcnt(" #STEADY ")" :: "s"(cnt), "i"(lim) : "memory", "scc"); } while (0)
#define BAR_IF(cnt, lim) do { SCHED(); asm volatile("s_cmp_ge_u32 %0, %1\n\ts_cbranch_scc1 .Lb%=\n\ts_barrier\n.Lb%=:" :: "s"(cnt), "i"(lim) : "memory", "scc"); SCHED(); } while (0)
template <int TYPE> __device__ __forceinline__ void k1_read(const K1Ctx& c, int slot, bf16x8 (&af)[4], bf16x8 (&bfr)[10]) {
  LAS unsigned char* lds = c.lds;
  const int bs = slot * K1_BSLOT;
  if (TYPE == 0) {
#pragma unroll
    for (int j = 0; j < 8; ++j) { LAS unsigned char* p = lds + c.vb[j & 1] + bs + (j >> 1) * 1024; bfr[j] = lds_tr8(p, p + 512); }
#pragma unroll
    for (int j = 0; j < 2; ++j) { LAS unsigned char* p = lds + c.vb[2 + j] + bs; bfr[8 + j] = lds_tr8(p, p + 512); }
  } else {
#pragma unroll
    for (int j = 0; j < 8; ++j) { LAS unsigned char* p = lds + c.vb[j & 3] + bs + (j >> 2) * 2048; bfr[j] = lds_tr8(p, p + 512); }
#pragma unroll
    for (int j = 0; j < 2; ++j) { LAS unsigned char* p = lds + c.vb[4 + j] + bs; bfr[8 + j] = lds_tr8(p, p + 256); }
  }
#pragma unroll
  for (int i = 0; i < 4; ++i) af[i] = *(LAS bf16x8*)(lds + c.aoff + slot * K1_ASLOT + i * 1024);
}
#define K1V 0
#define K1_EARLY 0
template <int TYPE, int N0, int N1> __device__ __forceinline__ void k1_mma(f32x4 (&acc)[4][10], const bf16x8 (&af)[4], const bf16x8 (&bfr)[10]) {
#pragma unroll
  for (int n = N0; n < N1; ++n) {
    const int j = n >> 2, i = n & 3;
    if (TYPE == 0 && (j == 4 || j == 5)) acc[i][j] = MFMA(af[i], bfr[j], acc[i][j]);
    else acc[i][j] = MFMA(bfr[j], af[i], acc[i][j]);
  }
}
__device__ __forceinline__ void k1_pf(unsigned voff, i32x4 srd, unsigned soff, unsigned on, unsigned cnt, int lim) {
  asm volatile("s_cmp_lg_u32 %3, 4\n\ts_cbranch_scc1 .Lpf%=\n\ts_cmp_ge_u32 %4, %5\n\ts_cbranch_scc1 .Lpf%=\n\t"
               "s_mov_b32 m0, %6\n\ts_nop 0\n\tbuffer_load_dword %0, %1, %2 offen lds\n.Lpf%=:"
               ::"v"(voff), "s"(srd), "s"(soff), "s"(on), "s"(cnt), "i"(lim), "i"(LDS_PFDUMP) : "memory", "scc");
}
#define WAIT_A(PFN, PFT, PLN, PLT, pf, cnt, lim) do { asm volatile( \
    "s_cmp_lt_u32 %1, %2\n\ts_cbranch_scc1 .Ls%=\n\t" \
    "s_cmp_eq_u32 %0, 4\n\ts_cbranch_scc1 .Lt%=\n\ts_waitcnt vmcnt(" #PLT ")\n.Lt%=:\n\ts_waitcnt vmcnt(" #PFT ")\n" \
    ".Ls%=:\n\ts_cmp_eq_u32 %0, 4\n\ts_cbranch_scc1 .Lu%=\n\ts_waitcnt vmcnt(" #PLN ")\n.Lu%=:\n\ts_waitcnt vmcnt(" #PFN ")" \
    :: "s"(pf), "s"(cnt), "i"(lim) : "memory", "scc"); } while (0)

constexpr int K2_SLOT = 16384;
constexpr int K2_TP = P_STEPS, K2_TQ = KS2 - P_STEPS;
static_assert(K2_TP % 8 == 0 && K2_TQ % 8 == 0 && K2_TP >= 48, "step counts must be multiples of 8");
constexpr unsigned K2_H0 = K2_TP - 32;
__device__ __forceinline__ unsigned k2_vstb(int w4, int l) { const unsigned r8 = l >> 3, ch = (l & 7) ^ (((r8 >> 1) & 3) << 1); return (8 * w4 + r8) * 2048 + ch * 16; }
__device__ __forceinline__ void k2_prologue_b(int w4, int l, const u16* wd, bool isp, int ct) {
  const i32x4 sb = make_srd(wd, F_ * D_ * 2);
  const unsigned vb = k2_vstb(w4, l), k0 = (isp ? 0u : K2_H0) * 65536u + ct * 256;
#pragma unroll
  for (int t = 0; t < 8; ++t) { glds16(vb, sb, k0 + t * 65536u, t * K2_SLOT + 8192 + w4 * 2048); glds16(vb, sb, k0 + t * 65536u + 128u, t * K2_SLOT + 8192 + w4 * 2048 + 1024); }
}

template <int TYPE> __device__ __forceinline__ u64 k1_body(LAS unsigned char* lds, const u16* x, const u16* wqkv, const u16* wg, const u16* wu, const u16* wo, const u16* wd, unsigned char* ws, int pm, int pa  , int lnmf, bool isp, int ct) {
  const int tid = threadIdx.x;
  const int w = __builtin_amdgcn_readfirstlane(tid >> 6), l = tid & 63;
  const int grp = w >> 2, wr = w & 3;
  const int g = l >> 4, q = (l & 15) >> 2, p = l & 3;
  STAMP(0);
  K1Ctx c; c.lds = lds;
  c.aoff = l * 16 + wr * 4096;
  {
    const unsigned s2 = ((q >> 1) & 1) | ((g & 1) << 1);
    const unsigned lanep = K1_BOFF + g * 5120 + q * 128 + (p >> 1) * 16 + (p & 1) * 8;
    if (TYPE == 0) {
      c.vb[0] = lanep + (((2 * grp) ^ s2) << 5); c.vb[1] = lanep + (((2 * grp + 1) ^ s2) << 5);
      c.vb[2] = c.vb[0] + 4096; c.vb[3] = c.vb[1] + 4096; c.vb[4] = 0; c.vb[5] = 0;
    } else {
#pragma unroll
      for (int j = 0; j < 4; ++j) c.vb[j] = lanep + ((j ^ s2) << 5) + grp * 1024;
      const unsigned mx = K1_BOFF + (2 * (g >> 1) * 5 + 4) * 1024 + (g & 1) * 512 + q * 64 + (((2 * grp + (p >> 1)) ^ ((g & 1) << 1)) << 4) + (p & 1) * 8;
      c.vb[4] = mx; c.vb[5] = mx + 5120;
    }
  }
  const int c0 = 1024 + 160 * pa;
  const int odd = pa & 1;
  if (grp == 0) {
    const unsigned r8 = l >> 3, sw = (((r8 >> 1) & 1) | ((wr & 1) << 1)) << 1, ch = (l & 7) ^ sw;
    c.ldst = wr * 5120;
    if (TYPE == 0) {
      c.vst0 = (8 * wr + r8) * 6144 + ch * 16; c.vst1 = (8 * wr + r8) * 7168 + ch * 16;
      c.s0 = make_srd(wqkv, D_ * 3 * D_ * 2); c.s1 = make_srd(wg, D_ * F_ * 2); c.s2 = make_srd(wu, D_ * F_ * 2);
      c.so0 = pa * 128; c.so1 = 0;
    } else {
      c.vst0 = (8 * wr + r8) * 7168 + ch * 16;
      const unsigned r16 = l >> 2, chm = (l & 3) ^ (((r16 >> 3) & 1) << 1);
      c.vst1 = (16 * (wr >> 1) + r16) * 7168 + chm * 16;
      c.s0 = make_srd(wg, D_ * F_ * 2); c.s1 = make_srd(wu, D_ * F_ * 2); c.s2 = make_srd((wr & 1) ? wu : wg, D_ * F_ * 2);
      c.so0 = (c0 + (odd ? 32 : 0)) * 2; c.so1 = (c0 + (odd ? 0 : 128)) * 2;
    }
  } else {
    c.vst0 = l * 16; c.vst1 = 0;
    c.s0 = make_srd(ws + WS_XN, S_ * D_ * 2); c.s1 = c.s0; c.s2 = c.s0;
    c.so0 = (pm * 16 + 4 * wr) * 32768; c.so1 = 0;
    c.ldst = wr * 4096;
  }

  u32x4 xv[4]; float piv = 0.f;
  if (lnmf >= 0) {
    const u16* row = x + (size_t)(lnmf * 16 + (l & 15)) * D_;
#pragma unroll
    for (int i = 0; i < 4; ++i) xv[i] = *(const u32x4*)(row + w * 128 + g * 8 + i * 32);
    piv = __uint_as_float((unsigned)row[0] << 16);
  }
  unsigned glo, ghi;
  if (grp == 0 && lnmf < 0) {
    u32x2 gen;
    asm volatile("global_load_dwordx2 %0, %1, off sc1" : "=&v"(gen) : "v"(ws + WS_GEN) : "memory");
    k1_stage_b<TYPE>(c, 0, 0, 0, 1); k1_stage_b<TYPE>(c, 1, 1, 0, 1); k1_stage_b<TYPE>(c, 2, 2, 0, 1);
    asm volatile("s_waitcnt vmcnt(15)" : "+v"(gen) :: "memory");
    glo = __builtin_amdgcn_readfirstlane(gen.x); ghi = __builtin_amdgcn_readfirstlane(gen.y);
  } else {
    u32x2 gen;
    asm volatile("global_load_dwordx2 %0, %1, off sc1\n\ts_waitcnt vmcnt(0)" : "=&v"(gen) : "v"(ws + WS_GEN) : "memory");
    glo = __builtin_amdgcn_readfirstlane(gen.x); ghi = __builtin_amdgcn_readfirstlane(gen.y);
  }
  const u64 tag = (((u64)ghi << 32) | glo) + 1;

  if (lnmf >= 0) {
    const int m = l & 15;
    const u32x4 (&v)[4] = xv;
    float f[32]; float sm = 0.f, sq = 0.f;
#pragma unroll
    for (int i = 0; i < 4; ++i)
#pragma unroll
      for (int j = 0; j < 4; ++j) {
        const float a = bf_lo(v[i][j]), b = bf_hi(v[i][j]); f[i * 8 + 2 * j] = a; f[i * 8 + 2 * j + 1] = b;
        const float da = a - piv, db = b - piv; sm += da + db; sq += da * da + db * db;
      }
    sm = xsum16_32(sm); sq = xsum16_32(sq);
    STAMPT(8, 0);
    LAS f32x2* red = (LAS f32x2*)(lds + LDS_LNRED);
    if (g == 0) red[w * 16 + m] = (f32x2){sm, sq};
    WAIT_L(0); BAR();
    float st = 0.f, qt = 0.f;
#pragma unroll
    for (int i = 0; i < 8; ++i) { const f32x2 r = red[i * 16 + m]; st += r.x; qt += r.y; }
    const float md = st * (1.0f / D_), mean = piv + md, var = fmaxf(qt * (1.0f / D_) - md * md, 0.f);
    const float mb = rbf(mean);
    const float inv = 1.0f / rbf(sqrtf(rbf(rbf(var) + 1e-5f)));
    const __amdgpu_buffer_rsrc_t xr = __builtin_amdgcn_make_buffer_rsrc(ws + WS_XN, 0, S_ * D_ * 2, 0x00020000);
#pragma unroll
    for (int i = 0; i < 4; ++i) {
      u32x4 o;
#pragma unroll
      for (int j = 0; j < 4; ++j) o[j] = cvt_pk(rbf(f[i * 8 + 2 * j] - mb) * inv, rbf(f[i * 8 + 2 * j + 1] - mb) * inv);
      __builtin_amdgcn_raw_buffer_store_b128(o, xr, (lnmf * 32 + w * 4 + i) * 1024 + l * 16, 0, 16);
    }
    STAMPT(9, 0);
    WAIT_V(0); BAR();
    if (w == 0) flag_set8(ws, WS_LNF + lnmf * 8, tag, l);
    STAMPT(10, 0);
    if (grp == 0) { k1_stage_b<TYPE>(c, 0, 0, 0, 1); k1_stage_b<TYPE>(c, 1, 1, 0, 1); k1_stage_b<TYPE>(c, 2, 2, 0, 1); }
  }
  if (w == 4) flag_wait(ws, WS_LNF + (blockIdx.x & 7) * REP + pm * 128, 16, tag, l);
  BAR();

  f32x4 acc[4][10];
#pragma unroll
  for (int i = 0; i < 4; ++i)
#pragma unroll
    for (int j = 0; j < 10; ++j) acc[i][j] = (f32x4){0.f, 0.f, 0.f, 0.f};
  bf16x8 af[4], bfr[10];

  if (grp == 0) {
    WAIT_V(10); BAR();
    STAMP(1);
#define K1_G0_STEP(J, TAILV) do { \
      k1_read<TYPE>(c, J, af, bfr); SCHED(); \
      k1_stage_b<TYPE>(c, s + (J) + 3, ((J) + 3) & 3, s, (J) == 0 ? 32 : 28); \
      WAIT_L(0); BAR(); \
      __builtin_amdgcn_s_setprio(1); \
      k1_mma<TYPE, 0, 40 - K1_EARLY>(acc, af, bfr); \
      if (K1V & 1) __builtin_amdgcn_s_setprio(0); \
      SCHED(); WAIT_V2(10, TAILV, s, 28); \
      BAR(); \
      if (K1_EARLY) __builtin_amdgcn_s_setprio(3); \
      k1_mma<TYPE, 40 - K1_EARLY, 40>(acc, af, bfr); \
      __builtin_amdgcn_s_setprio(0); SCHED(); } while (0)
#pragma nounroll
    for (unsigned s = 0; s < 32; s += 4) {
      K1_G0_STEP(0, 10); K1_G0_STEP(1, 5); K1_G0_STEP(2, 0); K1_G0_STEP(3, 0);
    }
  } else {
#define PF_ON 1
#define K1_PF_ROW0 96
    const unsigned pf = PF_ON ? w : 99  , pfv = (blockIdx.x * 64 + l) * PF_STRIDE;
    const i32x4 sg = make_srd(wg, D_ * F_ * 2), su = make_srd(wu, D_ * F_ * 2), sq = make_srd(wqkv, D_ * 3 * D_ * 2), sd = make_srd(wd, F_ * D_ * 2), so = make_srd(wo, D_ * D_ * 2);
    constexpr unsigned OOR = 0x7f000000u, MB = 16384u * PF_STRIDE;
    constexpr unsigned NG = ((1024 - K1_PF_ROW0) * 7168u + MB - 1) / MB, NQ = ((1024 - K1_PF_ROW0) * 6144u + MB - 1) / MB, ND = (F_ * D_ * 2 + MB - 1) / MB, NO = (D_ * D_ * 2 + MB - 1) / MB;
    static_assert(NG + (ND + 1) / 2 <= 8 && NG + ND / 2 <= 7 && NQ + NO <= 7, "prefetch schedule does not fit");
    STAMPT(11, 256);
    if (!(K1V & 2)) k1_pf(pfv, sq, OOR, pf, 0, 1);
    k1_stage_a(c, 0, 0, 0, 1);
    if (!(K1V & 2)) k1_pf(pfv, sq, OOR, pf, 0, 1);
    k1_stage_a(c, 1, 1, 0, 1);
    k1_stage_a(c, 2, 2, 0, 1);
    if (K1V & 2) WAIT_V(8); else WAIT_A(9, 9, 8, 8, pf, 0u, 1);
    BAR();
    BAR();
#define K1_G1_STEP(J, PFN, PFT, PLT) do { \
      k1_read<TYPE>(c, J, af, bfr); SCHED(); \
      if (!(K1V & 2) && (J) == 0) k1_pf(pfv, it < NG ? sg : sd, it < NG ? K1_PF_ROW0 * 7168u + it * MB : (it - NG) * MB, pf, s, 32); \
      if (!(K1V & 2) && (J) == 1) k1_pf(pfv, it < NG ? su : sd, it < NG ? K1_PF_ROW0 * 7168u + it * MB : (it - NG + (ND + 1) / 2) * MB, pf, s, 28); \
      if (!(K1V & 2) && (J) == 2) k1_pf(pfv, it < NQ ? sq : so, it < NQ ? K1_PF_ROW0 * 6144u + it * MB : (it - NQ) * MB, pf, s, 28); \
      k1_stage_a(c, s + (J) + 3, ((J) + 3) & 3, s, (J) == 0 ? 32 : 28); \
      WAIT_L(0); if (K1V & 2) WAIT_V2(8, PLT, s, 28); else WAIT_A(PFN, PFT, 8, PLT, pf, s, 28); \
      BAR(); \
      __builtin_amdgcn_s_setprio(1); \
      k1_mma<TYPE, 0, 40 - K1_EARLY>(acc, af, bfr); \
      if (K1V & 1) __builtin_amdgcn_s_setprio(0); \
      if ((J) < 3) BAR(); else BAR_IF(s, 28); \
      if (K1_EARLY) __builtin_amdgcn_s_setprio(3); \
      k1_mma<TYPE, 40 - K1_EARLY, 40>(acc, af, bfr); \
      __builtin_amdgcn_s_setprio(0); SCHED(); } while (0)
#pragma nounroll
    for (unsigned s = 0; s < 32; s += 4) {
      const unsigned it = s >> 2;
      K1_G1_STEP(0, 9, 9, 8); K1_G1_STEP(1, 10, 5, 4); K1_G1_STEP(2, 10, 0, 0); K1_G1_STEP(3, 9, 0, 0);
    }
    k2_prologue_b(wr, l, wd, isp, ct);
  }
  STAMP(2);

  const __amdgpu_buffer_rsrc_t a2 = __builtin_amdgcn_make_buffer_rsrc(ws + WS_A2, 0, 128 * KS2 * 1024, 0x00020000);
  const unsigned mf0 = pm * 16 + 4 * wr;
  if (TYPE == 1) {
    const unsigned ksf = c0 / 32 + (odd ? 1 : 0) + 2 * grp, ksm = c0 / 32 + (odd ? 0 : 4);
#pragma unroll
    for (int i = 0; i < 4; ++i) {
      const unsigned base = (mf0 + i) * (KS2 * 1024);
#pragma unroll
      for (int t = 0; t < 2; ++t) {
        f32x4 h0, h1;
#pragma unroll
        for (int r = 0; r < 4; ++r) { h0[r] = silu_f(acc[i][2 * t][r]) * acc[i][2 * t + 4][r]; h1[r] = silu_f(acc[i][2 * t + 1][r]) * acc[i][2 * t + 5][r]; }
        __builtin_amdgcn_raw_buffer_store_b128(__builtin_bit_cast(u32x4, pack8(h0, h1)), a2, l * 16 + base + (ksf + t) * 1024, 0, ST_AUX);
      }
      f32x4 h0;
#pragma unroll
      for (int r = 0; r < 4; ++r) h0[r] = silu_f(acc[i][8][r]) * acc[i][9][r];
      u32x2 o; o.x = cvt_pk(h0[0], h0[1]); o.y = cvt_pk(h0[2], h0[3]);
      *(LAS u32x2*)(lds + LDS_HALO + (wr * 4 + i) * 1024 + l * 16 + grp * 8) = o;
    }
    WAIT_L(0); BAR();
#pragma unroll
    for (int ii = 0; ii < 2; ++ii) {
      const int i = 2 * grp + ii;
      const u32x4 v = *(LAS u32x4*)(lds + LDS_HALO + (wr * 4 + i) * 1024 + l * 16);
      __builtin_amdgcn_raw_buffer_store_b128(v, a2, l * 16 + (mf0 + i) * (KS2 * 1024) + ksm * 1024, 0, ST_AUX);
    }
  } else {
    const int head = 2 * pa + grp;
    bf16x8 kop[5]; u32x2 vp[5][2];
#pragma unroll
    for (int i = 0; i < 4; ++i) {
      kop[i + 1] = pack8(acc[i][2], acc[i][3]);
#pragma unroll
      for (int d = 0; d < 2; ++d) { vp[i + 1][d].x = cvt_pk(acc[i][4 + d][0], acc[i][4 + d][1]); vp[i + 1][d].y = cvt_pk(acc[i][4 + d][2], acc[i][4 + d][3]); }
    }
    const u32x4 klast = __builtin_bit_cast(u32x4, kop[4]);
    const u32x4 vlast = (u32x4){vp[4][0].x, vp[4][0].y, vp[4][1].x, vp[4][1].y};
    const __amdgpu_buffer_rsrc_t hr = __builtin_amdgcn_make_buffer_rsrc(ws + WS_HALO, 0, 8 * 32 * 2048, 0x00020000);
    const bool send = (wr == 3) && (pm < 7);
    if (send) {
      const unsigned ho = (pm * 32 + head) * 2048;
      __builtin_amdgcn_raw_buffer_store_b128(klast, hr, l * 16 + ho, 0, 16);
      __builtin_amdgcn_raw_buffer_store_b128(vlast, hr, l * 16 + ho + 1024, 0, 16);
    }
    if (wr != 3) {
      *(LAS u32x4*)(lds + LDS_HALO + w * 2048 + l * 16) = klast;
      *(LAS u32x4*)(lds + LDS_HALO + w * 2048 + 1024 + l * 16) = vlast;
    }
    bf16x8 qop[4];
#pragma unroll
    for (int i = 0; i < 4; ++i) qop[i] = pack8(acc[i][0], acc[i][1]);
#pragma unroll
    for (int i = 0; i < 4; ++i) {
      f32x4 h0, h1;
#pragma unroll
      for (int r = 0; r < 4; ++r) { h0[r] = silu_f(acc[i][6][r]) * acc[i][8][r]; h1[r] = silu_f(acc[i][7][r]) * acc[i][9][r]; }
      __builtin_amdgcn_raw_buffer_store_b128(__builtin_bit_cast(u32x4, pack8(h0, h1)), a2, l * 16 + (mf0 + i) * (KS2 * 1024) + (2 * pa + grp) * 1024, 0, ST_AUX);
    }
    WAIT_L(0); BAR();
    const int m = l & 15; const bool par = m & 1;
    const bool own0 = 4 * g + (m & 1) <= m, own1 = 4 * g + (m & 1) + 2 <= m;
    const unsigned hmask = par ? 0xffff0000u : 0x0000ffffu;
    const float scale = 0.17677669529663687f * 1.44269504089f;
    auto attend = [&](int i, bool have_prev) {
      const f32x4 z = (f32x4){0.f, 0.f, 0.f, 0.f};
      const f32x4 s0 = MFMA(kop[i + 1], qop[i], z);
      const f32x4 s1 = MFMA(kop[i], qop[i], z);
      float l0 = own0 ? (par ? s0[1] : s0[0]) : (par ? s1[1] : s1[0]);
      float l1 = own1 ? (par ? s0[3] : s0[2]) : (par ? s1[3] : s1[2]);
      l0 *= scale; l1 *= scale;
      if (!have_prev) { l0 = own0 ? l0 : -3.0e38f; l1 = own1 ? l1 : -3.0e38f; }
      const float mx = xmax16_32(fmaxf(l0, l1));
      const float e0 = __builtin_amdgcn_exp2f(l0 - mx), e1 = __builtin_amdgcn_exp2f(l1 - mx);
      const float inv = __builtin_amdgcn_rcpf(xsum16_32(e0 + e1));
      const unsigned w0 = cvt_pk(e0 * inv, e0 * inv) & hmask, w1 = cvt_pk(e1 * inv, e1 * inv) & hmask;
      const u32x4 pw = (u32x4){own0 ? 0u : w0, own1 ? 0u : w1, own0 ? w0 : 0u, own1 ? w1 : 0u};
      const bf16x8 pop = __builtin_bit_cast(bf16x8, pw);
      f32x4 o[2];
#pragma unroll
      for (int d = 0; d < 2; ++d) {
        const u32x4 vv = (u32x4){vp[i][d].x, vp[i][d].y, vp[i + 1][d].x, vp[i + 1][d].y};
        o[d] = MFMA(__builtin_bit_cast(bf16x8, vv), pop, z);
      }
      __builtin_amdgcn_raw_buffer_store_b128(__builtin_bit_cast(u32x4, pack8(o[0], o[1])), a2, l * 16 + (mf0 + i) * (KS2 * 1024) + (112 + head) * 1024, 0, ST_AUX);
    };
    attend(1, true); attend(2, true); attend(3, true);
    STAMPT(12, 0);
    WAIT_V(3);
    if (send && l == 0) flag_set(ws, WS_HLF + (pm * 32 + head) * 64, tag);
    BAR();
    if (w == 0) flag_set8(ws, WS_T1A + (pm * 16 + pa) * 8, tag, l);
    STAMPT(13, 0);
    bool have_prev = true;
    u32x4 kh, vh;
    if (wr == 0) {
      if (pm > 0) {
        flag_wait(ws, WS_HLF + ((pm - 1) * 32 + head) * 64, 1, tag, l);
        const unsigned ho = ((pm - 1) * 32 + head) * 2048;
        kh = __builtin_amdgcn_raw_buffer_load_b128(hr, l * 16 + ho, 0, 16);
        vh = __builtin_amdgcn_raw_buffer_load_b128(hr, l * 16 + ho + 1024, 0, 16);
      } else { kh = (u32x4){0u, 0u, 0u, 0u}; vh = kh; have_prev = false; }
    } else {
      kh = *(LAS u32x4*)(lds + LDS_HALO + (w - 1) * 2048 + l * 16);
      vh = *(LAS u32x4*)(lds + LDS_HALO + (w - 1) * 2048 + 1024 + l * 16);
    }
    kop[0] = __builtin_bit_cast(bf16x8, kh);
    vp[0][0] = (u32x2){vh.x, vh.y}; vp[0][1] = (u32x2){vh.z, vh.w};
    attend(0, have_prev);
  }
  STAMPT(14, 0);
  WAIT_V(0); BAR();
  if (w == 0) flag_set8(ws, WS_T1F + (pm * 32 + TYPE * 16 + pa) * 8, tag, l);
  STAMP(3);
  return tag;
}

struct K2Frag { bf16x8 a[4], b[4]; };
struct K2Rd { LAS unsigned char* lds; unsigned aoff[2], vb[2][4]; };
template <int SLOT> __device__ __forceinline__ void k2_read(const K2Rd& c, K2Frag& f) {
  constexpr int H = SLOT >> 2, O = (SLOT & 3) * K2_SLOT;
#pragma unroll
  for (int j = 0; j < 4; ++j) { LAS unsigned char* p = c.lds + c.vb[H][j] + O; f.b[j] = lds_tr8(p, p + 4096); }
#pragma unroll
  for (int i = 0; i < 4; ++i) f.a[i] = *(LAS bf16x8*)(c.lds + c.aoff[H] + O + i * 1024);
}
template <int SLOTN> __device__ __forceinline__ void k2_step(const K2Rd& c, f32x4 (&acc)[4][4], const K2Frag& cur, K2Frag& nxt) {
  k2_read<SLOTN>(c, nxt);
#pragma unroll
  for (int j = 0; j < 4; ++j)
#pragma unroll
    for (int i = 0; i < 4; ++i) acc[i][j] = MFMA(cur.b[j], cur.a[i], acc[i][j]);
#pragma unroll
  for (int k = 0; k < 6; ++k) { __builtin_amdgcn_sched_group_barrier(0x8, 1, 0); __builtin_amdgcn_sched_group_barrier(0x100, 2, 0); }
  __builtin_amdgcn_sched_group_barrier(0x8, 10, 0);
  SCHED(); __builtin_amdgcn_s_waitcnt(0xC07F); asm volatile("" ::: "memory"); BAR();
}
__device__ __forceinline__ void k2_consume(const K2Rd& c, f32x4 (&acc)[4][4], K2Frag& f0, K2Frag& f1, int iters) {
#pragma nounroll
  for (int it = 0; it < iters; ++it) {
    k2_step<1>(c, acc, f0, f1); k2_step<2>(c, acc, f1, f0); k2_step<3>(c, acc, f0, f1); k2_step<4>(c, acc, f1, f0);
    k2_step<5>(c, acc, f0, f1); k2_step<6>(c, acc, f1, f0); k2_step<7>(c, acc, f0, f1); k2_step<0>(c, acc, f1, f0);
  }
}

struct K2Ld { unsigned vsta, vstb; i32x4 sa; unsigned ldst; };
__device__ __forceinline__ void k2_stage(const K2Ld& c, i32x4 sb, unsigned ka, unsigned kb, int slot) {
  const unsigned d = slot * K2_SLOT + c.ldst;
  unsigned t0;
  asm volatile(
    "s_mov_b32 m0, %7\n\ts_add_u32 %0, %5, 0x24000\n\tbuffer_load_dwordx4 %1, %3, %5 offen sc1 lds\n\t"
    "s_add_u32 m0, %7, 0x400\n\ts_nop 0\n\tbuffer_load_dwordx4 %1, %3, %0 offen sc1 lds\n\t"
    "s_add_u32 m0, %7, 0x2000\n\ts_add_u32 %0, %6, 0x80\n\tbuffer_load_dwordx4 %2, %4, %6 offen lds\n\t"
    "s_add_u32 m0, %7, 0x2400\n\ts_nop 0\n\tbuffer_load_dwordx4 %2, %4, %0 offen lds"
    : "=&s"(t0) : "v"(c.vsta), "v"(c.vstb), "s"(c.sa), "s"(sb), "s"(ka), "s"(kb), "s"(d) : "memory", "scc");
}
__device__ __forceinline__ void k2_load(const K2Ld& c, i32x4 sb, unsigned ka, unsigned kb, int iters) {
#pragma nounroll
  for (int it = 0; it < iters; ++it) {
#pragma unroll
    for (int j = 0; j < 8; ++j) {
      k2_stage(c, sb, ka, kb, j);
      ka += 1024u; kb += 65536u;
      WAIT_V(24); BAR();
    }
  }
}
__device__ __forceinline__ bool k2_load_poll(const K2Ld& c, i32x4 sb, unsigned ka, unsigned kb, const unsigned char* fl, int n, u64 tag, int l) {
  u32x2 pv;
  const unsigned char* fp = fl + (l < n ? l : 0) * 8;
#pragma unroll
  for (int j = 0; j < 8; ++j) {
    if (j == 1) asm volatile("global_load_dwordx2 %0, %1, off sc1" : "=&v"(pv) : "v"(fp) : "memory");
    k2_stage(c, sb, ka, kb, j);
    ka += 1024u; kb += 65536u;
    WAIT_V(24);
    if (j < 7) BAR();
  }
  asm volatile("" : "+v"(pv));
  return __all((((u64)pv.y << 32) | pv.x) == tag);
}
template <int AUX> __device__ __forceinline__ void glds16x(unsigned voff, i32x4 srd, unsigned soff, unsigned ldsaddr) {
  if (AUX == 16) asm volatile("s_mov_b32 m0, %3\n\ts_nop 0\n\tbuffer_load_dwordx4 %0, %1, %2 offen sc1 lds" ::"v"(voff), "s"(srd), "s"(soff), "s"(ldsaddr) : "memory");
  else asm volatile("s_mov_b32 m0, %3\n\ts_nop 0\n\tbuffer_load_dwordx4 %0, %1, %2 offen lds" ::"v"(voff), "s"(srd), "s"(soff), "s"(ldsaddr) : "memory");
}

template <bool ISP> __device__ __forceinline__ void k2_body(LAS unsigned char* lds, const u16* x, const u16* wo, const u16* wd, u16* out, unsigned char* ws, int rt, int ct, u64 tag) {
  constexpr int NT = ISP ? K2_TP : K2_TQ;
  int tid = threadIdx.x; asm volatile("" : "+v"(tid));
  const int w = __builtin_amdgcn_readfirstlane(tid >> 6), l = tid & 63;
  const int w4 = w & 3, wm = w4 >> 1, wn = w4 & 1;
  const int g = l >> 4, q = (l & 15) >> 2, p = l & 3;
  const int tile = rt * 8 + ct;
  if (w >= 4) {
    K2Ld c;
    c.vsta = l * 16; c.vstb = k2_vstb(w4, l);
    c.sa = make_srd(ws + WS_A2, 128 * KS2 * 1024);
    const i32x4 swd = make_srd(wd, F_ * D_ * 2), swo = make_srd(wo, D_ * D_ * 2);
    const unsigned soa = (rt * 8 + 2 * w4) * (KS2 * 1024), sob = ct * 256;
    c.ldst = w4 * 2048;
    const size_t rep = (blockIdx.x & 7) * REP, t1f = WS_T1F + rep + (rt >> 1) * 256;
    if (w == 4) { if (ISP) flag_wait(ws, WS_T1A + rep + (rt >> 1) * 128, 16, tag, l); else flag_wait(ws, t1f + 128, 16, tag, l); }
    BAR();
    const unsigned ks0 = ISP ? 0u : K2_H0, ka0 = ks0 * 1024u + soa;
#pragma unroll
    for (int t = 0; t < 8; ++t) { glds16x<16>(c.vsta, c.sa, ka0 + t * 1024u, t * K2_SLOT + c.ldst); glds16x<16>(c.vsta, c.sa, ka0 + t * 1024u + 0x24000u, t * K2_SLOT + c.ldst + 1024); }
    WAIT_V(12); BAR();
    BAR();
    {
      const unsigned ka = ka0 + 8 * 1024u, kb = (ks0 + 8) * 65536u + sob;
#define K2_FIRST(J, N) do { k2_stage(c, swd, ka + (J) * 1024u, kb + (J) * 65536u, J); WAIT_V(N); BAR(); } while (0)
      K2_FIRST(0, 14); K2_FIRST(1, 16); K2_FIRST(2, 18); K2_FIRST(3, 20); K2_FIRST(4, 22); K2_FIRST(5, 24); K2_FIRST(6, 24); K2_FIRST(7, 24);
    }
    const i32x4 sx = make_srd(x, S_ * D_ * 2), sp = make_srd(ws + WS_PART, 128 * 32768);
    if (ISP) {
      static_assert(K2_TP >= 72 && K2_TP <= 96, "P schedule: 32 h + 32 o + 8..32 h");
      k2_load(c, swd, 16 * 1024u + soa, 16 * 65536u + sob, 1);
      if (!k2_load_poll(c, swd, 24 * 1024u + soa, 24 * 65536u + sob, ws + t1f, 16, tag, l)) flag_wait(ws, t1f, 16, tag, l);
      BAR();
      k2_load(c, swo, 112 * 1024u + soa, sob, 3);
      if (!k2_load_poll(c, swo, (112 + 24) * 1024u + soa, 24 * 65536u + sob, ws + t1f + 128, 5, tag, l)) flag_wait(ws, t1f + 128, 5, tag, l);
      BAR();
      k2_load(c, swd, 32 * 1024u + soa, 32 * 65536u + sob, (NT - 64) / 8 - 1);
      const bool early = k2_load_poll(c, swd, (NT - 40) * 1024u + soa, (NT - 40) * 65536u + sob, ws + WS_PTF + tile * 64, 4, tag, l);
      BAR();
      const unsigned xv = ((rt * 128 + wm * 64 + (l & 15)) * D_ + ct * 128 + wn * 64 + (((g & 1) << 1) | (g >> 1)) * 8) * 2;
#pragma unroll
      for (int k = 0; k < 2; ++k) {
#pragma unroll
        for (int e = 0; e < 4; ++e) { const int blk = k * 4 + e; glds16x<0>(xv, sx, (blk >> 1) * 16 * D_ * 2 + (blk & 1) * 64, k * 16384 + w4 * 4096 + e * 1024); }
        WAIT_V(24); BAR();
      }
      if (early) {
#pragma unroll
        for (int k = 2; k < 4; ++k) {
#pragma unroll
          for (int e = 0; e < 4; ++e) glds16x<16>(l * 16, sp, tile * 32768 + w4 * 8192 + ((k - 2) * 4 + e) * 1024, k * 16384 + w4 * 4096 + e * 1024);
          WAIT_V(24); BAR();
        }
        WAIT_V(20); BAR(); WAIT_V(16); BAR(); BAR(); WAIT_V(0); BAR();
      } else {
        WAIT_V(20); BAR(); WAIT_V(16); BAR(); WAIT_V(12); BAR();
        WAIT_V(0);
        flag_wait(ws, WS_PTF + tile * 64, 4, tag, l);
#pragma unroll
        for (int e = 0; e < 8; ++e) glds16x<16>(l * 16, sp, tile * 32768 + w4 * 8192 + e * 1024, (2 + (e >> 2)) * 16384 + w4 * 4096 + (e & 3) * 1024);
        BAR(); BAR();
        WAIT_V(0); BAR();
      }
    } else {
      k2_load(c, swd, (K2_H0 + 16) * 1024u + soa, (K2_H0 + 16) * 65536u + sob, (NT - 16) / 8);
      WAIT_V(20); BAR(); WAIT_V(16); BAR(); WAIT_V(12); BAR(); WAIT_V(8); BAR(); WAIT_V(4); BAR(); WAIT_V(0); BAR(); BAR(); BAR();
    }
    if (!ISP && blockIdx.x == 4 && w == 5) {
      gu64* p = (gu64*)(ws + WS_T1F) + l;
      for (unsigned spins = 0; spins < (1u << 18); ++spins) {
        const u64 a = __hip_atomic_load(p, __ATOMIC_RELAXED, __HIP_MEMORY_SCOPE_AGENT), b = __hip_atomic_load(p + 64, __ATOMIC_RELAXED, __HIP_MEMORY_SCOPE_AGENT);
        const u64 c2 = __hip_atomic_load(p + 128, __ATOMIC_RELAXED, __HIP_MEMORY_SCOPE_AGENT), d = __hip_atomic_load(p + 192, __ATOMIC_RELAXED, __HIP_MEMORY_SCOPE_AGENT);
        if (__all(a == tag && b == tag && c2 == tag && d == tag)) break;
        __builtin_amdgcn_s_sleep(2);
      }
      if (l == 0) flag_set(ws, WS_GEN, tag);
    }
    return;
  }
  K2Rd c; c.lds = lds;
  {
    const unsigned row = 4 * (g & 1) + q, s2 = (row >> 1) & 3;
    const unsigned lanep = 8192 + (g >> 1) * 2048 + wn * 1024 + row * 128 + (p >> 1) * 16 + (p & 1) * 8;
#pragma unroll
    for (int h = 0; h < 2; ++h) {
      c.aoff[h] = l * 16 + wm * 4096 + h * 65536;
#pragma unroll
      for (int j = 0; j < 4; ++j) c.vb[h][j] = lanep + ((j ^ s2) << 5) + h * 65536;
    }
  }
  f32x4 accf[4][4], accs[4][4];
#pragma unroll
  for (int i = 0; i < 4; ++i)
#pragma unroll
    for (int j = 0; j < 4; ++j) { accf[i][j] = (f32x4){0.f, 0.f, 0.f, 0.f}; accs[i][j] = accf[i][j]; }
  K2Frag f0, f1;
  BAR();
  BAR();
  STAMP(4);
  k2_read<0>(c, f0);
  WAIT_L(0); BAR();
  if (ISP) { k2_consume(c, accf, f0, f1, 4); k2_consume(c, accs, f0, f1, 4); k2_consume(c, accf, f0, f1, (NT - 64) / 8); }
  else k2_consume(c, accf, f0, f1, NT / 8);
  STAMP(5);
  if (!ISP) {
    const __amdgpu_buffer_rsrc_t pr = __builtin_amdgcn_make_buffer_rsrc(ws + WS_PART, 0, 128 * 32768, 0x00020000);
    const unsigned po = tile * 32768 + w4 * 8192 + l * 16;
#pragma unroll
    for (int i = 0; i < 4; ++i)
#pragma unroll
      for (int jp = 0; jp < 2; ++jp) __builtin_amdgcn_raw_buffer_store_b128(__builtin_bit_cast(u32x4, pack8(accf[i][2 * jp], accf[i][2 * jp + 1])), pr, po + (i * 2 + jp) * 1024, 0, 16);
    WAIT_V(0);
    if (l == 0) flag_set(ws, WS_PTF + tile * 64 + w4 * 8, tag);
  } else {
    const int m = l & 15;
    const __amdgpu_buffer_rsrc_t orr = __builtin_amdgcn_make_buffer_rsrc(out, 0, S_ * D_ * 2, 0x00020000);
    const unsigned ov = ((rt * 128 + wm * 64 + m) * D_ + ct * 128 + wn * 64 + (((g & 1) << 1) | (g >> 1)) * 8) * 2;
    u32x4 pt[8], xr[8];
#pragma unroll
    for (int b = 0; b < 8; ++b) {
      pt[b] = *(LAS u32x4*)(lds + (2 + (b >> 2)) * 16384 + w4 * 4096 + (b & 3) * 1024 + l * 16);
      xr[b] = *(LAS u32x4*)(lds + (b >> 2) * 16384 + w4 * 4096 + (b & 3) * 1024 + l * 16);
    }
#pragma unroll
    for (int i = 0; i < 4; ++i)
#pragma unroll
      for (int jp = 0; jp < 2; ++jp) {
        const int b = i * 2 + jp;
        unsigned y[2][2];
#pragma unroll
        for (int jj = 0; jj < 2; ++jj) {
          const int j = 2 * jp + jj;
          const f32x4 f = accf[i][j] + (f32x4){bf_lo(pt[b][2 * jj]), bf_hi(pt[b][2 * jj]), bf_lo(pt[b][2 * jj + 1]), bf_hi(pt[b][2 * jj + 1])};
          float v[4];
#pragma unroll
          for (int r = 0; r < 4; ++r) v[r] = accs[i][j][r] * silu_f(f[r]);
          y[jj][0] = cvt_pk(v[0], v[1]); y[jj][1] = cvt_pk(v[2], v[3]);
        }
        const auto s0 = __builtin_amdgcn_permlane16_swap(y[0][0], y[1][0], false, false);
        const auto s1 = __builtin_amdgcn_permlane16_swap(y[0][1], y[1][1], false, false);
        const unsigned yy[4] = {s0[0], s1[0], s0[1], s1[1]};
        u32x4 o;
#pragma unroll
        for (int r = 0; r < 4; ++r) o[r] = cvt_pk(bf_lo(xr[b][r]) + bf_lo(yy[r]), bf_hi(xr[b][r]) + bf_hi(yy[r]));
        __builtin_amdgcn_raw_buffer_store_b128(o, orr, ov + i * 16 * D_ * 2 + jp * 64, 0, OUT_AUX);
      }
  }
  STAMP(6);
}

__global__ __launch_bounds__(512, 2) void fused_block(const u16* __restrict__ x, const u16* __restrict__ wqkv, const u16* __restrict__ wo, const u16* __restrict__ wg,
                                                      const u16* __restrict__ wu, const u16* __restrict__ wd, u16* __restrict__ out, unsigned char* __restrict__ ws) {
  extern __shared__ __attribute__((aligned(16))) unsigned char shm[];
  LAS unsigned char* lds = (LAS unsigned char*)shm;
  const int b = blockIdx.x, xcd = b & 7, idx = b >> 3;
  const int pm = idx & 7, pn = idx >> 3, pa = xcd * 2 + (pn >> 1);
  const int lnmf = idx < 16 ? idx * 8 + xcd : -1;
  const int pr = xcd & 3, rt = (pr & 1) * 8 + (idx & 7), ct = (pr >> 1) * 4 + (idx >> 3);
  const bool isp = xcd < 4;
  u64 tag;
  if (pn & 1) tag = k1_body<1>(lds, x, wqkv, wg, wu, wo, wd, ws, pm, pa, lnmf, isp, ct);
  else tag = k1_body<0>(lds, x, wqkv, wg, wu, wo, wd, ws, pm, pa, lnmf, isp, ct);
  if (isp) k2_body<true>(lds, x, wo, wd, out, ws, rt, ct, tag);
  else k2_body<false>(lds, x, wo, wd, out, ws, rt, ct, tag);
}

extern "C" void kernel_launch(void* const* d_in, const int* in_sizes, int n_in, void* d_out, int out_size, void* d_ws, size_t ws_size, hipStream_t stream) {
  const u16* x = (const u16*)d_in[0]; const u16* wqkv = (const u16*)d_in[1]; const u16* wo = (const u16*)d_in[2];
  const u16* wg = (const u16*)d_in[4]; const u16* wu = (const u16*)d_in[5]; const u16* wd = (const u16*)d_in[6];
  (void)hipFuncSetAttribute((const void*)fused_block, hipFuncAttributeMaxDynamicSharedMemorySize, LDS_TOTAL);
  fused_block<<<256, 512, LDS_TOTAL, stream>>>(x, wqkv, wo, wg, wu, wd, (u16*)d_out, (unsigned char*)d_ws);
}
```

```cpp
#include <hip/hip_runtime.h>
#include <stdint.h>
#include <type_traits>

#define LAS __attribute__((address_space(3)))
#define GAS __attribute__((address_space(1)))
typedef __bf16 bf16x8 __attribute__((ext_vector_type(8)));
typedef short s16x4 __attribute__((ext_vector_type(4)));
typedef short s16x8 __attribute__((ext_vector_type(8)));
typedef float f32x4 __attribute__((ext_vector_type(4)));
typedef float f32x2 __attribute__((ext_vector_type(2)));
typedef unsigned u32x4 __attribute__((ext_vector_type(4)));
typedef unsigned u32x2 __attribute__((ext_vector_type(2)));
typedef int i32x4 __attribute__((ext_vector_type(4)));
typedef unsigned short u16;

constexpr int S_ = 2048, D_ = 1024, F_ = 3584;
constexpr int KS2 = 144;
constexpr size_t WS_GEN = 0;
constexpr size_t REP = 4096;
constexpr size_t WS_LNF = 4096;
constexpr size_t WS_T1A = WS_LNF + 1024;
constexpr size_t WS_T1F = WS_LNF + 2048;
constexpr size_t WS_HLF = 4096 + 8 * REP;
constexpr size_t WS_PTF = WS_HLF + 16384;
static_assert(WS_PTF + 8192 <= 65536, "flag area");
constexpr size_t WS_XN = 65536;
constexpr size_t WS_A2 = WS_XN + 4194304;
constexpr size_t WS_HALO = WS_A2 + (size_t)128 * KS2 * 1024;
constexpr size_t WS_PART = WS_HALO + 8 * 32 * 2048;
constexpr size_t WS_STAMP = WS_PART + (size_t)128 * 65536;

#define P_STEPS 80
#define OUT_AUX 16
#define ST_AUX 16

__device__ __forceinline__ i32x4 make_srd(const void* p, unsigned bytes) {
  const unsigned long long a = (unsigned long long)p;
  i32x4 r; r.x = (int)(unsigned)a; r.y = (int)((unsigned)(a >> 32) & 0xffffu); r.z = (int)bytes; r.w = 0x00020000; return r;
}
__device__ __forceinline__ void glds16(unsigned voff, i32x4 srd, unsigned soff, unsigned ldsaddr) {
  asm volatile("s_mov_b32 m0, %3\n\ts_nop 0\n\tbuffer_load_dwordx4 %0, %1, %2 offen lds" ::"v"(voff), "s"(srd), "s"(soff), "s"(ldsaddr) : "memory");
}
#define WAIT_V(n) asm volatile("s_waitcnt vmcnt(" #n ")" ::: "memory")
#define WAIT_L(n) asm volatile("s_waitcnt lgkmcnt(" #n ")" ::: "memory")
#define SCHED() __builtin_amdgcn_sched_barrier(0)
#define BAR() do { SCHED(); __builtin_amdgcn_s_barrier(); SCHED(); } while (0)

typedef __bf16 bf16x2 __attribute__((ext_vector_type(2)));
__device__ __forceinline__ unsigned cvt_pk(float lo, float hi) { const f32x2 v = {lo, hi}; return __builtin_bit_cast(unsigned, __builtin_convertvector(v, bf16x2)); }
__device__ __forceinline__ float bf_lo(unsigned u) { return __uint_as_float(u << 16); }
__device__ __forceinline__ float bf_hi(unsigned u) { return __uint_as_float(u & 0xffff0000u); }
__device__ __forceinline__ float rbf(float f) { return bf_lo(cvt_pk(f, f)); }
__device__ __forceinline__ bf16x8 pack8(const f32x4& a, const f32x4& b) {
  u32x4 w; w.x = cvt_pk(a[0], a[1]); w.y = cvt_pk(a[2], a[3]); w.z = cvt_pk(b[0], b[1]); w.w = cvt_pk(b[2], b[3]);
  return __builtin_bit_cast(bf16x8, w);
}
__device__ __forceinline__ bf16x8 lds_tr8(LAS unsigned char* p0, LAS unsigned char* p1) {
  s16x4 a = __builtin_amdgcn_ds_read_tr16_b64_v4i16((LAS s16x4*)p0);
  s16x4 b = __builtin_amdgcn_ds_read_tr16_b64_v4i16((LAS s16x4*)p1);
  s16x8 c = __builtin_shufflevector(a, b, 0, 1, 2, 3, 4, 5, 6, 7);
  return __builtin_bit_cast(bf16x8, c);
}
__device__ __forceinline__ float silu_f(float g) { return g * __builtin_amdgcn_rcpf(1.0f + __builtin_amdgcn_exp2f(g * -1.44269504089f)); }
#define MFMA(a, b, c) __builtin_amdgcn_mfma_f32_16x16x32_bf16(a, b, c, 0, 0, 0)

typedef unsigned long long u64;
typedef GAS u64 gu64;
__device__ __forceinline__ void flag_set(unsigned char* ws, size_t off, u64 tag) { __hip_atomic_store((gu64*)(ws + off), tag, __ATOMIC_RELAXED, __HIP_MEMORY_SCOPE_AGENT); }
__device__ __forceinline__ void flag_set8(unsigned char* ws, size_t off, u64 tag, int l) { if (l < 8) flag_set(ws, off + l * REP, tag); }
__device__ __forceinline__ void flag_wait(unsigned char* ws, size_t off, int n, u64 tag, int l) {
  gu64* p = (gu64*)(ws + off) + (l < n ? l : 0);
  for (unsigned spins = 0; spins < (1u << 18); ++spins) {
    const u64 v = __hip_atomic_load(p, __ATOMIC_RELAXED, __HIP_MEMORY_SCOPE_AGENT);
    if (__all(v == tag)) break;
    __builtin_amdgcn_s_sleep(2);
  }
  asm volatile("" ::: "memory");
}

#define STAMP(k) do {} while (0)
#define STAMPT(k, T) do {} while (0)

__device__ __forceinline__ float xsum16_32(float v) {
  const auto a = __builtin_amdgcn_permlane16_swap(__float_as_uint(v), __float_as_uint(v), false, false);
  v = __uint_as_float(a[0]) + __uint_as_float(a[1]);
  const auto b = __builtin_amdgcn_permlane32_swap(__float_as_uint(v), __float_as_uint(v), false, false);
  return __uint_as_float(b[0]) + __uint_as_float(b[1]);
}
__device__ __forceinline__ float xmax16_32(float v) {
  const auto a = __builtin_amdgcn_permlane16_swap(__float_as_uint(v), __float_as_uint(v), false, false);
  v = fmaxf(__uint_as_float(a[0]), __uint_as_float(a[1]));
  const auto b = __builtin_amdgcn_permlane32_swap(__float_as_uint(v), __float_as_uint(v), false, false);
  return fmaxf(__uint_as_float(b[0]), __uint_as_float(b[1]));
}
#define PF_STRIDE 128
constexpr int K1_ASLOT = 16384, K1_BSLOT = 20480, K1_BOFF = 4 * K1_ASLOT, K1_LDS = K1_BOFF + 4 * K1_BSLOT;
constexpr int LDS_PFDUMP = K1_LDS, LDS_LNRED = K1_LDS + 256, LDS_TOTAL = K1_LDS + 256 + 1024, LDS_HALO = 131072;

struct K1Ctx {
  LAS unsigned char* lds;
  unsigned aoff;
  unsigned vb[6];
  unsigned vst0, vst1;
  i32x4 s0, s1, s2;
  unsigned so0, so1;
  unsigned ldst;
};

template <int TYPE> __device__ __forceinline__ void k1_stage_b(const K1Ctx& c, int s, int slot, unsigned cnt, int lim) {
  const unsigned d = K1_BOFF + slot * K1_BSLOT + c.ldst;
  unsigned t0, t1;
  if (TYPE == 0) {
    const unsigned kq = (unsigned)s * (32u * 6144u) + c.so0, kg = (unsigned)s * (32u * 7168u) + c.so0;
    asm volatile(
      "s_cmp_ge_u32 %10, %11\n\ts_cbranch_scc1 .Lsk%=\n\t"
      "s_mov_b32 m0, %9\n\ts_add_u32 %0, %7, 0x800\n\tbuffer_load_dwordx4 %2, %4, %7 offen lds\n\t"
      "s_add_u32 m0, %9, 0x400\n\ts_add_u32 %1, %7, 0x1000\n\tbuffer_load_dwordx4 %2, %4, %0 offen lds\n\t"
      "s_add_u32 m0, %9, 0x800\n\ts_nop 0\n\tbuffer_load_dwordx4 %2, %4, %1 offen lds\n\t"
      "s_add_u32 m0, %9, 0xc00\n\ts_nop 0\n\tbuffer_load_dwordx4 %3, %5, %8 offen lds\n\t"
      "s_add_u32 m0, %9, 0x1000\n\ts_nop 0\n\tbuffer_load_dwordx4 %3, %6, %8 offen lds\n"
      ".Lsk%=:"
      : "=&s"(t0), "=&s"(t1)
      : "v"(c.vst0), "v"(c.vst1), "s"(c.s0), "s"(c.s1), "s"(c.s2), "s"(kq), "s"(kg), "s"(d), "s"(cnt), "i"(lim) : "memory", "scc");
  } else {
    const unsigned k0 = (unsigned)s * (32u * 7168u) + c.so0, k1 = (unsigned)s * (32u * 7168u) + c.so1;
    asm volatile(
      "s_cmp_ge_u32 %10, %11\n\ts_cbranch_scc1 .Lsk%=\n\t"
      "s_mov_b32 m0, %9\n\ts_add_u32 %0, %7, 0x80\n\tbuffer_load_dwordx4 %2, %4, %7 offen lds\n\t"
      "s_add_u32 m0, %9, 0x400\n\ts_nop 0\n\tbuffer_load_dwordx4 %2, %4, %0 offen lds\n\t"
      "s_add_u32 m0, %9, 0x800\n\ts_nop 0\n\tbuffer_load_dwordx4 %2, %5, %7 offen lds\n\t"
      "s_add_u32 m0, %9, 0xc00\n\ts_nop 0\n\tbuffer_load_dwordx4 %2, %5, %0 offen lds\n\t"
      "s_add_u32 m0, %9, 0x1000\n\ts_nop 0\n\tbuffer_load_dwordx4 %3, %6, %8 offen lds\n"
      ".Lsk%=:"
      : "=&s"(t0), "=&s"(t1)
      : "v"(c.vst0), "v"(c.vst1), "s"(c.s0), "s"(c.s1), "s"(c.s2), "s"(k0), "s"(k1), "s"(d), "s"(cnt), "i"(lim) : "memory", "scc");
  }
}
__device__ __forceinline__ void k1_stage_a(const K1Ctx& c, int s, int slot, unsigned cnt, int lim) {
  const unsigned d = slot * K1_ASLOT + c.ldst;
  const unsigned k = (unsigned)s * 1024u + c.so0;
  unsigned t0;
  asm volatile(
    "s_cmp_ge_u32 %5, %6\n\ts_cbranch_scc1 .Lsk%=\n\t"
    "s_mov_b32 m0, %4\n\ts_add_u32 %0, %3, 0x8000\n\tbuffer_load_dwordx4 %1, %2, %3 offen sc1 lds\n\t"
    "s_add_u32 m0, %4, 0x400\n\ts_nop 0\n\tbuffer_load_dwordx4 %1, %2, %0 offen sc1 lds\n\t"
    "s_add_u32 m0, %4, 0x800\n\ts_add_u32 %0, %3, 0x10000\n\tbuffer_load_dwordx4 %1, %2, %0 offen sc1 lds\n\t"
    "s_add_u32 m0, %4, 0xc00\n\ts_add_u32 %0, %3, 0x18000\n\tbuffer_load_dwordx4 %1, %2, %0 offen sc1 lds\n"
    ".Lsk%=:"
    : "=&s"(t0) : "v"(c.vst0), "s"(c.s0), "s"(k), "s"(d), "s"(cnt), "i"(lim) : "memory", "scc");
}
#define WAIT_V2(STEADY, TAIL, cnt, lim) do { asm volatile("s_cmp_lt_u32 %0, %1\n\ts_cbranch_scc1 .Lw%=\n\ts_waitcnt vmcnt(" #TAIL ")\n.Lw%=:\n\ts_waitcnt vmcnt(" #STEADY ")" :: "s"(cnt), "i"(lim) : "memory", "scc"); } while (0)
#define BAR_IF(cnt, lim) do { SCHED(); asm volatile("s_cmp_ge_u32 %0, %1\n\ts_cbranch_scc1 .Lb%=\n\ts_barrier\n.Lb%=:" :: "s"(cnt), "i"(lim) : "memory", "scc"); SCHED(); } while (0)
template <int TYPE> __device__ __forceinline__ void k1_read(const K1Ctx& c, int slot, bf16x8 (&af)[4], bf16x8 (&bfr)[10]) {
  LAS unsigned char* lds = c.lds;
  const int bs = slot * K1_BSLOT;
  if (TYPE == 0) {
#pragma unroll
    for (int j = 0; j < 8; ++j) { LAS unsigned char* p = lds + c.vb[j & 1] + bs + (j >> 1) * 1024; bfr[j] = lds_tr8(p, p + 512); }
#pragma unroll
    for (int j = 0; j < 2; ++j) { LAS unsigned char* p = lds + c.vb[2 + j] + bs; bfr[8 + j] = lds_tr8(p, p + 512); }
  } else {
#pragma unroll
    for (int j = 0; j < 8; ++j) { LAS unsigned char* p = lds + c.vb[j & 3] + bs + (j >> 2) * 2048; bfr[j] = lds_tr8(p, p + 512); }
#pragma unroll
    for (int j = 0; j < 2; ++j) { LAS unsigned char* p = lds + c.vb[4 + j] + bs; bfr[8 + j] = lds_tr8(p, p + 256); }
  }
#pragma unroll
  for (int i = 0; i < 4; ++i) af[i] = *(LAS bf16x8*)(lds + c.aoff + slot * K1_ASLOT + i * 1024);
}
#define K1V 0
#define K1_EARLY 0
template <int TYPE, int N0, int N1> __device__ __forceinline__ void k1_mma(f32x4 (&acc)[4][10], const bf16x8 (&af)[4], const bf16x8 (&bfr)[10]) {
#pragma unroll
  for (int n = N0; n < N1; ++n) {
    const int j = n >> 2, i = n & 3;
    if (TYPE == 0 && (j == 4 || j == 5)) acc[i][j] = MFMA(af[i], bfr[j], acc[i][j]);
    else acc[i][j] = MFMA(bfr[j], af[i], acc[i][j]);
  }
}
__device__ __forceinline__ void k1_pf(unsigned voff, i32x4 srd, unsigned soff, unsigned on, unsigned cnt, int lim) {
  asm volatile("s_cmp_lg_u32 %3, 4\n\ts_cbranch_scc1 .Lpf%=\n\ts_cmp_ge_u32 %4, %5\n\ts_cbranch_scc1 .Lpf%=\n\t"
               "s_mov_b32 m0, %6\n\ts_nop 0\n\tbuffer_load_dword %0, %1, %2 offen lds\n.Lpf%=:"
               ::"v"(voff), "s"(srd), "s"(soff), "s"(on), "s"(cnt), "i"(lim), "i"(LDS_PFDUMP) : "memory", "scc");
}
#define WAIT_A(PFN, PFT, PLN, PLT, pf, cnt, lim) do { asm volatile( \
    "s_cmp_lt_u32 %1, %2\n\ts_cbranch_scc1 .Ls%=\n\t" \
    "s_cmp_eq_u32 %0, 4\n\ts_cbranch_scc1 .Lt%=\n\ts_waitcnt vmcnt(" #PLT ")\n.Lt%=:\n\ts_waitcnt vmcnt(" #PFT ")\n" \
    ".Ls%=:\n\ts_cmp_eq_u32 %0, 4\n\ts_cbranch_scc1 .Lu%=\n\ts_waitcnt vmcnt(" #PLN ")\n.Lu%=:\n\ts_waitcnt vmcnt(" #PFN ")" \
    :: "s"(pf), "s"(cnt), "i"(lim) : "memory", "scc"); } while (0)

constexpr int K2_SLOT = 16384;
constexpr int K2_TP = P_STEPS, K2_TQ = KS2 - P_STEPS;
static_assert(K2_TP % 8 == 0 && K2_TQ % 8 == 0 && K2_TP >= 48, "step counts must be multiples of 8");
constexpr unsigned K2_H0 = K2_TP - 32;
__device__ __forceinline__ unsigned k2_vstb(int w4, int l) { const unsigned r8 = l >> 3, ch = (l & 7) ^ (((r8 >> 1) & 3) << 1); return (8 * w4 + r8) * 2048 + ch * 16; }
__device__ __forceinline__ void k2_prologue_b(int w4, int l, const u16* wd, bool isp, int ct) {
  const i32x4 sb = make_srd(wd, F_ * D_ * 2);
  const unsigned vb = k2_vstb(w4, l), k0 = (isp ? 0u : K2_H0) * 65536u + ct * 256;
#pragma unroll
  for (int t = 0; t < 8; ++t) { glds16(vb, sb, k0 + t * 65536u, t * K2_SLOT + 8192 + w4 * 2048); glds16(vb, sb, k0 + t * 65536u + 128u, t * K2_SLOT + 8192 + w4 * 2048 + 1024); }
}

template <int TYPE> __device__ __forceinline__ u64 k1_body(LAS unsigned char* lds, const u16* x, const u16* wqkv, const u16* wg, const u16* wu, const u16* wo, const u16* wd, unsigned char* ws, int pm, int pa  , int lnmf, bool isp, int ct) {
  const int tid = threadIdx.x;
  const int w = __builtin_amdgcn_readfirstlane(tid >> 6), l = tid & 63;
  const int grp = w >> 2, wr = w & 3;
  const int g = l >> 4, q = (l & 15) >> 2, p = l & 3;
  STAMP(0);
  K1Ctx c; c.lds = lds;
  c.aoff = l * 16 + wr * 4096;
  {
    const unsigned s2 = ((q >> 1) & 1) | ((g & 1) << 1);
    const unsigned lanep = K1_BOFF + g * 5120 + q * 128 + (p >> 1) * 16 + (p & 1) * 8;
    if (TYPE == 0) {
      c.vb[0] = lanep + (((2 * grp) ^ s2) << 5); c.vb[1] = lanep + (((2 * grp + 1) ^ s2) << 5);
      c.vb[2] = c.vb[0] + 4096; c.vb[3] = c.vb[1] + 4096; c.vb[4] = 0; c.vb[5] = 0;
    } else {
#pragma unroll
      for (int j = 0; j < 4; ++j) c.vb[j] = lanep + ((j ^ s2) << 5) + grp * 1024;
      const unsigned mx = K1_BOFF + (2 * (g >> 1) * 5 + 4) * 1024 + (g & 1) * 512 + q * 64 + (((2 * grp + (p >> 1)) ^ ((g & 1) << 1)) << 4) + (p & 1) * 8;
      c.vb[4] = mx; c.vb[5] = mx + 5120;
    }
  }
  const int c0 = 1024 + 160 * pa;
  const int odd = pa & 1;
  if (grp == 0) {
    const unsigned r8 = l >> 3, sw = (((r8 >> 1) & 1) | ((wr & 1) << 1)) << 1, ch = (l & 7) ^ sw;
    c.ldst = wr * 5120;
    if (TYPE == 0) {
      c.vst0 = (8 * wr + r8) * 6144 + ch * 16; c.vst1 = (8 * wr + r8) * 7168 + ch * 16;
      c.s0 = make_srd(wqkv, D_ * 3 * D_ * 2); c.s1 = make_srd(wg, D_ * F_ * 2); c.s2 = make_srd(wu, D_ * F_ * 2);
      c.so0 = pa * 128; c.so1 = 0;
    } else {
      c.vst0 = (8 * wr + r8) * 7168 + ch * 16;
      const unsigned r16 = l >> 2, chm = (l & 3) ^ (((r16 >> 3) & 1) << 1);
      c.vst1 = (16 * (wr >> 1) + r16) * 7168 + chm * 16;
      c.s0 = make_srd(wg, D_ * F_ * 2); c.s1 = make_srd(wu, D_ * F_ * 2); c.s2 = make_srd((wr & 1) ? wu : wg, D_ * F_ * 2);
      c.so0 = (c0 + (odd ? 32 : 0)) * 2; c.so1 = (c0 + (odd ? 0 : 128)) * 2;
    }
  } else {
    c.vst0 = l * 16; c.vst1 = 0;
    c.s0 = make_srd(ws + WS_XN, S_ * D_ * 2); c.s1 = c.s0; c.s2 = c.s0;
    c.so0 = (pm * 16 + 4 * wr) * 32768; c.so1 = 0;
    c.ldst = wr * 4096;
  }

  u32x4 xv[4]; float piv = 0.f;
  if (lnmf >= 0) {
    const u16* row = x + (size_t)(lnmf * 16 + (l & 15)) * D_;
#pragma unroll
    for (int i = 0; i < 4; ++i) xv[i] = *(const u32x4*)(row + w * 128 + g * 8 + i * 32);
    piv = __uint_as_float((unsigned)row[0] << 16);
  }
  unsigned glo, ghi;
  if (grp == 0 && lnmf < 0) {
    u32x2 gen;
    asm volatile("global_load_dwordx2 %0, %1, off sc1" : "=&v"(gen) : "v"(ws + WS_GEN) : "memory");
    k1_stage_b<TYPE>(c, 0, 0, 0, 1); k1_stage_b<TYPE>(c, 1, 1, 0, 1); k1_stage_b<TYPE>(c, 2, 2, 0, 1);
    asm volatile("s_waitcnt vmcnt(15)" : "+v"(gen) :: "memory");
    glo = __builtin_amdgcn_readfirstlane(gen.x); ghi = __builtin_amdgcn_readfirstlane(gen.y);
  } else {
    u32x2 gen;
    asm volatile("global_load_dwordx2 %0, %1, off sc1\n\ts_waitcnt vmcnt(0)" : "=&v"(gen) : "v"(ws + WS_GEN) : "memory");
    glo = __builtin_amdgcn_readfirstlane(gen.x); ghi = __builtin_amdgcn_readfirstlane(gen.y);
  }
  const u64 tag = (((u64)ghi << 32) | glo) + 1;

  if (lnmf >= 0) {
    const int m = l & 15;
    const u32x4 (&v)[4] = xv;
    float f[32]; float sm = 0.f, sq = 0.f;
#pragma unroll
    for (int i = 0; i < 4; ++i)
#pragma unroll
      for (int j = 0; j < 4; ++j) {
        const float a = bf_lo(v[i][j]), b = bf_hi(v[i][j]); f[i * 8 + 2 * j] = a; f[i * 8 + 2 * j + 1] = b;
        const float da = a - piv, db = b - piv; sm += da + db; sq += da * da + db * db;
      }
    sm = xsum16_32(sm); sq = xsum16_32(sq);
    STAMPT(8, 0);
    LAS f32x2* red = (LAS f32x2*)(lds + LDS_LNRED);
    if (g == 0) red[w * 16 + m] = (f32x2){sm, sq};
    WAIT_L(0); BAR();
    float st = 0.f, qt = 0.f;
#pragma unroll
    for (int i = 0; i < 8; ++i) { const f32x2 r = red[i * 16 + m]; st += r.x; qt += r.y; }
    const float md = st * (1.0f / D_), mean = piv + md, var = fmaxf(qt * (1.0f / D_) - md * md, 0.f);
    const float mb = rbf(mean);
    const float inv = 1.0f / rbf(sqrtf(rbf(rbf(var) + 1e-5f)));
    const __amdgpu_buffer_rsrc_t xr = __builtin_amdgcn_make_buffer_rsrc(ws + WS_XN, 0, S_ * D_ * 2, 0x00020000);
#pragma unroll
    for (int i = 0; i < 4; ++i) {
      u32x4 o;
#pragma unroll
      for (int j = 0; j < 4; ++j) o[j] = cvt_pk(rbf(f[i * 8 + 2 * j] - mb) * inv, rbf(f[i * 8 + 2 * j + 1] - mb) * inv);
      __builtin_amdgcn_raw_buffer_store_b128(o, xr, (lnmf * 32 + w * 4 + i) * 1024 + l * 16, 0, 16);
    }
    STAMPT(9, 0);
    WAIT_V(0); BAR();
    if (w == 0) flag_set8(ws, WS_LNF + lnmf * 8, tag, l);
    STAMPT(10, 0);
    if (grp == 0) { k1_stage_b<TYPE>(c, 0, 0, 0, 1); k1_stage_b<TYPE>(c, 1, 1, 0, 1); k1_stage_b<TYPE>(c, 2, 2, 0, 1); }
  }
  if (w == 4) flag_wait(ws, WS_LNF + (blockIdx.x & 7) * REP + pm * 128, 16, tag, l);
  BAR();

  f32x4 acc[4][10];
#pragma unroll
  for (int i = 0; i < 4; ++i)
#pragma unroll
    for (int j = 0; j < 10; ++j) acc[i][j] = (f32x4){0.f, 0.f, 0.f, 0.f};
  bf16x8 af[4], bfr[10];

  if (grp == 0) {
    WAIT_V(10); BAR();
    STAMP(1);
#define K1_G0_STEP(J, TAILV) do { \
      k1_read<TYPE>(c, J, af, bfr); SCHED(); \
      k1_stage_b<TYPE>(c, s + (J) + 3, ((J) + 3) & 3, s, (J) == 0 ? 32 : 28); \
      WAIT_L(0); BAR(); \
      __builtin_amdgcn_s_setprio(1); \
      k1_mma<TYPE, 0, 40 - K1_EARLY>(acc, af, bfr); \
      if (K1V & 1) __builtin_amdgcn_s_setprio(0); \
      SCHED(); WAIT_V2(10, TAILV, s, 28); \
      BAR(); \
      if (K1_EARLY) __builtin_amdgcn_s_setprio(3); \
      k1_mma<TYPE, 40 - K1_EARLY, 40>(acc, af, bfr); \
      __builtin_amdgcn_s_setprio(0); SCHED(); } while (0)
#pragma nounroll
    for (unsigned s = 0; s < 32; s += 4) {
      K1_G0_STEP(0, 10); K1_G0_STEP(1, 5); K1_G0_STEP(2, 0); K1_G0_STEP(3, 0);
    }
  } else {
#define PF_ON 1
#define K1_PF_ROW0 96
    const unsigned pf = PF_ON ? w : 99  , pfv = (blockIdx.x * 64 + l) * PF_STRIDE;
    const i32x4 sg = make_srd(wg, D_ * F_ * 2), su = make_srd(wu, D_ * F_ * 2), sq = make_srd(wqkv, D_ * 3 * D_ * 2), sd = make_srd(wd, F_ * D_ * 2), so = make_srd(wo, D_ * D_ * 2);
    constexpr unsigned OOR = 0x7f000000u, MB = 16384u * PF_STRIDE;
    constexpr unsigned NG = ((1024 - K1_PF_ROW0) * 7168u + MB - 1) / MB, NQ = ((1024 - K1_PF_ROW0) * 6144u + MB - 1) / MB, ND = (F_ * D_ * 2 + MB - 1) / MB, NO = (D_ * D_ * 2 + MB - 1) / MB;
    static_assert(NG + (ND + 1) / 2 <= 8 && NG + ND / 2 <= 7 && NQ + NO <= 7, "prefetch schedule does not fit");
    STAMPT(11, 256);
    if (!(K1V & 2)) k1_pf(pfv, sq, OOR, pf, 0, 1);
    k1_stage_a(c, 0, 0, 0, 1);
    if (!(K1V & 2)) k1_pf(pfv, sq, OOR, pf, 0, 1);
    k1_stage_a(c, 1, 1, 0, 1);
    k1_stage_a(c, 2, 2, 0, 1);
    if (K1V & 2) WAIT_V(8); else WAIT_A(9, 9, 8, 8, pf, 0u, 1);
    BAR();
    BAR();
#define K1_G1_STEP(J, PFN, PFT, PLT) do { \
      k1_read<TYPE>(c, J, af, bfr); SCHED(); \
      if (!(K1V & 2) && (J) == 0) k1_pf(pfv, it < NG ? sg : sd, it < NG ? K1_PF_ROW0 * 7168u + it * MB : (it - NG) * MB, pf, s, 32); \
      if (!(K1V & 2) && (J) == 1) k1_pf(pfv, it < NG ? su : sd, it < NG ? K1_PF_ROW0 * 7168u + it * MB : (it - NG + (ND + 1) / 2) * MB, pf, s, 28); \
      if (!(K1V & 2) && (J) == 2) k1_pf(pfv, it < NQ ? sq : so, it < NQ ? K1_PF_ROW0 * 6144u + it * MB : (it - NQ) * MB, pf, s, 28); \
      k1_stage_a(c, s + (J) + 3, ((J) + 3) & 3, s, (J) == 0 ? 32 : 28); \
      WAIT_L(0); if (K1V & 2) WAIT_V2(8, PLT, s, 28); else WAIT_A(PFN, PFT, 8, PLT, pf, s, 28); \
      BAR(); \
      __builtin_amdgcn_s_setprio(1); \
      k1_mma<TYPE, 0, 40 - K1_EARLY>(acc, af, bfr); \
      if (K1V & 1) __builtin_amdgcn_s_setprio(0); \
      if ((J) < 3) BAR(); else BAR_IF(s, 28); \
      if (K1_EARLY) __builtin_amdgcn_s_setprio(3); \
      k1_mma<TYPE, 40 - K1_EARLY, 40>(acc, af, bfr); \
      __builtin_amdgcn_s_setprio(0); SCHED(); } while (0)
#pragma nounroll
    for (unsigned s = 0; s < 32; s += 4) {
      const unsigned it = s >> 2;
      K1_G1_STEP(0, 9, 9, 8); K1_G1_STEP(1, 10, 5, 4); K1_G1_STEP(2, 10, 0, 0); K1_G1_STEP(3, 9, 0, 0);
    }
  }
  STAMP(2);

  const __amdgpu_buffer_rsrc_t a2 = __builtin_amdgcn_make_buffer_rsrc(ws + WS_A2, 0, 128 * KS2 * 1024, 0x00020000);
  const unsigned mf0 = pm * 16 + 4 * wr;
  if (TYPE == 1) {
    const unsigned ksf = c0 / 32 + (odd ? 1 : 0) + 2 * grp, ksm = c0 / 32 + (odd ? 0 : 4);
#pragma unroll
    for (int i = 0; i < 4; ++i) {
      const unsigned base = (mf0 + i) * (KS2 * 1024);
#pragma unroll
      for (int t = 0; t < 2; ++t) {
        f32x4 h0, h1;
#pragma unroll
        for (int r = 0; r < 4; ++r) { h0[r] = silu_f(acc[i][2 * t][r]) * acc[i][2 * t + 4][r]; h1[r] = silu_f(acc[i][2 * t + 1][r]) * acc[i][2 * t + 5][r]; }
        __builtin_amdgcn_raw_buffer_store_b128(__builtin_bit_cast(u32x4, pack8(h0, h1)), a2, l * 16 + base + (ksf + t) * 1024, 0, ST_AUX);
      }
      f32x4 h0;
#pragma unroll
      for (int r = 0; r < 4; ++r) h0[r] = silu_f(acc[i][8][r]) * acc[i][9][r];
      u32x2 o; o.x = cvt_pk(h0[0], h0[1]); o.y = cvt_pk(h0[2], h0[3]);
      *(LAS u32x2*)(lds + LDS_HALO + (wr * 4 + i) * 1024 + l * 16 + grp * 8) = o;
    }
    WAIT_L(0); BAR();
#pragma unroll
    for (int ii = 0; ii < 2; ++ii) {
      const int i = 2 * grp + ii;
      const u32x4 v = *(LAS u32x4*)(lds + LDS_HALO + (wr * 4 + i) * 1024 + l * 16);
      __builtin_amdgcn_raw_buffer_store_b128(v, a2, l * 16 + (mf0 + i) * (KS2 * 1024) + ksm * 1024, 0, ST_AUX);
    }
    if (grp == 1) k2_prologue_b(wr, l, wd, isp, ct);
  } else {
    const int head = 2 * pa + grp;
    bf16x8 kop[5]; u32x2 vp[5][2];
#pragma unroll
    for (int i = 0; i < 4; ++i) {
      kop[i + 1] = pack8(acc[i][2], acc[i][3]);
#pragma unroll
      for (int d = 0; d < 2; ++d) { vp[i + 1][d].x = cvt_pk(acc[i][4 + d][0], acc[i][4 + d][1]); vp[i + 1][d].y = cvt_pk(acc[i][4 + d][2], acc[i][4 + d][3]); }
    }
    const u32x4 klast = __builtin_bit_cast(u32x4, kop[4]);
    const u32x4 vlast = (u32x4){vp[4][0].x, vp[4][0].y, vp[4][1].x, vp[4][1].y};
    const __amdgpu_buffer_rsrc_t hr = __builtin_amdgcn_make_buffer_rsrc(ws + WS_HALO, 0, 8 * 32 * 2048, 0x00020000);
    const bool send = (wr == 3) && (pm < 7);
    if (send) {
      const unsigned ho = (pm * 32 + head) * 2048;
      __builtin_amdgcn_raw_buffer_store_b128(klast, hr, l * 16 + ho, 0, 16);
      __builtin_amdgcn_raw_buffer_store_b128(vlast, hr, l * 16 + ho + 1024, 0, 16);
    }
    if (wr != 3) {
      *(LAS u32x4*)(lds + LDS_HALO + w * 2048 + l * 16) = klast;
      *(LAS u32x4*)(lds + LDS_HALO + w * 2048 + 1024 + l * 16) = vlast;
    }
#pragma unroll
    for (int i = 0; i < 4; ++i) {
      f32x4 h0, h1;
#pragma unroll
      for (int r = 0; r < 4; ++r) { h0[r] = silu_f(acc[i][6][r]) * acc[i][8][r]; h1[r] = silu_f(acc[i][7][r]) * acc[i][9][r]; }
      __builtin_amdgcn_raw_buffer_store_b128(__builtin_bit_cast(u32x4, pack8(h0, h1)), a2, l * 16 + (mf0 + i) * (KS2 * 1024) + (2 * pa + grp) * 1024, 0, ST_AUX);
    }
    bf16x8 qop[4];
#pragma unroll
    for (int i = 0; i < 4; ++i) qop[i] = pack8(acc[i][0], acc[i][1]);
    const int m = l & 15; const bool par = m & 1;
    const bool own0 = 4 * g + (m & 1) <= m, own1 = 4 * g + (m & 1) + 2 <= m;
    const unsigned hmask = par ? 0xffff0000u : 0x0000ffffu;
    const float scale = 0.17677669529663687f * 1.44269504089f;
    auto attend = [&](auto n_, int I0, bool have_prev) {
      constexpr int N = decltype(n_)::value;
      const f32x4 z = (f32x4){0.f, 0.f, 0.f, 0.f};
      f32x4 s0[N], s1[N];
#pragma unroll
      for (int k = 0; k < N; ++k) { s0[k] = MFMA(kop[I0 + k + 1], qop[I0 + k], z); s1[k] = MFMA(kop[I0 + k], qop[I0 + k], z); }
      float l0[N], l1[N], mx[N], e0[N], e1[N], inv[N];
#pragma unroll
      for (int k = 0; k < N; ++k) {
        l0[k] = (own0 ? (par ? s0[k][1] : s0[k][0]) : (par ? s1[k][1] : s1[k][0])) * scale;
        l1[k] = (own1 ? (par ? s0[k][3] : s0[k][2]) : (par ? s1[k][3] : s1[k][2])) * scale;
        if (!have_prev) { l0[k] = own0 ? l0[k] : -3.0e38f; l1[k] = own1 ? l1[k] : -3.0e38f; }
        mx[k] = fmaxf(l0[k], l1[k]);
      }
#pragma unroll
      for (int k = 0; k < N; ++k) mx[k] = xmax16_32(mx[k]);
#pragma unroll
      for (int k = 0; k < N; ++k) { e0[k] = __builtin_amdgcn_exp2f(l0[k] - mx[k]); e1[k] = __builtin_amdgcn_exp2f(l1[k] - mx[k]); inv[k] = e0[k] + e1[k]; }
#pragma unroll
      for (int k = 0; k < N; ++k) inv[k] = __builtin_amdgcn_rcpf(xsum16_32(inv[k]));
      f32x4 o[N][2];
#pragma unroll
      for (int k = 0; k < N; ++k) {
        const unsigned w0 = cvt_pk(e0[k] * inv[k], e0[k] * inv[k]) & hmask, w1 = cvt_pk(e1[k] * inv[k], e1[k] * inv[k]) & hmask;
        const u32x4 pw = (u32x4){own0 ? 0u : w0, own1 ? 0u : w1, own0 ? w0 : 0u, own1 ? w1 : 0u};
#pragma unroll
        for (int d = 0; d < 2; ++d) {
          const u32x4 vv = (u32x4){vp[I0 + k][d].x, vp[I0 + k][d].y, vp[I0 + k + 1][d].x, vp[I0 + k + 1][d].y};
          o[k][d] = MFMA(__builtin_bit_cast(bf16x8, vv), __builtin_bit_cast(bf16x8, pw), z);
        }
      }
#pragma unroll
      for (int k = 0; k < N; ++k)
        __builtin_amdgcn_raw_buffer_store_b128(__builtin_bit_cast(u32x4, pack8(o[k][0], o[k][1])), a2, l * 16 + (mf0 + I0 + k) * (KS2 * 1024) + (112 + head) * 1024, 0, ST_AUX);
    };
    attend(std::integral_constant<int, 3>{}, 1, true);
    STAMPT(12, 0);
    WAIT_V(3);
    if (send && l == 0) flag_set(ws, WS_HLF + (pm * 32 + head) * 64, tag);
    WAIT_L(0); BAR();
    if (w == 0) flag_set8(ws, WS_T1A + (pm * 16 + pa) * 8, tag, l);
    STAMPT(13, 0);
    if (grp == 1) k2_prologue_b(wr, l, wd, isp, ct);
    bool have_prev = true;
    u32x4 kh, vh;
    if (wr == 0) {
      if (pm > 0) {
        flag_wait(ws, WS_HLF + ((pm - 1) * 32 + head) * 64, 1, tag, l);
        const unsigned ho = ((pm - 1) * 32 + head) * 2048;
        kh = __builtin_amdgcn_raw_buffer_load_b128(hr, l * 16 + ho, 0, 16);
        vh = __builtin_amdgcn_raw_buffer_load_b128(hr, l * 16 + ho + 1024, 0, 16);
      } else { kh = (u32x4){0u, 0u, 0u, 0u}; vh = kh; have_prev = false; }
    } else {
      kh = *(LAS u32x4*)(lds + LDS_HALO + (w - 1) * 2048 + l * 16);
      vh = *(LAS u32x4*)(lds + LDS_HALO + (w - 1) * 2048 + 1024 + l * 16);
    }
    kop[0] = __builtin_bit_cast(bf16x8, kh);
    vp[0][0] = (u32x2){vh.x, vh.y}; vp[0][1] = (u32x2){vh.z, vh.w};
    attend(std::integral_constant<int, 1>{}, 0, have_prev);
  }
  STAMPT(14, 0);
  WAIT_V(0); BAR();
  if (w == 0) flag_set8(ws, WS_T1F + (pm * 32 + TYPE * 16 + pa) * 8, tag, l);
  STAMP(3);
  return tag;
}

struct K2Frag { bf16x8 a[4], b[4]; };
struct K2Rd { LAS unsigned char* lds; unsigned aoff[2], vb[2][4]; };
template <int SLOT> __device__ __forceinline__ void k2_read(const K2Rd& c, K2Frag& f) {
  constexpr int H = SLOT >> 2, O = (SLOT & 3) * K2_SLOT;
#pragma unroll
  for (int j = 0; j < 4; ++j) { LAS unsigned char* p = c.lds + c.vb[H][j] + O; f.b[j] = lds_tr8(p, p + 4096); }
#pragma unroll
  for (int i = 0; i < 4; ++i) f.a[i] = *(LAS bf16x8*)(c.lds + c.aoff[H] + O + i * 1024);
}
template <int SLOTN> __device__ __forceinline__ void k2_step(const K2Rd& c, f32x4 (&acc)[4][4], const K2Frag& cur, K2Frag& nxt) {
  k2_read<SLOTN>(c, nxt);
#pragma unroll
  for (int j = 0; j < 4; ++j)
#pragma unroll
    for (int i = 0; i < 4; ++i) acc[i][j] = MFMA(cur.b[j], cur.a[i], acc[i][j]);
#pragma unroll
  for (int k = 0; k < 6; ++k) { __builtin_amdgcn_sched_group_barrier(0x8, 1, 0); __builtin_amdgcn_sched_group_barrier(0x100, 2, 0); }
  __builtin_amdgcn_sched_group_barrier(0x8, 10, 0);
  SCHED(); __builtin_amdgcn_s_waitcnt(0xC07F); asm volatile("" ::: "memory"); BAR();
}
__device__ __forceinline__ void k2_consume(const K2Rd& c, f32x4 (&acc)[4][4], K2Frag& f0, K2Frag& f1, int iters) {
#pragma nounroll
  for (int it = 0; it < iters; ++it) {
    k2_step<1>(c, acc, f0, f1); k2_step<2>(c, acc, f1, f0); k2_step<3>(c, acc, f0, f1); k2_step<4>(c, acc, f1, f0);
    k2_step<5>(c, acc, f0, f1); k2_step<6>(c, acc, f1, f0); k2_step<7>(c, acc, f0, f1); k2_step<0>(c, acc, f1, f0);
  }
}

struct K2Ld { unsigned vsta, vstb; i32x4 sa; unsigned ldst; };
__device__ __forceinline__ void k2_stage(const K2Ld& c, i32x4 sb, unsigned ka, unsigned kb, int slot) {
  const unsigned d = slot * K2_SLOT + c.ldst;
  unsigned t0;
  asm volatile(
    "s_mov_b32 m0, %7\n\ts_add_u32 %0, %5, 0x24000\n\tbuffer_load_dwordx4 %1, %3, %5 offen sc1 lds\n\t"
    "s_add_u32 m0, %7, 0x400\n\ts_nop 0\n\tbuffer_load_dwordx4 %1, %3, %0 offen sc1 lds\n\t"
    "s_add_u32 m0, %7, 0x2000\n\ts_add_u32 %0, %6, 0x80\n\tbuffer_load_dwordx4 %2, %4, %6 offen lds\n\t"
    "s_add_u32 m0, %7, 0x2400\n\ts_nop 0\n\tbuffer_load_dwordx4 %2, %4, %0 offen lds"
    : "=&s"(t0) : "v"(c.vsta), "v"(c.vstb), "s"(c.sa), "s"(sb), "s"(ka), "s"(kb), "s"(d) : "memory", "scc");
}
__device__ __forceinline__ void k2_load(const K2Ld& c, i32x4 sb, unsigned ka, unsigned kb, int iters) {
#pragma nounroll
  for (int it = 0; it < iters; ++it) {
#pragma unroll
    for (int j = 0; j < 8; ++j) {
      k2_stage(c, sb, ka, kb, j);
      ka += 1024u; kb += 65536u;
      WAIT_V(24); BAR();
    }
  }
}
__device__ __forceinline__ bool k2_load_poll(const K2Ld& c, i32x4 sb, unsigned ka, unsigned kb, const unsigned char* fl, int n, u64 tag, int l) {
  u32x2 pv;
  const unsigned char* fp = fl + (l < n ? l : 0) * 8;
#pragma unroll
  for (int j = 0; j < 8; ++j) {
    if (j == 1) asm volatile("global_load_dwordx2 %0, %1, off sc1" : "=&v"(pv) : "v"(fp) : "memory");
    k2_stage(c, sb, ka, kb, j);
    ka += 1024u; kb += 65536u;
    WAIT_V(24);
    if (j < 7) BAR();
  }
  asm volatile("" : "+v"(pv));
  return __all((((u64)pv.y << 32) | pv.x) == tag);
}
template <int AUX> __device__ __forceinline__ void glds16x(unsigned voff, i32x4 srd, unsigned soff, unsigned ldsaddr) {
  if (AUX == 16) asm volatile("s_mov_b32 m0, %3\n\ts_nop 0\n\tbuffer_load_dwordx4 %0, %1, %2 offen sc1 lds" ::"v"(voff), "s"(srd), "s"(soff), "s"(ldsaddr) : "memory");
  else asm volatile("s_mov_b32 m0, %3\n\ts_nop 0\n\tbuffer_load_dwordx4 %0, %1, %2 offen lds" ::"v"(voff), "s"(srd), "s"(soff), "s"(ldsaddr) : "memory");
}

template <bool ISP> __device__ __forceinline__ void k2_body(LAS unsigned char* lds, const u16* x, const u16* wo, const u16* wd, u16* out, unsigned char* ws, int rt, int ct, u64 tag) {
  constexpr int NT = ISP ? K2_TP : K2_TQ;
  int tid = threadIdx.x; asm volatile("" : "+v"(tid));
  const int w = __builtin_amdgcn_readfirstlane(tid >> 6), l = tid & 63;
  const int w4 = w & 3, wm = w4 >> 1, wn = w4 & 1;
  const int g = l >> 4, q = (l & 15) >> 2, p = l & 3;
  const int tile = rt * 8 + ct;
  if (w >= 4) {
    K2Ld c;
    c.vsta = l * 16; c.vstb = k2_vstb(w4, l);
    c.sa = make_srd(ws + WS_A2, 128 * KS2 * 1024);
    const i32x4 swd = make_srd(wd, F_ * D_ * 2), swo = make_srd(wo, D_ * D_ * 2);
    const unsigned soa = (rt * 8 + 2 * w4) * (KS2 * 1024), sob = ct * 256;
    c.ldst = w4 * 2048;
    const size_t rep = (blockIdx.x & 7) * REP, t1f = WS_T1F + rep + (rt >> 1) * 256;
    if (w == 4) { if (ISP) flag_wait(ws, WS_T1A + rep + (rt >> 1) * 128, 16, tag, l); else flag_wait(ws, t1f + 128, 16, tag, l); }
    BAR();
    const unsigned ks0 = ISP ? 0u : K2_H0, ka0 = ks0 * 1024u + soa;
#pragma unroll
    for (int t = 0; t < 8; ++t) { glds16x<16>(c.vsta, c.sa, ka0 + t * 1024u, t * K2_SLOT + c.ldst); glds16x<16>(c.vsta, c.sa, ka0 + t * 1024u + 0x24000u, t * K2_SLOT + c.ldst + 1024); }
    WAIT_V(12); BAR();
    BAR();
    {
      const unsigned ka = ka0 + 8 * 1024u, kb = (ks0 + 8) * 65536u + sob;
#define K2_FIRST(J, N) do { k2_stage(c, swd, ka + (J) * 1024u, kb + (J) * 65536u, J); WAIT_V(N); BAR(); } while (0)
      K2_FIRST(0, 14); K2_FIRST(1, 16); K2_FIRST(2, 18); K2_FIRST(3, 20); K2_FIRST(4, 22); K2_FIRST(5, 24); K2_FIRST(6, 24); K2_FIRST(7, 24);
    }
    const i32x4 sx = make_srd(x, S_ * D_ * 2), sp = make_srd(ws + WS_PART, 128 * 32768);
    if (ISP) {
      static_assert(K2_TP >= 72 && K2_TP <= 96, "P schedule: 32 h + 32 o + 8..32 h");
      k2_load(c, swd, 16 * 1024u + soa, 16 * 65536u + sob, 1);
      if (!k2_load_poll(c, swd, 24 * 1024u + soa, 24 * 65536u + sob, ws + t1f, 16, tag, l)) flag_wait(ws, t1f, 16, tag, l);
      BAR();
      k2_load(c, swo, 112 * 1024u + soa, sob, 3);
      if (!k2_load_poll(c, swo, (112 + 24) * 1024u + soa, 24 * 65536u + sob, ws + t1f + 128, 5, tag, l)) flag_wait(ws, t1f + 128, 5, tag, l);
      BAR();
      k2_load(c, swd, 32 * 1024u + soa, 32 * 65536u + sob, (NT - 64) / 8 - 1);
      const bool early = k2_load_poll(c, swd, (NT - 40) * 1024u + soa, (NT - 40) * 65536u + sob, ws + WS_PTF + tile * 64, 4, tag, l);
      BAR();
      const unsigned xr8 = l >> 3, xv = ((rt * 128 + wm * 64 + xr8) * D_ + ct * 128 + wn * 64) * 2;
#pragma unroll
      for (int k = 0; k < 2; ++k) {
#pragma unroll
        for (int e = 0; e < 4; ++e) {
          const int blk = k * 4 + e;
          glds16x<0>(xv + (((l & 7) ^ (4 * (blk & 1) + (xr8 >> 1))) << 4), sx, blk * 8 * D_ * 2, k * 16384 + w4 * 4096 + e * 1024);
        }
        WAIT_V(24); BAR();
      }
      if (early) {
#pragma unroll
        for (int k = 2; k < 4; ++k) {
#pragma unroll
          for (int e = 0; e < 4; ++e) glds16x<16>(l * 16, sp, tile * 32768 + w4 * 8192 + ((k - 2) * 4 + e) * 1024, k * 16384 + w4 * 4096 + e * 1024);
          WAIT_V(24); BAR();
        }
        WAIT_V(20); BAR(); WAIT_V(16); BAR(); BAR(); WAIT_V(0); BAR();
      } else {
        WAIT_V(20); BAR(); WAIT_V(16); BAR(); WAIT_V(12); BAR();
        WAIT_V(0);
        flag_wait(ws, WS_PTF + tile * 64, 4, tag, l);
#pragma unroll
        for (int e = 0; e < 8; ++e) glds16x<16>(l * 16, sp, tile * 32768 + w4 * 8192 + e * 1024, (2 + (e >> 2)) * 16384 + w4 * 4096 + (e & 3) * 1024);
        BAR(); BAR();
        WAIT_V(0); BAR();
      }
    } else {
      k2_load(c, swd, (K2_H0 + 16) * 1024u + soa, (K2_H0 + 16) * 65536u + sob, (NT - 16) / 8);
      WAIT_V(20); BAR(); WAIT_V(16); BAR(); WAIT_V(12); BAR(); WAIT_V(8); BAR(); WAIT_V(4); BAR(); WAIT_V(0); BAR(); BAR(); BAR();
    }
    if (!ISP && blockIdx.x == 4 && w == 5) {
      gu64* p = (gu64*)(ws + WS_T1F) + l;
      for (unsigned spins = 0; spins < (1u << 18); ++spins) {
        const u64 a = __hip_atomic_load(p, __ATOMIC_RELAXED, __HIP_MEMORY_SCOPE_AGENT), b = __hip_atomic_load(p + 64, __ATOMIC_RELAXED, __HIP_MEMORY_SCOPE_AGENT);
        const u64 c2 = __hip_atomic_load(p + 128, __ATOMIC_RELAXED, __HIP_MEMORY_SCOPE_AGENT), d = __hip_atomic_load(p + 192, __ATOMIC_RELAXED, __HIP_MEMORY_SCOPE_AGENT);
        if (__all(a == tag && b == tag && c2 == tag && d == tag)) break;
        __builtin_amdgcn_s_sleep(2);
      }
      if (l == 0) flag_set(ws, WS_GEN, tag);
    }
    return;
  }
  K2Rd c; c.lds = lds;
  {
    const unsigned row = 4 * (g & 1) + q, s2 = (row >> 1) & 3;
    const unsigned lanep = 8192 + (g >> 1) * 2048 + wn * 1024 + row * 128 + (p >> 1) * 16 + (p & 1) * 8;
#pragma unroll
    for (int h = 0; h < 2; ++h) {
      c.aoff[h] = l * 16 + wm * 4096 + h * 65536;
#pragma unroll
      for (int j = 0; j < 4; ++j) c.vb[h][j] = lanep + ((j ^ s2) << 5) + h * 65536;
    }
  }
  f32x4 accf[4][4], accs[4][4];
#pragma unroll
  for (int i = 0; i < 4; ++i)
#pragma unroll
    for (int j = 0; j < 4; ++j) { accf[i][j] = (f32x4){0.f, 0.f, 0.f, 0.f}; accs[i][j] = accf[i][j]; }
  K2Frag f0, f1;
  BAR();
  BAR();
  STAMP(4);
  k2_read<0>(c, f0);
  WAIT_L(0); BAR();
  if (ISP) { k2_consume(c, accf, f0, f1, 4); k2_consume(c, accs, f0, f1, 4); k2_consume(c, accf, f0, f1, (NT - 64) / 8); }
  else k2_consume(c, accf, f0, f1, NT / 8);
  STAMP(5);
  if (!ISP) {
    const __amdgpu_buffer_rsrc_t pr = __builtin_amdgcn_make_buffer_rsrc(ws + WS_PART, 0, 128 * 32768, 0x00020000);
    const unsigned po = tile * 32768 + w4 * 8192 + l * 16;
#pragma unroll
    for (int i = 0; i < 4; ++i)
#pragma unroll
      for (int jp = 0; jp < 2; ++jp) __builtin_amdgcn_raw_buffer_store_b128(__builtin_bit_cast(u32x4, pack8(accf[i][2 * jp], accf[i][2 * jp + 1])), pr, po + (i * 2 + jp) * 1024, 0, 16);
    WAIT_V(0);
    if (l == 0) flag_set(ws, WS_PTF + tile * 64 + w4 * 8, tag);
  } else {
    const int m = l & 15;
    const __amdgpu_buffer_rsrc_t orr = __builtin_amdgcn_make_buffer_rsrc(out, 0, S_ * D_ * 2, 0x00020000);
    const unsigned ov = ((rt * 128 + wm * 64 + m) * D_ + ct * 128 + wn * 64 + (((g & 1) << 1) | (g >> 1)) * 8) * 2;
    u32x4 pt[8]; u32x2 xr[4][4];
#pragma unroll
    for (int b = 0; b < 8; ++b) pt[b] = *(LAS u32x4*)(lds + (2 + (b >> 2)) * 16384 + w4 * 4096 + (b & 3) * 1024 + l * 16);
#pragma unroll
    for (int i = 0; i < 4; ++i)
#pragma unroll
      for (int j = 0; j < 4; ++j) {
        const int blk = 2 * i + (m >> 3);
        xr[i][j] = *(LAS u32x2*)(lds + (i >> 1) * 16384 + w4 * 4096 + ((blk & 3) * 1024) + (m & 7) * 128 + (((2 * j + (g >> 1)) ^ (m >> 1)) << 4) + (g & 1) * 8);
      }
#pragma unroll
    for (int i = 0; i < 4; ++i)
#pragma unroll
      for (int jp = 0; jp < 2; ++jp) {
        const int b = i * 2 + jp;
        unsigned y[2][2];
#pragma unroll
        for (int jj = 0; jj < 2; ++jj) {
          const int j = 2 * jp + jj;
          const f32x4 f = accf[i][j] + (f32x4){bf_lo(pt[b][2 * jj]), bf_hi(pt[b][2 * jj]), bf_lo(pt[b][2 * jj + 1]), bf_hi(pt[b][2 * jj + 1])};
          const f32x4 xv4 = (f32x4){bf_lo(xr[i][j].x), bf_hi(xr[i][j].x), bf_lo(xr[i][j].y), bf_hi(xr[i][j].y)};
          float v[4];
#pragma unroll
          for (int r = 0; r < 4; ++r) v[r] = xv4[r] + accs[i][j][r] * silu_f(f[r]);
          y[jj][0] = cvt_pk(v[0], v[1]); y[jj][1] = cvt_pk(v[2], v[3]);
        }
        const auto s0 = __builtin_amdgcn_permlane16_swap(y[0][0], y[1][0], false, false);
        const auto s1 = __builtin_amdgcn_permlane16_swap(y[0][1], y[1][1], false, false);
        __builtin_amdgcn_raw_buffer_store_b128((u32x4){s0[0], s1[0], s0[1], s1[1]}, orr, ov + i * 16 * D_ * 2 + jp * 64, 0, OUT_AUX);
      }
  }
  STAMP(6);
}

__global__ __launch_bounds__(512, 2) void fused_block(const u16* __restrict__ x, const u16* __restrict__ wqkv, const u16* __restrict__ wo, const u16* __restrict__ wg,
                                                      const u16* __restrict__ wu, const u16* __restrict__ wd, u16* __restrict__ out, unsigned char* __restrict__ ws) {
  extern __shared__ __attribute__((aligned(16))) unsigned char shm[];
  LAS unsigned char* lds = (LAS unsigned char*)shm;
  const int b = blockIdx.x, xcd = b & 7, idx = b >> 3;
  const int pm = idx & 7, pn = idx >> 3, pa = xcd * 2 + (pn >> 1);
  const int lnmf = idx < 16 ? idx * 8 + xcd : -1;
  const int pr = xcd & 3, rt = (pr & 1) * 8 + (idx & 7), ct = (pr >> 1) * 4 + (idx >> 3);
  const bool isp = xcd < 4;
  u64 tag;
  if (pn & 1) tag = k1_body<1>(lds, x, wqkv, wg, wu, wo, wd, ws, pm, pa, lnmf, isp, ct);
  else tag = k1_body<0>(lds, x, wqkv, wg, wu, wo, wd, ws, pm, pa, lnmf, isp, ct);
  if (isp) k2_body<true>(lds, x, wo, wd, out, ws, rt, ct, tag);
  else k2_body<false>(lds, x, wo, wd, out, ws, rt, ct, tag);
}

extern "C" void kernel_launch(void* const* d_in, const int* in_sizes, int n_in, void* d_out, int out_size, void* d_ws, size_t ws_size, hipStream_t stream) {
  const u16* x = (const u16*)d_in[0]; const u16* wqkv = (const u16*)d_in[1]; const u16* wo = (const u16*)d_in[2];
  const u16* wg = (const u16*)d_in[4]; const u16* wu = (const u16*)d_in[5]; const u16* wd = (const u16*)d_in[6];
  (void)hipFuncSetAttribute((const void*)fused_block, hipFuncAttributeMaxDynamicSharedMemorySize, LDS_TOTAL);
  fused_block<<<256, 512, LDS_TOTAL, stream>>>(x, wqkv, wo, wg, wu, wd, (u16*)d_out, (unsigned char*)d_ws);
}
```

```cpp
#include <hip/hip_runtime.h>
#include <stdint.h>
#include <type_traits>

#define LAS __attribute__((address_space(3)))
#define GAS __attribute__((address_space(1)))
typedef __bf16 bf16x8 __attribute__((ext_vector_type(8)));
typedef short s16x4 __attribute__((ext_vector_type(4)));
typedef short s16x8 __attribute__((ext_vector_type(8)));
typedef float f32x4 __attribute__((ext_vector_type(4)));
typedef float f32x2 __attribute__((ext_vector_type(2)));
typedef unsigned u32x4 __attribute__((ext_vector_type(4)));
typedef unsigned u32x2 __attribute__((ext_vector_type(2)));
typedef int i32x4 __attribute__((ext_vector_type(4)));
typedef unsigned short u16;

constexpr int S_ = 2048, D_ = 1024, F_ = 3584;
constexpr int KS2 = 144;
constexpr size_t WS_GEN = 0;
constexpr size_t REP = 8192;
constexpr size_t WS_LNF = 4096;
constexpr size_t WS_T1A = WS_LNF + 2048;
constexpr size_t WS_T1F = WS_LNF + 4096;
constexpr size_t WS_HLF = 4096 + 8 * REP;
constexpr size_t WS_PTF = WS_HLF + 16384;
static_assert(WS_PTF + 8192 <= 131072, "flag area");
constexpr size_t WS_XN = 131072;
constexpr size_t WS_A2 = WS_XN + 4194304;
constexpr size_t WS_HALO = WS_A2 + (size_t)128 * KS2 * 1024;
constexpr size_t WS_PART = WS_HALO + 8 * 32 * 2048;

constexpr int P_STEPS = 76;
constexpr int SC1 = 16;

__device__ __forceinline__ i32x4 make_srd(const void* p, unsigned bytes) {
  const unsigned long long a = (unsigned long long)p;
  i32x4 r; r.x = (int)(unsigned)a; r.y = (int)((unsigned)(a >> 32) & 0xffffu); r.z = (int)bytes; r.w = 0x00020000; return r;
}
__device__ __forceinline__ void glds16(unsigned voff, i32x4 srd, unsigned soff, unsigned ldsaddr) {
  asm volatile("s_mov_b32 m0, %3\n\ts_nop 0\n\tbuffer_load_dwordx4 %0, %1, %2 offen lds" ::"v"(voff), "s"(srd), "s"(soff), "s"(ldsaddr) : "memory");
}
#define WAIT_V(n) asm volatile("s_waitcnt vmcnt(" #n ")" ::: "memory")
#define WAIT_L(n) asm volatile("s_waitcnt lgkmcnt(" #n ")" ::: "memory")
#define SCHED() __builtin_amdgcn_sched_barrier(0)
#define BAR() do { SCHED(); __builtin_amdgcn_s_barrier(); SCHED(); } while (0)

typedef __bf16 bf16x2 __attribute__((ext_vector_type(2)));
__device__ __forceinline__ unsigned cvt_pk(float lo, float hi) { const f32x2 v = {lo, hi}; return __builtin_bit_cast(unsigned, __builtin_convertvector(v, bf16x2)); }
__device__ __forceinline__ float bf_lo(unsigned u) { return __uint_as_float(u << 16); }
__device__ __forceinline__ float bf_hi(unsigned u) { return __uint_as_float(u & 0xffff0000u); }
__device__ __forceinline__ float rbf(float f) { return bf_lo(cvt_pk(f, f)); }
__device__ __forceinline__ bf16x8 pack8(const f32x4& a, const f32x4& b) {
  u32x4 w; w.x = cvt_pk(a[0], a[1]); w.y = cvt_pk(a[2], a[3]); w.z = cvt_pk(b[0], b[1]); w.w = cvt_pk(b[2], b[3]);
  return __builtin_bit_cast(bf16x8, w);
}
__device__ __forceinline__ bf16x8 lds_tr8(LAS unsigned char* p0, LAS unsigned char* p1) {
  s16x4 a = __builtin_amdgcn_ds_read_tr16_b64_v4i16((LAS s16x4*)p0);
  s16x4 b = __builtin_amdgcn_ds_read_tr16_b64_v4i16((LAS s16x4*)p1);
  s16x8 c = __builtin_shufflevector(a, b, 0, 1, 2, 3, 4, 5, 6, 7);
  return __builtin_bit_cast(bf16x8, c);
}
__device__ __forceinline__ float silu_f(float g) { return g * __builtin_amdgcn_rcpf(1.0f + __builtin_amdgcn_exp2f(g * -1.44269504089f)); }
#define MFMA(a, b, c) __builtin_amdgcn_mfma_f32_16x16x32_bf16(a, b, c, 0, 0, 0)

typedef unsigned long long u64;
typedef GAS u64 gu64;
__device__ __forceinline__ void flag_set(unsigned char* ws, size_t off, u64 tag) { __hip_atomic_store((gu64*)(ws + off), tag, __ATOMIC_RELAXED, __HIP_MEMORY_SCOPE_AGENT); }
__device__ __forceinline__ void flag_set8(unsigned char* ws, size_t off, u64 tag, int l) { if (l < 8) flag_set(ws, off + l * REP, tag); }
__device__ __forceinline__ void flag_wait(unsigned char* ws, size_t off, int n, u64 tag, int l) {
  gu64* p = (gu64*)(ws + off) + (l < n ? l : 0);
  for (unsigned spins = 0; spins < (1u << 18); ++spins) {
    const u64 v = __hip_atomic_load(p, __ATOMIC_RELAXED, __HIP_MEMORY_SCOPE_AGENT);
    if (__all(v == tag)) break;
    __builtin_amdgcn_s_sleep(2);
  }
  asm volatile("" ::: "memory");
}

struct PollKeep { u32x2 a, b, c; };
#define POLL_LD(r, p) asm volatile("global_load_dwordx2 %0, %1, off sc1" : "=&v"(r) : "v"(p) : "memory")
#define POLL_WT(r) asm volatile("s_waitcnt vmcnt(2)" : "+v"(r) :: "memory")
#define POLL_KEEP(k) asm volatile("" :: "v"((k).a), "v"((k).b), "v"((k).c))
__device__ __forceinline__ void flag_wait3(unsigned char* ws, size_t off, int n, u64 tag, int l, PollKeep& k) {
  const unsigned char* p = ws + off + (l < n ? l : 0) * 8;
  const unsigned tlo = (unsigned)tag, thi = (unsigned)(tag >> 32);
  POLL_LD(k.a, p); POLL_LD(k.b, p); POLL_LD(k.c, p);
  for (unsigned spins = 0; spins < (1u << 17); ++spins) {
    POLL_WT(k.a); if (__all(k.a.x == tlo && k.a.y == thi)) break; POLL_LD(k.a, p);
    POLL_WT(k.b); if (__all(k.b.x == tlo && k.b.y == thi)) break; POLL_LD(k.b, p);
    POLL_WT(k.c); if (__all(k.c.x == tlo && k.c.y == thi)) break; POLL_LD(k.c, p);
  }
  asm volatile("" ::: "memory");
}

__device__ __forceinline__ float xsum16_32(float v) {
  const auto a = __builtin_amdgcn_permlane16_swap(__float_as_uint(v), __float_as_uint(v), false, false);
  v = __uint_as_float(a[0]) + __uint_as_float(a[1]);
  const auto b = __builtin_amdgcn_permlane32_swap(__float_as_uint(v), __float_as_uint(v), false, false);
  return __uint_as_float(b[0]) + __uint_as_float(b[1]);
}
__device__ __forceinline__ float xmax16_32(float v) {
  const auto a = __builtin_amdgcn_permlane16_swap(__float_as_uint(v), __float_as_uint(v), false, false);
  v = fmaxf(__uint_as_float(a[0]), __uint_as_float(a[1]));
  const auto b = __builtin_amdgcn_permlane32_swap(__float_as_uint(v), __float_as_uint(v), false, false);
  return fmaxf(__uint_as_float(b[0]), __uint_as_float(b[1]));
}
constexpr unsigned PF_STRIDE = 128;
constexpr int K1_ASLOT = 16384, K1_BSLOT = 20480, K1_BOFF = 4 * K1_ASLOT, K1_LDS = K1_BOFF + 4 * K1_BSLOT;
constexpr int LDS_PFDUMP = K1_LDS, LDS_LNRED = K1_LDS + 256, LDS_TOTAL = K1_LDS + 256 + 1024, LDS_HALO = 131072;

struct K1Ctx {
  LAS unsigned char* lds;
  unsigned aoff;
  unsigned vb[6];
  unsigned vst0, vst1;
  i32x4 s0, s1, s2;
  unsigned so0, so1;
  unsigned ldst;
};

template <bool DEV> __device__ __forceinline__ void dma1(unsigned voff, i32x4 srd, unsigned soff, unsigned ldsaddr) {
  if (DEV) asm volatile("s_mov_b32 m0, %3\n\ts_nop 0\n\tbuffer_load_dwordx4 %0, %1, %2 offen sc1 lds" ::"v"(voff), "s"(srd), "s"(soff), "s"(ldsaddr) : "memory");
  else asm volatile("s_mov_b32 m0, %3\n\ts_nop 0\n\tbuffer_load_dwordx4 %0, %1, %2 offen lds" ::"v"(voff), "s"(srd), "s"(soff), "s"(ldsaddr) : "memory");
}
constexpr unsigned OOR = 0x7f000000u;
template <int TYPE, int P> __device__ __forceinline__ void k1_piece_b(const K1Ctx& c, unsigned s, int slot) {
  const unsigned d = K1_BOFF + slot * K1_BSLOT + c.ldst + P * 1024;
  const bool in = s < 32;
  if (TYPE == 0) {
    if (P < 3) dma1<false>(c.vst0, c.s0, in ? s * (32u * 6144u) + c.so0 + P * 2048u : OOR, d);
    else dma1<false>(c.vst1, P == 3 ? c.s1 : c.s2, in ? s * (32u * 7168u) + c.so0 : OOR, d);
  } else {
    if (P < 4) dma1<false>(c.vst0, P < 2 ? c.s0 : c.s1, in ? s * (32u * 7168u) + c.so0 + (P & 1) * 128u : OOR, d);
    else dma1<false>(c.vst1, c.s2, in ? s * (32u * 7168u) + c.so1 : OOR, d);
  }
}
template <int TYPE> __device__ __forceinline__ void k1_stage_b(const K1Ctx& c, unsigned s, int slot) {
  k1_piece_b<TYPE, 0>(c, s, slot); k1_piece_b<TYPE, 1>(c, s, slot); k1_piece_b<TYPE, 2>(c, s, slot); k1_piece_b<TYPE, 3>(c, s, slot); k1_piece_b<TYPE, 4>(c, s, slot);
}
template <int P> __device__ __forceinline__ void k1_piece_a(const K1Ctx& c, unsigned s, int slot) {
  dma1<true>(c.vst0, c.s0, s < 32 ? s * 1024u + c.so0 + P * 32768u : OOR, slot * K1_ASLOT + c.ldst + P * 1024);
}
__device__ __forceinline__ void k1_stage_a(const K1Ctx& c, unsigned s, int slot) {
  k1_piece_a<0>(c, s, slot); k1_piece_a<1>(c, s, slot); k1_piece_a<2>(c, s, slot); k1_piece_a<3>(c, s, slot);
}
#define BAR_IF(cnt, lim) do { SCHED(); asm volatile("s_cmp_ge_u32 %0, %1\n\ts_cbranch_scc1 .Lb%=\n\ts_barrier\n.Lb%=:" :: "s"(cnt), "i"(lim) : "memory", "scc"); SCHED(); } while (0)
template <int TYPE, int J0> __device__ __forceinline__ void k1_read_b(const K1Ctx& c, int slot, bf16x8 (&bfr)[10]) {
  LAS unsigned char* lds = c.lds;
  const int bs = slot * K1_BSLOT;
#pragma unroll
  for (int j = J0; j < J0 + 2; ++j) {
    if (TYPE == 0) {
      LAS unsigned char* p = j < 8 ? lds + c.vb[j & 1] + bs + (j >> 1) * 1024 : lds + c.vb[2 + (j & 1)] + bs;
      bfr[j] = lds_tr8(p, p + 512);
    } else if (j < 8) { LAS unsigned char* p = lds + c.vb[j & 3] + bs + (j >> 2) * 2048; bfr[j] = lds_tr8(p, p + 512); }
    else { LAS unsigned char* p = lds + c.vb[4 + (j & 1)] + bs; bfr[j] = lds_tr8(p, p + 256); }
  }
}
__device__ __forceinline__ void k1_read_a(const K1Ctx& c, int slot, bf16x8 (&af)[4]) {
#pragma unroll
  for (int i = 0; i < 4; ++i) af[i] = *(LAS bf16x8*)(c.lds + c.aoff + slot * K1_ASLOT + i * 1024);
}
constexpr int K1_EARLY = 2;
template <int TYPE, int N0, int N1> __device__ __forceinline__ void k1_mma(f32x4 (&acc)[4][10], const bf16x8 (&af)[4], const bf16x8 (&bfr)[10]) {
#pragma unroll
  for (int n = N0; n < N1; ++n) {
    const int j = n >> 2, i = (j & 1) ? 3 - (n & 3) : (n & 3);
    if (TYPE == 0 && (j == 4 || j == 5)) acc[i][j] = MFMA(af[i], bfr[j], acc[i][j]);
    else acc[i][j] = MFMA(bfr[j], af[i], acc[i][j]);
  }
}
__device__ __forceinline__ void k1_pf(unsigned voff, i32x4 srd, unsigned soff, unsigned on) {
  asm volatile("s_cmp_lg_u32 %3, 4\n\ts_cbranch_scc1 .Lpf%=\n\ts_mov_b32 m0, %4\n\ts_nop 0\n\tbuffer_load_dword %0, %1, %2 offen lds\n.Lpf%=:"
               ::"v"(voff), "s"(srd), "s"(soff), "s"(on), "i"(LDS_PFDUMP) : "memory", "scc");
}
#define WAIT_A(PFN, on) do { asm volatile("s_cmp_eq_u32 %0, 4\n\ts_cbranch_scc1 .Lu%=\n\ts_waitcnt vmcnt(8)\n.Lu%=:\n\ts_waitcnt vmcnt(" #PFN ")" :: "s"(on) : "memory", "scc"); } while (0)

constexpr int K2_SLOT = 16384;
constexpr int K2_TP = P_STEPS, K2_TQ = KS2 - P_STEPS;
static_assert(K2_TP % 4 == 0 && K2_TQ % 4 == 0 && (K2_TP % 8) == (K2_TQ % 8) && K2_TP >= 72 && K2_TP <= 96, "step counts: multiples of 4");
constexpr unsigned K2_H0 = K2_TP - 32;
__device__ __forceinline__ unsigned k2_vstb(int w4, int l) { const unsigned r8 = l >> 3, ch = (l & 7) ^ (((r8 >> 1) & 3) << 1); return (8 * w4 + r8) * 2048 + ch * 16; }
__device__ __forceinline__ void k2_prologue_b(int w4, int l, const u16* wd, bool isp, int ct) {
  const i32x4 sb = make_srd(wd, F_ * D_ * 2);
  const unsigned vb = k2_vstb(w4, l), k0 = (isp ? 0u : K2_H0) * 65536u + ct * 256;
#pragma unroll
  for (int t = 0; t < 8; ++t) { glds16(vb, sb, k0 + t * 65536u, t * K2_SLOT + 8192 + w4 * 2048); glds16(vb, sb, k0 + t * 65536u + 128u, t * K2_SLOT + 8192 + w4 * 2048 + 1024); }
}

template <int TYPE> __device__ __forceinline__ u64 k1_body(LAS unsigned char* lds, const u16* x, const u16* wqkv, const u16* wg, const u16* wu, const u16* wo, const u16* wd, unsigned char* ws, int pm, int pa  , bool isp, int ct) {
  const int tid = threadIdx.x;
  const int w = __builtin_amdgcn_readfirstlane(tid >> 6), l = tid & 63;
  const int grp = w >> 2, wr = w & 3;
  const int g = l >> 4, q = (l & 15) >> 2, p = l & 3;
  K1Ctx c; c.lds = lds;
  c.aoff = l * 16 + wr * 4096;
  {
    const unsigned s2 = ((q >> 1) & 1) | ((g & 1) << 1);
    const unsigned lanep = K1_BOFF + g * 5120 + q * 128 + (p >> 1) * 16 + (p & 1) * 8;
    if (TYPE == 0) {
      c.vb[0] = lanep + (((2 * grp) ^ s2) << 5); c.vb[1] = lanep + (((2 * grp + 1) ^ s2) << 5);
      c.vb[2] = c.vb[0] + 4096; c.vb[3] = c.vb[1] + 4096; c.vb[4] = 0; c.vb[5] = 0;
    } else {
#pragma unroll
      for (int j = 0; j < 4; ++j) c.vb[j] = lanep + ((j ^ s2) << 5) + grp * 1024;
      const unsigned mx = K1_BOFF + (2 * (g >> 1) * 5 + 4) * 1024 + (g & 1) * 512 + q * 64 + (((2 * grp + (p >> 1)) ^ ((g & 1) << 1)) << 4) + (p & 1) * 8;
      c.vb[4] = mx; c.vb[5] = mx + 5120;
    }
  }
  const int c0 = 1024 + 160 * pa;
  const int odd = pa & 1;
  if (grp == 0) {
    const unsigned r8 = l >> 3, sw = (((r8 >> 1) & 1) | ((wr & 1) << 1)) << 1, ch = (l & 7) ^ sw;
    c.ldst = wr * 5120;
    if (TYPE == 0) {
      c.vst0 = (8 * wr + r8) * 6144 + ch * 16; c.vst1 = (8 * wr + r8) * 7168 + ch * 16;
      c.s0 = make_srd(wqkv, D_ * 3 * D_ * 2); c.s1 = make_srd(wg, D_ * F_ * 2); c.s2 = make_srd(wu, D_ * F_ * 2);
      c.so0 = pa * 128; c.so1 = 0;
    } else {
      c.vst0 = (8 * wr + r8) * 7168 + ch * 16;
      const unsigned r16 = l >> 2, chm = (l & 3) ^ (((r16 >> 3) & 1) << 1);
      c.vst1 = (16 * (wr >> 1) + r16) * 7168 + chm * 16;
      c.s0 = make_srd(wg, D_ * F_ * 2); c.s1 = make_srd(wu, D_ * F_ * 2); c.s2 = make_srd((wr & 1) ? wu : wg, D_ * F_ * 2);
      c.so0 = (c0 + (odd ? 32 : 0)) * 2; c.so1 = (c0 + (odd ? 0 : 128)) * 2;
    }
  } else {
    c.vst0 = l * 16; c.vst1 = 0;
    c.s0 = make_srd(ws + WS_XN, S_ * D_ * 2); c.s1 = c.s0; c.s2 = c.s0;
    c.so0 = (pm * 16 + 4 * wr) * 32768; c.so1 = 0;
    c.ldst = wr * 4096;
  }

  const int lb = blockIdx.x, lr = l & 7, lc = l >> 3;
  u32x4 xv0, xv1; u32x2 gen;
  {
    const u16* xp = x + (size_t)(lb * 8 + lr) * D_ + w * 128 + lc * 8;
#define LN_LOADS() asm volatile("global_load_dwordx4 %0, %3, off\n\tglobal_load_dwordx4 %1, %3, off offset:128\n\tglobal_load_dwordx2 %2, %4, off sc1" \
                 : "=&v"(xv0), "=&v"(xv1), "=&v"(gen) : "v"(xp), "v"(ws + WS_GEN) : "memory")
    if (grp == 0) {
      LN_LOADS();
      k1_stage_b<TYPE>(c, 0, 0); k1_stage_b<TYPE>(c, 1, 1); k1_stage_b<TYPE>(c, 2, 2);
      asm volatile("s_waitcnt vmcnt(15)" : "+v"(xv0), "+v"(xv1), "+v"(gen) :: "memory");
    } else {
      LN_LOADS();
      asm volatile("s_waitcnt vmcnt(0)" : "+v"(xv0), "+v"(xv1), "+v"(gen) :: "memory");
    }
#undef LN_LOADS
  }
  const u64 tag = (((u64)(unsigned)__builtin_amdgcn_readfirstlane(gen.y) << 32) | (unsigned)__builtin_amdgcn_readfirstlane(gen.x)) + 1;
  {
    float f[16]; float sm = 0.f, sq = 0.f;
#pragma unroll
    for (int j = 0; j < 4; ++j) { f[2 * j] = bf_lo(xv0[j]); f[2 * j + 1] = bf_hi(xv0[j]); f[8 + 2 * j] = bf_lo(xv1[j]); f[9 + 2 * j] = bf_hi(xv1[j]); }
    const float piv = __shfl(f[0], lr);
#pragma unroll
    for (int j = 0; j < 16; ++j) { const float d = f[j] - piv; sm += d; sq += d * d; }
    sm += __uint_as_float(__builtin_amdgcn_update_dpp(0u, __float_as_uint(sm), 0x128, 0xf, 0xf, false));
    sq += __uint_as_float(__builtin_amdgcn_update_dpp(0u, __float_as_uint(sq), 0x128, 0xf, 0xf, false));
    sm = xsum16_32(sm); sq = xsum16_32(sq);
    const float mw = sm * (1.0f / 128.0f);
    LAS f32x2* red = (LAS f32x2*)(lds + LDS_LNRED);
    if (l < 8) red[w * 8 + lr] = (f32x2){piv + mw, sq - sm * mw};
    WAIT_L(0); BAR();
    float mean = 0.f, m2 = 0.f; f32x2 r8[8];
#pragma unroll
    for (int i = 0; i < 8; ++i) { r8[i] = red[i * 8 + lr]; mean += r8[i].x; m2 += r8[i].y; }
    mean *= 0.125f;
#pragma unroll
    for (int i = 0; i < 8; ++i) { const float d = r8[i].x - mean; m2 += 128.0f * d * d; }
    const float mb = rbf(mean);
    const float inv = 1.0f / rbf(sqrtf(rbf(rbf(m2 * (1.0f / D_)) + 1e-5f)));
    const __amdgpu_buffer_rsrc_t xr = __builtin_amdgcn_make_buffer_rsrc(ws + WS_XN, 0, S_ * D_ * 2, 0x00020000);
    const unsigned xo = ((lb >> 1) * 32 + 4 * w + (lc >> 2)) * 1024 + ((lc & 3) * 16 + 8 * (lb & 1) + lr) * 16;
    u32x4 o0, o1;
#pragma unroll
    for (int j = 0; j < 4; ++j) {
      o0[j] = cvt_pk(rbf(f[2 * j] - mb) * inv, rbf(f[2 * j + 1] - mb) * inv);
      o1[j] = cvt_pk(rbf(f[8 + 2 * j] - mb) * inv, rbf(f[9 + 2 * j] - mb) * inv);
    }
    __builtin_amdgcn_raw_buffer_store_b128(o0, xr, xo, 0, 16);
    __builtin_amdgcn_raw_buffer_store_b128(o1, xr, xo + 2048, 0, 16);
    WAIT_V(0); BAR();
    if (w == 0) flag_set8(ws, WS_LNF + lb * 8, tag, l);
  }
  if (w == 4) flag_wait(ws, WS_LNF + (blockIdx.x & 7) * REP + pm * 256, 32, tag, l);
  BAR();

  f32x4 acc[4][10];
#pragma unroll
  for (int i = 0; i < 4; ++i)
#pragma unroll
    for (int j = 0; j < 10; ++j) acc[i][j] = (f32x4){0.f, 0.f, 0.f, 0.f};
  bf16x8 af[4], bfr[10];

  if (grp == 0) {
    WAIT_V(10); BAR();
#define K1_G0_STEP(J) do { \
      k1_read_b<TYPE, 0>(c, J, bfr); k1_piece_b<TYPE, 0>(c, s + (J) + 3, ((J) + 3) & 3); \
      k1_read_b<TYPE, 2>(c, J, bfr); k1_piece_b<TYPE, 1>(c, s + (J) + 3, ((J) + 3) & 3); \
      k1_read_b<TYPE, 4>(c, J, bfr); k1_piece_b<TYPE, 2>(c, s + (J) + 3, ((J) + 3) & 3); \
      k1_read_b<TYPE, 6>(c, J, bfr); k1_piece_b<TYPE, 3>(c, s + (J) + 3, ((J) + 3) & 3); \
      k1_read_b<TYPE, 8>(c, J, bfr); k1_read_a(c, J, af); k1_piece_b<TYPE, 4>(c, s + (J) + 3, ((J) + 3) & 3); \
      WAIT_L(0); BAR(); \
      __builtin_amdgcn_s_setprio(1); \
      k1_mma<TYPE, 0, 40 - K1_EARLY>(acc, af, bfr); \
      SCHED(); WAIT_V(10); \
      BAR(); \
      __builtin_amdgcn_s_setprio(3); k1_mma<TYPE, 40 - K1_EARLY, 40>(acc, af, bfr); __builtin_amdgcn_s_setprio(0); SCHED(); } while (0)
#pragma nounroll
    for (unsigned s = 0; s < 32; s += 4) {
      K1_G0_STEP(0); K1_G0_STEP(1); K1_G0_STEP(2); K1_G0_STEP(3);
    }
  } else {
    constexpr unsigned K1_PF_ROW0 = 96;
    const unsigned pf = w  , pfv = (blockIdx.x * 64 + l) * PF_STRIDE;
    const i32x4 sg = make_srd(wg, D_ * F_ * 2), su = make_srd(wu, D_ * F_ * 2), sq = make_srd(wqkv, D_ * 3 * D_ * 2), sd = make_srd(wd, F_ * D_ * 2), so = make_srd(wo, D_ * D_ * 2);
    constexpr unsigned MB = 16384u * PF_STRIDE;
    constexpr unsigned NG = ((1024 - K1_PF_ROW0) * 7168u + MB - 1) / MB, NQ = ((1024 - K1_PF_ROW0) * 6144u + MB - 1) / MB, ND = (F_ * D_ * 2 + MB - 1) / MB, NO = (D_ * D_ * 2 + MB - 1) / MB;
    static_assert(NG + ND <= 8 && NG <= 8 && NQ + NO <= 8, "prefetch schedule does not fit");
    k1_pf(pfv, sq, OOR, pf); k1_stage_a(c, 0, 0);
    WAIT_V(0); BAR();
    k1_pf(pfv, sq, OOR, pf); k1_stage_a(c, 1, 1);
    k1_stage_a(c, 2, 2);
    BAR();
#define K1_G1_STEP(J, PFN) do { \
      k1_read_b<TYPE, 0>(c, J, bfr); \
      if ((J) == 0) k1_pf(pfv, it < NG ? sg : sd, it < NG ? K1_PF_ROW0 * 7168u + it * MB : (it - NG) * MB, pf); \
      if ((J) == 1) k1_pf(pfv, su, it < NG ? K1_PF_ROW0 * 7168u + it * MB : OOR, pf); \
      if ((J) == 2) k1_pf(pfv, it < NQ ? sq : so, it < NQ ? K1_PF_ROW0 * 6144u + it * MB : (it - NQ) * MB, pf); \
      k1_read_b<TYPE, 2>(c, J, bfr); k1_piece_a<0>(c, s + (J) + 3, ((J) + 3) & 3); \
      k1_read_b<TYPE, 4>(c, J, bfr); k1_piece_a<1>(c, s + (J) + 3, ((J) + 3) & 3); \
      k1_read_b<TYPE, 6>(c, J, bfr); k1_piece_a<2>(c, s + (J) + 3, ((J) + 3) & 3); \
      k1_read_b<TYPE, 8>(c, J, bfr); k1_read_a(c, J, af); k1_piece_a<3>(c, s + (J) + 3, ((J) + 3) & 3); \
      WAIT_L(0); WAIT_A(PFN, pf); \
      BAR(); \
      __builtin_amdgcn_s_setprio(1); \
      k1_mma<TYPE, 0, 40 - K1_EARLY>(acc, af, bfr); \
      if ((J) < 3) BAR(); else BAR_IF(s, 28); \
      __builtin_amdgcn_s_setprio(3); k1_mma<TYPE, 40 - K1_EARLY, 40>(acc, af, bfr); __builtin_amdgcn_s_setprio(0); SCHED(); } while (0)
#pragma nounroll
    for (unsigned s = 0; s < 32; s += 4) {
      const unsigned it = s >> 2;
      K1_G1_STEP(0, 9); K1_G1_STEP(1, 10); K1_G1_STEP(2, 10); K1_G1_STEP(3, 9);
    }
  }
  WAIT_V(0);

  const __amdgpu_buffer_rsrc_t a2 = __builtin_amdgcn_make_buffer_rsrc(ws + WS_A2, 0, 128 * KS2 * 1024, 0x00020000);
  const unsigned mf0 = pm * 16 + 4 * wr;
  if (TYPE == 1) {
    const unsigned ksf = c0 / 32 + (odd ? 1 : 0) + 2 * grp, ksm = c0 / 32 + (odd ? 0 : 4);
#pragma unroll
    for (int i = 0; i < 4; ++i) {
      const unsigned base = (mf0 + i) * (KS2 * 1024);
#pragma unroll
      for (int t = 0; t < 2; ++t) {
        f32x4 h0, h1;
#pragma unroll
        for (int r = 0; r < 4; ++r) { h0[r] = silu_f(acc[i][2 * t][r]) * acc[i][2 * t + 4][r]; h1[r] = silu_f(acc[i][2 * t + 1][r]) * acc[i][2 * t + 5][r]; }
        __builtin_amdgcn_raw_buffer_store_b128(__builtin_bit_cast(u32x4, pack8(h0, h1)), a2, l * 16 + base + (ksf + t) * 1024, 0, SC1);
      }
      f32x4 h0;
#pragma unroll
      for (int r = 0; r < 4; ++r) h0[r] = silu_f(acc[i][8][r]) * acc[i][9][r];
      u32x2 o; o.x = cvt_pk(h0[0], h0[1]); o.y = cvt_pk(h0[2], h0[3]);
      *(LAS u32x2*)(lds + LDS_HALO + (wr * 4 + i) * 1024 + l * 16 + grp * 8) = o;
    }
    WAIT_L(0); BAR();
#pragma unroll
    for (int ii = 0; ii < 2; ++ii) {
      const int i = 2 * grp + ii;
      const u32x4 v = *(LAS u32x4*)(lds + LDS_HALO + (wr * 4 + i) * 1024 + l * 16);
      __builtin_amdgcn_raw_buffer_store_b128(v, a2, l * 16 + (mf0 + i) * (KS2 * 1024) + ksm * 1024, 0, SC1);
    }
    if (grp == 1) k2_prologue_b(wr, l, wd, isp, ct);
  } else {
    const int head = 2 * pa + grp;
#pragma unroll
    for (int i = 0; i < 4; ++i) {
      f32x4 h0, h1;
#pragma unroll
      for (int r = 0; r < 4; ++r) { h0[r] = silu_f(acc[i][6][r]) * acc[i][8][r]; h1[r] = silu_f(acc[i][7][r]) * acc[i][9][r]; }
      __builtin_amdgcn_raw_buffer_store_b128(__builtin_bit_cast(u32x4, pack8(h0, h1)), a2, l * 16 + (mf0 + i) * (KS2 * 1024) + (2 * pa + grp) * 1024, 0, SC1);
    }
    bf16x8 kop[5]; u32x2 vp[5][2];
#pragma unroll
    for (int i = 0; i < 4; ++i) {
      kop[i + 1] = pack8(acc[i][2], acc[i][3]);
#pragma unroll
      for (int d = 0; d < 2; ++d) { vp[i + 1][d].x = cvt_pk(acc[i][4 + d][0], acc[i][4 + d][1]); vp[i + 1][d].y = cvt_pk(acc[i][4 + d][2], acc[i][4 + d][3]); }
    }
    const u32x4 klast = __builtin_bit_cast(u32x4, kop[4]);
    const u32x4 vlast = (u32x4){vp[4][0].x, vp[4][0].y, vp[4][1].x, vp[4][1].y};
    const __amdgpu_buffer_rsrc_t hr = __builtin_amdgcn_make_buffer_rsrc(ws + WS_HALO, 0, 8 * 32 * 2048, 0x00020000);
    const bool send = (wr == 3) && (pm < 7);
    if (send) {
      const unsigned ho = (pm * 32 + head) * 2048;
      __builtin_amdgcn_raw_buffer_store_b128(klast, hr, l * 16 + ho, 0, 16);
      __builtin_amdgcn_raw_buffer_store_b128(vlast, hr, l * 16 + ho + 1024, 0, 16);
    }
    if (wr != 3) {
      *(LAS u32x4*)(lds + LDS_HALO + w * 2048 + l * 16) = klast;
      *(LAS u32x4*)(lds + LDS_HALO + w * 2048 + 1024 + l * 16) = vlast;
    }
    bf16x8 qop[4];
#pragma unroll
    for (int i = 0; i < 4; ++i) qop[i] = pack8(acc[i][0], acc[i][1]);
    const int m = l & 15; const bool par = m & 1;
    const bool own0 = 4 * g + (m & 1) <= m, own1 = 4 * g + (m & 1) + 2 <= m;
    const unsigned hmask = par ? 0xffff0000u : 0x0000ffffu;
    const float scale = 0.17677669529663687f * 1.44269504089f;
    u32x4 ob[4];
    auto store_o = [&](int i) { __builtin_amdgcn_raw_buffer_store_b128(ob[i], a2, l * 16 + (mf0 + i) * (KS2 * 1024) + (112 + head) * 1024, 0, SC1); };
    auto attend = [&](auto n_, int I0, bool have_prev) {
      constexpr int N = decltype(n_)::value;
      const f32x4 z = (f32x4){0.f, 0.f, 0.f, 0.f};
      f32x4 s0[N], s1[N];
#pragma unroll
      for (int k = 0; k < N; ++k) { s0[k] = MFMA(kop[I0 + k + 1], qop[I0 + k], z); s1[k] = MFMA(kop[I0 + k], qop[I0 + k], z); }
      float l0[N], l1[N], mx[N], e0[N], e1[N], inv[N];
#pragma unroll
      for (int k = 0; k < N; ++k) {
        l0[k] = (own0 ? (par ? s0[k][1] : s0[k][0]) : (par ? s1[k][1] : s1[k][0])) * scale;
        l1[k] = (own1 ? (par ? s0[k][3] : s0[k][2]) : (par ? s1[k][3] : s1[k][2])) * scale;
        if (!have_prev) { l0[k] = own0 ? l0[k] : -3.0e38f; l1[k] = own1 ? l1[k] : -3.0e38f; }
        mx[k] = fmaxf(l0[k], l1[k]);
      }
#pragma unroll
      for (int k = 0; k < N; ++k) mx[k] = xmax16_32(mx[k]);
#pragma unroll
      for (int k = 0; k < N; ++k) { e0[k] = __builtin_amdgcn_exp2f(l0[k] - mx[k]); e1[k] = __builtin_amdgcn_exp2f(l1[k] - mx[k]); inv[k] = e0[k] + e1[k]; }
#pragma unroll
      for (int k = 0; k < N; ++k) inv[k] = __builtin_amdgcn_rcpf(xsum16_32(inv[k]));
      f32x4 o[N][2];
#pragma unroll
      for (int k = 0; k < N; ++k) {
        const unsigned w0 = cvt_pk(e0[k] * inv[k], e0[k] * inv[k]) & hmask, w1 = cvt_pk(e1[k] * inv[k], e1[k] * inv[k]) & hmask;
        const u32x4 pw = (u32x4){own0 ? 0u : w0, own1 ? 0u : w1, own0 ? w0 : 0u, own1 ? w1 : 0u};
#pragma unroll
        for (int d = 0; d < 2; ++d) {
          const u32x4 vv = (u32x4){vp[I0 + k][d].x, vp[I0 + k][d].y, vp[I0 + k + 1][d].x, vp[I0 + k + 1][d].y};
          o[k][d] = MFMA(__builtin_bit_cast(bf16x8, vv), __builtin_bit_cast(bf16x8, pw), z);
        }
      }
#pragma unroll
      for (int k = 0; k < N; ++k) ob[I0 + k] = __builtin_bit_cast(u32x4, pack8(o[k][0], o[k][1]));
    };
    attend(std::integral_constant<int, 3>{}, 1, true);
    WAIT_V(0);
    if (send && l == 0) flag_set(ws, WS_HLF + (pm * 32 + head) * 64, tag);
    WAIT_L(0); BAR();
    if (w == 0) flag_set8(ws, WS_T1A + (pm * 16 + pa) * 8, tag, l);
    store_o(1); store_o(2); store_o(3);
    if (grp == 1) k2_prologue_b(wr, l, wd, isp, ct);
    bool have_prev = true;
    u32x4 kh, vh;
    if (wr == 0) {
      if (pm > 0) {
        flag_wait(ws, WS_HLF + ((pm - 1) * 32 + head) * 64, 1, tag, l);
        const unsigned ho = ((pm - 1) * 32 + head) * 2048;
        kh = __builtin_amdgcn_raw_buffer_load_b128(hr, l * 16 + ho, 0, 16);
        vh = __builtin_amdgcn_raw_buffer_load_b128(hr, l * 16 + ho + 1024, 0, 16);
      } else { kh = (u32x4){0u, 0u, 0u, 0u}; vh = kh; have_prev = false; }
    } else {
      kh = *(LAS u32x4*)(lds + LDS_HALO + (w - 1) * 2048 + l * 16);
      vh = *(LAS u32x4*)(lds + LDS_HALO + (w - 1) * 2048 + 1024 + l * 16);
    }
    kop[0] = __builtin_bit_cast(bf16x8, kh);
    vp[0][0] = (u32x2){vh.x, vh.y}; vp[0][1] = (u32x2){vh.z, vh.w};
    attend(std::integral_constant<int, 1>{}, 0, have_prev);
    store_o(0);
  }
  WAIT_V(0); BAR();
  if (w == 0) flag_set8(ws, WS_T1F + (pm * 32 + TYPE * 16 + pa) * 8, tag, l);
  return tag;
}

struct K2Frag { bf16x8 a[4], b[4]; };
struct K2Rd { LAS unsigned char* lds; unsigned aoff[2], vb[2][4]; };
template <int SLOT> __device__ __forceinline__ void k2_read(const K2Rd& c, K2Frag& f) {
  constexpr int H = SLOT >> 2, O = (SLOT & 3) * K2_SLOT;
#pragma unroll
  for (int j = 0; j < 4; ++j) { LAS unsigned char* p = c.lds + c.vb[H][j] + O; f.b[j] = lds_tr8(p, p + 4096); }
#pragma unroll
  for (int i = 0; i < 4; ++i) f.a[i] = *(LAS bf16x8*)(c.lds + c.aoff[H] + O + i * 1024);
}
template <int SLOTN> __device__ __forceinline__ void k2_step(const K2Rd& c, f32x4 (&acc)[4][4], const K2Frag& cur, K2Frag& nxt) {
  k2_read<SLOTN>(c, nxt);
#pragma unroll
  for (int j = 0; j < 4; ++j)
#pragma unroll
    for (int ii = 0; ii < 4; ++ii) { const int i = (j & 1) ? 3 - ii : ii; acc[i][j] = MFMA(cur.b[j], cur.a[i], acc[i][j]); }
#pragma unroll
  for (int k = 0; k < 6; ++k) { __builtin_amdgcn_sched_group_barrier(0x8, 1, 0); __builtin_amdgcn_sched_group_barrier(0x100, 2, 0); }
  __builtin_amdgcn_sched_group_barrier(0x8, 10, 0);
  SCHED(); __builtin_amdgcn_s_waitcnt(0xC07F); asm volatile("" ::: "memory"); BAR();
}
__device__ __forceinline__ void k2_consume_half(const K2Rd& c, f32x4 (&acc)[4][4], K2Frag& f0, K2Frag& f1) {
  k2_step<1>(c, acc, f0, f1); k2_step<2>(c, acc, f1, f0); k2_step<3>(c, acc, f0, f1); k2_step<4>(c, acc, f1, f0);
}
__device__ __forceinline__ void k2_consume(const K2Rd& c, f32x4 (&acc)[4][4], K2Frag& f0, K2Frag& f1, int iters) {
#pragma nounroll
  for (int it = 0; it < iters; ++it) {
    k2_step<1>(c, acc, f0, f1); k2_step<2>(c, acc, f1, f0); k2_step<3>(c, acc, f0, f1); k2_step<4>(c, acc, f1, f0);
    k2_step<5>(c, acc, f0, f1); k2_step<6>(c, acc, f1, f0); k2_step<7>(c, acc, f0, f1); k2_step<0>(c, acc, f1, f0);
  }
}

struct K2Ld { unsigned vsta, vstb; i32x4 sa; unsigned ldst; };
__device__ __forceinline__ void k2_stage(const K2Ld& c, i32x4 sb, unsigned ka, unsigned kb, int slot) {
  const unsigned d = slot * K2_SLOT + c.ldst;
  unsigned t0;
  asm volatile(
    "s_mov_b32 m0, %7\n\ts_add_u32 %0, %5, 0x24000\n\tbuffer_load_dwordx4 %1, %3, %5 offen sc1 lds\n\t"
    "s_add_u32 m0, %7, 0x400\n\ts_nop 0\n\tbuffer_load_dwordx4 %1, %3, %0 offen sc1 lds\n\t"
    "s_add_u32 m0, %7, 0x2000\n\ts_add_u32 %0, %6, 0x80\n\tbuffer_load_dwordx4 %2, %4, %6 offen lds\n\t"
    "s_add_u32 m0, %7, 0x2400\n\ts_nop 0\n\tbuffer_load_dwordx4 %2, %4, %0 offen lds"
    : "=&s"(t0) : "v"(c.vsta), "v"(c.vstb), "s"(c.sa), "s"(sb), "s"(ka), "s"(kb), "s"(d) : "memory", "scc");
}
__device__ __forceinline__ void k2_load(const K2Ld& c, i32x4 sb, unsigned ka, unsigned kb, int iters) {
#pragma nounroll
  for (int it = 0; it < iters; ++it) {
#pragma unroll
    for (int j = 0; j < 8; ++j) {
      k2_stage(c, sb, ka, kb, j);
      ka += 1024u; kb += 65536u;
      WAIT_V(24); BAR();
    }
  }
}
__device__ __forceinline__ void k2_load_half(const K2Ld& c, i32x4 sb, unsigned ka, unsigned kb) {
#pragma unroll
  for (int j = 0; j < 4; ++j) { k2_stage(c, sb, ka + j * 1024u, kb + j * 65536u, j); WAIT_V(24); BAR(); }
}
template <int OFF = 0> __device__ __forceinline__ bool k2_load_poll(const K2Ld& c, i32x4 sb, unsigned ka, unsigned kb, const unsigned char* fl, int n, u64 tag, int l) {
  u32x2 pv;
  const unsigned char* fp = fl + (l < n ? l : 0) * 8;
#pragma unroll
  for (int j = 0; j < 8; ++j) {
    if (j == 1) asm volatile("global_load_dwordx2 %0, %1, off sc1" : "=&v"(pv) : "v"(fp) : "memory");
    k2_stage(c, sb, ka, kb, (j + OFF) & 7);
    ka += 1024u; kb += 65536u;
    WAIT_V(24);
    if (j < 7) BAR();
  }
  asm volatile("" : "+v"(pv));
  return __all((((u64)pv.y << 32) | pv.x) == tag);
}
template <int AUX> __device__ __forceinline__ void glds16x(unsigned voff, i32x4 srd, unsigned soff, unsigned ldsaddr) {
  if (AUX == 16) asm volatile("s_mov_b32 m0, %3\n\ts_nop 0\n\tbuffer_load_dwordx4 %0, %1, %2 offen sc1 lds" ::"v"(voff), "s"(srd), "s"(soff), "s"(ldsaddr) : "memory");
  else asm volatile("s_mov_b32 m0, %3\n\ts_nop 0\n\tbuffer_load_dwordx4 %0, %1, %2 offen lds" ::"v"(voff), "s"(srd), "s"(soff), "s"(ldsaddr) : "memory");
}

template <bool ISP> __device__ __forceinline__ void k2_body(LAS unsigned char* lds, const u16* x, const u16* wo, const u16* wd, u16* out, unsigned char* ws, int rt, int ct, u64 tag) {
  constexpr int NT = ISP ? K2_TP : K2_TQ;
  int tid = threadIdx.x; asm volatile("" : "+v"(tid));
  const int w = __builtin_amdgcn_readfirstlane(tid >> 6), l = tid & 63;
  const int w4 = w & 3, wm = w4 >> 1, wn = w4 & 1;
  const int g = l >> 4, q = (l & 15) >> 2, p = l & 3;
  const int tile = rt * 8 + ct;
  if (w >= 4) {
    K2Ld c;
    c.vsta = l * 16; c.vstb = k2_vstb(w4, l);
    c.sa = make_srd(ws + WS_A2, 128 * KS2 * 1024);
    const i32x4 swd = make_srd(wd, F_ * D_ * 2), swo = make_srd(wo, D_ * D_ * 2);
    const unsigned soa = (rt * 8 + 2 * w4) * (KS2 * 1024), sob = ct * 256;
    c.ldst = w4 * 2048;
    const size_t rep = (blockIdx.x & 7) * REP, t1f = WS_T1F + rep + (rt >> 1) * 256;
    PollKeep pk; pk.a = pk.b = pk.c = (u32x2){0u, 0u};
    if (w == 4) { if (ISP) flag_wait3(ws, WS_T1A + rep + (rt >> 1) * 128, 16, tag, l, pk); else flag_wait3(ws, t1f + 128, 16, tag, l, pk); }
    BAR();
    const unsigned ks0 = ISP ? 0u : K2_H0, ka0 = ks0 * 1024u + soa;
#pragma unroll
    for (int t = 0; t < 4; ++t) { glds16x<16>(c.vsta, c.sa, ka0 + t * 1024u, t * K2_SLOT + c.ldst); glds16x<16>(c.vsta, c.sa, ka0 + t * 1024u + 0x24000u, t * K2_SLOT + c.ldst + 1024); }
    WAIT_V(4); POLL_KEEP(pk); BAR();
#pragma unroll
    for (int t = 4; t < 8; ++t) { glds16x<16>(c.vsta, c.sa, ka0 + t * 1024u, t * K2_SLOT + c.ldst); glds16x<16>(c.vsta, c.sa, ka0 + t * 1024u + 0x24000u, t * K2_SLOT + c.ldst + 1024); }
    BAR();
    {
      const unsigned ka = ka0 + 8 * 1024u, kb = (ks0 + 8) * 65536u + sob;
#define K2_FIRST(J, N) do { k2_stage(c, swd, ka + (J) * 1024u, kb + (J) * 65536u, J); WAIT_V(N); BAR(); } while (0)
      K2_FIRST(0, 14); K2_FIRST(1, 16); K2_FIRST(2, 18); K2_FIRST(3, 20); K2_FIRST(4, 22); K2_FIRST(5, 24); K2_FIRST(6, 24); K2_FIRST(7, 24);
    }
    const i32x4 sx = make_srd(x, S_ * D_ * 2), sp = make_srd(ws + WS_PART, 128 * 32768);
    if (ISP) {
      constexpr int SB = NT & 7;
      constexpr int NPB = ((NT - 32) * 32 - 1024 + 159) / 160;
      k2_load(c, swd, 16 * 1024u + soa, 16 * 65536u + sob, 1);
      if (!k2_load_poll(c, swd, 24 * 1024u + soa, 24 * 65536u + sob, ws + t1f, 16, tag, l)) flag_wait(ws, t1f, 16, tag, l);
      BAR();
      k2_load(c, swo, 112 * 1024u + soa, sob, 3);
      if (!k2_load_poll(c, swo, (112 + 24) * 1024u + soa, 24 * 65536u + sob, ws + t1f + 128, NPB, tag, l)) flag_wait(ws, t1f + 128, NPB, tag, l);
      BAR();
      if ((NT - 72) / 8 > 0) k2_load(c, swd, 32 * 1024u + soa, 32 * 65536u + sob, (NT - 72) / 8);
      if (SB == 4) k2_load_half(c, swd, (NT - 44) * 1024u + soa, (NT - 44) * 65536u + sob);
      const bool early = k2_load_poll<SB>(c, swd, (NT - 40) * 1024u + soa, (NT - 40) * 65536u + sob, ws + WS_PTF + tile * 64, 4, tag, l);
      BAR();
      const unsigned xr8 = l >> 3, xv = ((rt * 128 + wm * 64 + xr8) * D_ + ct * 128 + wn * 64) * 2;
#pragma unroll
      for (int k = 0; k < 2; ++k) {
#pragma unroll
        for (int e = 0; e < 4; ++e) {
          const int blk = k * 4 + e;
          glds16x<0>(xv + (((l & 7) ^ (4 * (blk & 1) + (xr8 >> 1))) << 4), sx, blk * 8 * D_ * 2, (SB + k) * 16384 + w4 * 4096 + e * 1024);
        }
        WAIT_V(24); BAR();
      }
      if (early) {
#pragma unroll
        for (int k = 2; k < 4; ++k) {
#pragma unroll
          for (int e = 0; e < 4; ++e) glds16x<16>(l * 16, sp, tile * 32768 + w4 * 8192 + ((k - 2) * 4 + e) * 1024, (SB + k) * 16384 + w4 * 4096 + e * 1024);
          WAIT_V(24); BAR();
        }
        WAIT_V(20); BAR(); WAIT_V(16); BAR(); BAR(); WAIT_V(0); BAR();
      } else {
        WAIT_V(20); BAR(); WAIT_V(16); BAR(); WAIT_V(12); BAR();
        WAIT_V(0);
        flag_wait(ws, WS_PTF + tile * 64, 4, tag, l);
#pragma unroll
        for (int e = 0; e < 8; ++e) glds16x<16>(l * 16, sp, tile * 32768 + w4 * 8192 + e * 1024, (SB + 2 + (e >> 2)) * 16384 + w4 * 4096 + (e & 3) * 1024);
        BAR(); BAR();
        WAIT_V(0); BAR();
      }
    } else {
      k2_load(c, swd, (K2_H0 + 16) * 1024u + soa, (K2_H0 + 16) * 65536u + sob, (NT - 16) / 8);
      if ((NT & 7) == 4) k2_load_half(c, swd, (K2_H0 + NT - 4) * 1024u + soa, (K2_H0 + NT - 4) * 65536u + sob);
      WAIT_V(20); BAR(); WAIT_V(16); BAR(); WAIT_V(12); BAR(); WAIT_V(8); BAR(); WAIT_V(4); BAR(); WAIT_V(0); BAR(); BAR(); BAR();
    }
    if (!ISP && blockIdx.x == 4 && w == 5) {
      gu64* p = (gu64*)(ws + WS_T1F) + l;
      for (unsigned spins = 0; spins < (1u << 18); ++spins) {
        const u64 a = __hip_atomic_load(p, __ATOMIC_RELAXED, __HIP_MEMORY_SCOPE_AGENT), b = __hip_atomic_load(p + 64, __ATOMIC_RELAXED, __HIP_MEMORY_SCOPE_AGENT);
        const u64 c2 = __hip_atomic_load(p + 128, __ATOMIC_RELAXED, __HIP_MEMORY_SCOPE_AGENT), d = __hip_atomic_load(p + 192, __ATOMIC_RELAXED, __HIP_MEMORY_SCOPE_AGENT);
        if (__all(a == tag && b == tag && c2 == tag && d == tag)) break;
        __builtin_amdgcn_s_sleep(2);
      }
      if (l == 0) flag_set(ws, WS_GEN, tag);
    }
    return;
  }
  K2Rd c; c.lds = lds;
  {
    const unsigned row = 4 * (g & 1) + q, s2 = (row >> 1) & 3;
    const unsigned lanep = 8192 + (g >> 1) * 2048 + wn * 1024 + row * 128 + (p >> 1) * 16 + (p & 1) * 8;
#pragma unroll
    for (int h = 0; h < 2; ++h) {
      c.aoff[h] = l * 16 + wm * 4096 + h * 65536;
#pragma unroll
      for (int j = 0; j < 4; ++j) c.vb[h][j] = lanep + ((j ^ s2) << 5) + h * 65536;
    }
  }
  f32x4 accf[4][4], accs[4][4];
#pragma unroll
  for (int i = 0; i < 4; ++i)
#pragma unroll
    for (int j = 0; j < 4; ++j) { accf[i][j] = (f32x4){0.f, 0.f, 0.f, 0.f}; accs[i][j] = accf[i][j]; }
  K2Frag f0, f1;
  BAR();
  BAR();
  k2_read<0>(c, f0);
  WAIT_L(0); BAR();
  if (ISP) { k2_consume(c, accf, f0, f1, 4); k2_consume(c, accs, f0, f1, 4); k2_consume(c, accf, f0, f1, (NT - 64) / 8); }
  else k2_consume(c, accf, f0, f1, NT / 8);
  if ((NT & 7) == 4) k2_consume_half(c, accf, f0, f1);
  if (!ISP) {
    const __amdgpu_buffer_rsrc_t pr = __builtin_amdgcn_make_buffer_rsrc(ws + WS_PART, 0, 128 * 32768, 0x00020000);
    const unsigned po = tile * 32768 + w4 * 8192 + l * 16;
#pragma unroll
    for (int i = 0; i < 4; ++i)
#pragma unroll
      for (int jp = 0; jp < 2; ++jp) __builtin_amdgcn_raw_buffer_store_b128(__builtin_bit_cast(u32x4, pack8(accf[i][2 * jp], accf[i][2 * jp + 1])), pr, po + (i * 2 + jp) * 1024, 0, 16);
    WAIT_V(0);
    if (l == 0) flag_set(ws, WS_PTF + tile * 64 + w4 * 8, tag);
  } else {
    const int m = l & 15;
    const __amdgpu_buffer_rsrc_t orr = __builtin_amdgcn_make_buffer_rsrc(out, 0, S_ * D_ * 2, 0x00020000);
    const unsigned ov = ((rt * 128 + wm * 64 + m) * D_ + ct * 128 + wn * 64 + (((g & 1) << 1) | (g >> 1)) * 8) * 2;
    u32x4 pt[8]; u32x2 xr[4][4];
#pragma unroll
    for (int b = 0; b < 8; ++b) pt[b] = *(LAS u32x4*)(lds + ((NT & 7) + 2 + (b >> 2)) * 16384 + w4 * 4096 + (b & 3) * 1024 + l * 16);
#pragma unroll
    for (int i = 0; i < 4; ++i)
#pragma unroll
      for (int j = 0; j < 4; ++j) {
        const int blk = 2 * i + (m >> 3);
        xr[i][j] = *(LAS u32x2*)(lds + ((NT & 7) + (i >> 1)) * 16384 + w4 * 4096 + ((blk & 3) * 1024) + (m & 7) * 128 + (((2 * j + (g >> 1)) ^ (m >> 1)) << 4) + (g & 1) * 8);
      }
    f32x4 e[4][4];
#pragma unroll
    for (int i = 0; i < 4; ++i)
#pragma unroll
      for (int j = 0; j < 4; ++j) {
        const u32x4 pp = pt[i * 2 + (j >> 1)]; const int h = (j & 1) * 2;
        accf[i][j] += (f32x4){bf_lo(pp[h]), bf_hi(pp[h]), bf_lo(pp[h + 1]), bf_hi(pp[h + 1])};
        const f32x4 t = accf[i][j] * -1.44269504089f;
#pragma unroll
        for (int r = 0; r < 4; ++r) e[i][j][r] = __builtin_amdgcn_exp2f(t[r]);
      }
    SCHED();
#pragma unroll
    for (int i = 0; i < 4; ++i)
#pragma unroll
      for (int j = 0; j < 4; ++j) {
        const f32x4 d = e[i][j] + 1.0f;
#pragma unroll
        for (int r = 0; r < 4; ++r) e[i][j][r] = __builtin_amdgcn_rcpf(d[r]);
        accf[i][j] *= accs[i][j];
      }
    SCHED();
#pragma unroll
    for (int i = 0; i < 4; ++i)
#pragma unroll
      for (int jp = 0; jp < 2; ++jp) {
        unsigned y[2][2];
#pragma unroll
        for (int jj = 0; jj < 2; ++jj) {
          const int j = 2 * jp + jj;
          const f32x4 xv4 = (f32x4){bf_lo(xr[i][j].x), bf_hi(xr[i][j].x), bf_lo(xr[i][j].y), bf_hi(xr[i][j].y)};
          const f32x4 v = xv4 + accf[i][j] * e[i][j];
          y[jj][0] = cvt_pk(v[0], v[1]); y[jj][1] = cvt_pk(v[2], v[3]);
        }
        const auto s0 = __builtin_amdgcn_permlane16_swap(y[0][0], y[1][0], false, false);
        const auto s1 = __builtin_amdgcn_permlane16_swap(y[0][1], y[1][1], false, false);
        __builtin_amdgcn_raw_buffer_store_b128((u32x4){s0[0], s1[0], s0[1], s1[1]}, orr, ov + i * 16 * D_ * 2 + jp * 64, 0, SC1);
      }
  }
}

__global__ __launch_bounds__(512, 2) void fused_block(const u16* __restrict__ x, const u16* __restrict__ wqkv, const u16* __restrict__ wo, const u16* __restrict__ wg,
                                                      const u16* __restrict__ wu, const u16* __restrict__ wd, u16* __restrict__ out, unsigned char* __restrict__ ws) {
  extern __shared__ __attribute__((aligned(16))) unsigned char shm[];
  LAS unsigned char* lds = (LAS unsigned char*)shm;
  const int b = blockIdx.x, xcd = b & 7, idx = b >> 3;
  const int pm = idx & 7, pn = idx >> 3, pa = xcd * 2 + (pn >> 1);
  const int pr = xcd & 3, rt = (pr & 1) * 8 + (idx & 7), ct = (pr >> 1) * 4 + (idx >> 3);
  const bool isp = xcd < 4;
  u64 tag;
  if (pn & 1) tag = k1_body<1>(lds, x, wqkv, wg, wu, wo, wd, ws, pm, pa, isp, ct);
  else tag = k1_body<0>(lds, x, wqkv, wg, wu, wo, wd, ws, pm, pa, isp, ct);
  if (isp) k2_body<true>(lds, x, wo, wd, out, ws, rt, ct, tag);
  else k2_body<false>(lds, x, wo, wd, out, ws, rt, ct, tag);
}

extern "C" void kernel_launch(void* const* d_in, const int* in_sizes, int n_in, void* d_out, int out_size, void* d_ws, size_t ws_size, hipStream_t stream) {
  const u16* x = (const u16*)d_in[0]; const u16* wqkv = (const u16*)d_in[1]; const u16* wo = (const u16*)d_in[2];
  const u16* wg = (const u16*)d_in[4]; const u16* wu = (const u16*)d_in[5]; const u16* wd = (const u16*)d_in[6];
  (void)hipFuncSetAttribute((const void*)fused_block, hipFuncAttributeMaxDynamicSharedMemorySize, LDS_TOTAL);
  fused_block<<<256, 512, LDS_TOTAL, stream>>>(x, wqkv, wo, wg, wu, wd, (u16*)d_out, (unsigned char*)d_ws);
}
```
